```python
import torch

def _softmax(x, axis=-1):
    return torch.softmax(x, dim=axis)

def _gelu(x):
    return torch.nn.functional.gelu(x, approximate='tanh')

def _silu(x):
    return torch.nn.functional.silu(x)

def _sigmoid(x):
    return torch.sigmoid(x)

def _layernorm(x, axis=-1, eps=1e-05):
    m = x.mean(dim=axis, keepdim=True)
    v = x.var(dim=axis, keepdim=True, unbiased=False)
    return (x - m) / torch.sqrt(v + eps)

def _scatter_add(tgt, idx, src, axis=0):
    return tgt.clone().index_add_(axis, idx, src)

def _segment_sum(src, seg, n):
    out = torch.zeros((n,) + src.shape[1:], dtype=src.dtype)
    return out.index_add_(0, seg, src)

def _pad(x, pw):
    flat = tuple((v for pair in reversed(pw) for v in pair))
    return torch.nn.functional.pad(x, flat)
kernel_name = 'adaLN_cond__gqa_block_cached__geglu_ffn__decode'

def setup_inputs():

    _w = lambda a: (lambda t: t.to(torch.bfloat16) if t.is_floating_point() else t)(torch.as_tensor(a))

    def _A_setup():
        B, Sq, D = (1, 1, 512)
        Hq, Hkv, Sk, Dh = (32, 8, 16384, 128)
        assert Hq % Hkv == 0, (Hq, Hkv)
        return {'x': _w((lambda *s: torch.randn(*s, dtype=torch.bfloat16) * 0.1)(B, Sq, D)), 'k_cache': _w((lambda *s: torch.randn(*s, dtype=torch.bfloat16) * 0.8)(B, Hkv, Sk, Dh)), 'v_cache': _w((lambda *s: torch.randn(*s, dtype=torch.bfloat16) * 0.8)(B, Hkv, Sk, Dh)), 'wq': _w((lambda *s: torch.randn(*s, dtype=torch.bfloat16) * 0.8)(D, Hq * Dh) * D ** (-0.5)), 'wo': _w((lambda *s: torch.randn(*s, dtype=torch.bfloat16) * 0.8)(Hq * Dh, D) * (Hq * Dh) ** (-0.5))}
    _A_in = _A_setup()

    def _B_setup():
        B, S, D, F = (1, 1, 512, 1792)
        return {'x': _w((lambda *s: torch.randn(*s, dtype=torch.bfloat16) * 0.1)(B, S, D)), 'w_gate': _w((lambda *s: torch.randn(*s, dtype=torch.bfloat16) * 0.1)(D, F)), 'w_up': _w((lambda *s: torch.randn(*s, dtype=torch.bfloat16) * 0.1)(D, F)), 'w_down': _w((lambda *s: torch.randn(*s, dtype=torch.bfloat16) * 0.1)(F, D))}
    _B_in = _B_setup()
    return {'A_x': _A_in['x'], 'A_k_cache': _A_in['k_cache'], 'A_v_cache': _A_in['v_cache'], 'A_wq': _A_in['wq'], 'A_wo': _A_in['wo'], 'B_x': _B_in['x'], 'B_w_gate': _B_in['w_gate'], 'B_w_up': _B_in['w_up'], 'B_w_down': _B_in['w_down'], 'c': _w((lambda *s: torch.randn(*s, dtype=torch.bfloat16) * 0.4)(1, 512) * 8.0), 'w_mod': _w((lambda *s: torch.randn(*s, dtype=torch.bfloat16) * 0.4)(512, 6 * 512) * 512 ** (-0.5) * 8.0)}

def reference(A_x, A_k_cache, A_v_cache, A_wq, A_wo, B_x, B_w_gate, B_w_up, B_w_down, c, w_mod):

    def _A_ref(x, k_cache, v_cache, wq, wo):
        B, Sq, D = x.shape
        Hq, Dh = (32, 128)
        Hkv = k_cache.shape[1]
        rep = Hq // Hkv
        q = (lambda x, *d: x.permute(*d))((x @ wq).reshape(B, Sq, Hq, Dh), 0, 2, 1, 3)
        k = (lambda x, n, axis: torch.repeat_interleave(x, n, dim=axis))(k_cache, rep, axis=1)
        v = (lambda x, n, axis: torch.repeat_interleave(x, n, dim=axis))(v_cache, rep, axis=1)
        scores = torch.einsum('bhqd,bhkd->bhqk', q, k) * Dh ** (-0.5)
        p = _softmax(scores, axis=-1)
        o = torch.einsum('bhqk,bhkd->bhqd', p, v)
        return (lambda x, *d: x.permute(*d))(o, 0, 2, 1, 3).reshape(B, Sq, Hq * Dh) @ wo

    def _B_ref(x, w_gate, w_up, w_down):
        return _gelu(x @ w_gate) * (x @ w_up) @ w_down
    x = A_x
    mod = (_silu(c) @ w_mod).reshape(c.shape[0], 6, 1, -1)
    sa, ca, ga, sb, cb, gb = (mod[:, 0].clone(), mod[:, 1].clone(), mod[:, 2].clone(), mod[:, 3].clone(), mod[:, 4].clone(), mod[:, 5].clone())
    h = _layernorm(x, axis=-1) * (1 + ca) + sa
    x = x + ga * _A_ref(h, A_k_cache, A_v_cache, A_wq, A_wo)
    h = _layernorm(x, axis=-1) * (1 + cb) + sb
    x = x + gb * _B_ref(h, B_w_gate, B_w_up, B_w_down)
    return x
```

```cpp
#include <hip/hip_runtime.h>
#include <hip/hip_bf16.h>
#include <stdint.h>

typedef unsigned short u16;
typedef unsigned v4u __attribute__((ext_vector_type(4)));
typedef float f32x4 __attribute__((ext_vector_type(4)));
typedef __bf16 bf16x2 __attribute__((ext_vector_type(2)));

#define ROUND_SCORES 0

__device__ __forceinline__ float bf2f(unsigned b) { return __builtin_bit_cast(float, (b & 0xffffu) << 16); }
__device__ __forceinline__ float bfhi(unsigned b) { return __builtin_bit_cast(float, b & 0xffff0000u); }
__device__ __forceinline__ float rbf(float f) { __bf16 b = (__bf16)f; return (float)b; }
__device__ __forceinline__ u16 f2bf(float f) { __bf16 b = (__bf16)f; return __builtin_bit_cast(u16, b); }
__device__ __forceinline__ float dot2(unsigned k, unsigned q, float acc) {
  return __builtin_amdgcn_fdot2_f32_bf16(__builtin_bit_cast(bf16x2, k), __builtin_bit_cast(bf16x2, q), acc, false);
}
__device__ __forceinline__ float wave_sum(float v) {
  for (int o = 32; o > 0; o >>= 1) v += __shfl_xor(v, o);
  return v;
}
__device__ __forceinline__ float wave_max(float v) {
  for (int o = 32; o > 0; o >>= 1) v = fmaxf(v, __shfl_xor(v, o));
  return v;
}

template <int K, int N>
__device__ __forceinline__ void gemv64(const float* sin  , const u16* W, int n0, float* red  , float& result) {
  const int tid = threadIdx.x, kq = tid >> 3, c8 = tid & 7;
  float acc[8] = {0, 0, 0, 0, 0, 0, 0, 0};
#pragma unroll 4
  for (int k = kq; k < K; k += 32) {
    v4u w = *(const v4u*)(W + (size_t)k * N + n0 + c8 * 8);
    float a = sin[k];
#pragma unroll
    for (int j = 0; j < 4; ++j) { acc[2 * j] = fmaf(a, bf2f(w[j]), acc[2 * j]); acc[2 * j + 1] = fmaf(a, bfhi(w[j]), acc[2 * j + 1]); }
  }
#pragma unroll
  for (int j = 0; j < 8; ++j) red[kq * 64 + c8 * 8 + j] = acc[j];
  __syncthreads();
  float s = 0;
  if (tid < 64) { for (int g = 0; g < 32; ++g) s += red[g * 64 + tid]; }
  result = s;
}

__global__ void __launch_bounds__(256) k_mod(const u16* c, const u16* wmod, float* mod) {
  __shared__ float sin[512]; __shared__ float red[32 * 64];
  for (int i = threadIdx.x; i < 512; i += 256) { float v = bf2f(c[i]); sin[i] = rbf(v / (1.f + __expf(-v))); }
  __syncthreads();
  float r; gemv64<512, 3072>(sin, wmod, blockIdx.x * 64, red, r);
  if (threadIdx.x < 64) mod[blockIdx.x * 64 + threadIdx.x] = rbf(r);
}

__device__ __forceinline__ void ln_mod(const float* xin, const float* shift, const float* scale, float* out, float* tmp) {
  const int tid = threadIdx.x;
  float a = xin[tid], b = xin[tid + 256];
  float s = wave_sum(a + b);
  if ((tid & 63) == 0) tmp[tid >> 6] = s;
  __syncthreads();
  float mean = (tmp[0] + tmp[1] + tmp[2] + tmp[3]) * (1.f / 512.f);
  __syncthreads();
  float da = a - mean, db = b - mean;
  float s2 = wave_sum(da * da + db * db);
  if ((tid & 63) == 0) tmp[tid >> 6] = s2;
  __syncthreads();
  float var = (tmp[0] + tmp[1] + tmp[2] + tmp[3]) * (1.f / 512.f);
  float mb = rbf(mean), vb = rbf(var);
  float den = rbf(sqrtf(rbf(vb + 1e-5f)));
  float la = rbf(rbf(a - mb) / den), lb = rbf(rbf(b - mb) / den);
  out[tid] = rbf(rbf(la * rbf(1.f + scale[tid])) + shift[tid]);
  out[tid + 256] = rbf(rbf(lb * rbf(1.f + scale[tid + 256])) + shift[tid + 256]);
  __syncthreads();
}

__global__ void __launch_bounds__(256) k_q(const u16* x, const float* mod, const u16* wq, u16* q) {
  __shared__ float xin[512]; __shared__ float h[512]; __shared__ float red[32 * 64]; __shared__ float tmp[4];
  for (int i = threadIdx.x; i < 512; i += 256) xin[i] = bf2f(x[i]);
  __syncthreads();
  ln_mod(xin, mod, mod + 512, h, tmp);
  float r; gemv64<512, 4096>(h, wq, blockIdx.x * 64, red, r);
  if (threadIdx.x < 64) q[blockIdx.x * 64 + threadIdx.x] = f2bf(r);
}

__global__ void __launch_bounds__(256) k_attn(const u16* q, const u16* K, const u16* V, float* pm, float* pl, float* po) {
  __shared__ float sc[4][512];
  __shared__ float red[4][4][128];
  __shared__ float mx[4][4], sm[4][4];
  const int kvh = blockIdx.x >> 5, sp = blockIdx.x & 31;
  const int tid = threadIdx.x, lane = tid & 63, w = tid >> 6, r = lane >> 4, c = lane & 15;
  const u16* Kb = K + ((size_t)kvh * 16384 + sp * 512) * 128;
  const u16* Vb = V + ((size_t)kvh * 16384 + sp * 512) * 128;
  v4u qv[4];
#pragma unroll
  for (int g = 0; g < 4; ++g) qv[g] = *(const v4u*)(q + (kvh * 4 + g) * 128 + c * 8);
#pragma unroll 8
  for (int i = 0; i < 32; ++i) {
    int key = w * 128 + i * 4 + r;
    v4u kv = *(const v4u*)(Kb + (size_t)key * 128 + c * 8);
#pragma unroll
    for (int g = 0; g < 4; ++g) {
      float d = 0;
#pragma unroll
      for (int j = 0; j < 4; ++j) d = dot2(kv[j], qv[g][j], d);
      d += __shfl_xor(d, 1); d += __shfl_xor(d, 2); d += __shfl_xor(d, 4); d += __shfl_xor(d, 8);
      d = d * 0.08838834764831845f;
      if (c == 0) sc[g][key] = d;
    }
  }
  __syncthreads();
  float m = -INFINITY;
  for (int i = lane; i < 512; i += 64) m = fmaxf(m, sc[w][i]);
  m = wave_max(m);
  float l = 0;
  for (int i = lane; i < 512; i += 64) { float p = __expf(sc[w][i] - m); sc[w][i] = p; l += p; }
  l = wave_sum(l);
  __syncthreads();
  float acc[4][8];
#pragma unroll
  for (int g = 0; g < 4; ++g)
#pragma unroll
    for (int j = 0; j < 8; ++j) acc[g][j] = 0;
#pragma unroll 8
  for (int i = 0; i < 32; ++i) {
    int key = w * 128 + i * 4 + r;
    v4u vv = *(const v4u*)(Vb + (size_t)key * 128 + c * 8);
#pragma unroll
    for (int g = 0; g < 4; ++g) {
      float p = sc[g][key];
#pragma unroll
      for (int j = 0; j < 4; ++j) { acc[g][2 * j] = fmaf(p, bf2f(vv[j]), acc[g][2 * j]); acc[g][2 * j + 1] = fmaf(p, bfhi(vv[j]), acc[g][2 * j + 1]); }
    }
  }
#pragma unroll
  for (int g = 0; g < 4; ++g)
#pragma unroll
    for (int j = 0; j < 8; ++j) { float a = acc[g][j]; a += __shfl_xor(a, 16); a += __shfl_xor(a, 32); if (r == 0) red[w][g][c * 8 + j] = a; }
  __syncthreads();
  for (int i = tid; i < 512; i += 256) { int g = i >> 7, d = i & 127;
    po[((size_t)(kvh * 4 + g) * 32 + sp) * 128 + d] = red[0][g][d] + red[1][g][d] + red[2][g][d] + red[3][g][d]; }
  if (lane == 0) { pm[(kvh * 4 + w) * 32 + sp] = m; pl[(kvh * 4 + w) * 32 + sp] = l; }
}

__global__ void __launch_bounds__(256) k_wo(const float* pm, const float* pl, const float* po, const u16* wo, float* part) {
  __shared__ float o[128]; __shared__ float red[32 * 64];
  const int h = blockIdx.x >> 3, cc = blockIdx.x & 7, tid = threadIdx.x;
  if (tid < 128) {
    float M = -INFINITY;
    for (int s = 0; s < 32; ++s) M = fmaxf(M, pm[h * 32 + s]);
    float L = 0, a = 0;
    for (int s = 0; s < 32; ++s) { float wgt = __expf(pm[h * 32 + s] - M); L += wgt * pl[h * 32 + s]; a += wgt * po[((size_t)h * 32 + s) * 128 + tid]; }
    o[tid] = rbf(a / L);
  }
  __syncthreads();
  float r; gemv64<128, 512>(o, wo + (size_t)h * 128 * 512, cc * 64, red, r);
  if (tid < 64) part[h * 512 + cc * 64 + tid] = r;
}

__device__ __forceinline__ float gelu_tanh(float x) {
  float u = 0.7978845608028654f * (x + 0.044715f * x * x * x);
  return 0.5f * x * (1.f + tanhf(u));
}

__global__ void __launch_bounds__(256) k_gu(const u16* x, const float* mod, const float* part, const u16* wg, const u16* wu, float* x1out, float* g) {
  __shared__ float xin[512]; __shared__ float h[512]; __shared__ float red[32 * 64]; __shared__ float tmp[4];
  for (int i = threadIdx.x; i < 512; i += 256) {
    float s = 0; for (int hh = 0; hh < 32; ++hh) s += part[hh * 512 + i];
    float v = rbf(bf2f(x[i]) + rbf(mod[1024 + i] * rbf(s)));
    xin[i] = v; if (blockIdx.x == 0) x1out[i] = v;
  }
  __syncthreads();
  ln_mod(xin, mod + 1536, mod + 2048, h, tmp);
  float rg, ru;
  gemv64<512, 1792>(h, wg, blockIdx.x * 64, red, rg);
  __syncthreads();
  gemv64<512, 1792>(h, wu, blockIdx.x * 64, red, ru);
  if (threadIdx.x < 64) g[blockIdx.x * 64 + threadIdx.x] = rbf(rbf(gelu_tanh(rbf(rg))) * rbf(ru));
}

__global__ void __launch_bounds__(256) k_down(const float* x1, const float* mod, const float* g, const u16* wd, u16* out) {
  __shared__ float sin[1792]; __shared__ float red[32 * 64];
  for (int i = threadIdx.x; i < 1792; i += 256) sin[i] = g[i];
  __syncthreads();
  float r; gemv64<1792, 512>(sin, wd, blockIdx.x * 64, red, r);
  if (threadIdx.x < 64) { int n = blockIdx.x * 64 + threadIdx.x; out[n] = f2bf(x1[n] + rbf(mod[2560 + n] * rbf(r))); }
}

extern "C" void kernel_launch(void* const* d_in, const int* in_sizes, int n_in, void* d_out, int out_size, void* d_ws, size_t ws_size, hipStream_t stream) {
  const u16* x = (const u16*)d_in[0]; const u16* K = (const u16*)d_in[1]; const u16* V = (const u16*)d_in[2];
  const u16* wq = (const u16*)d_in[3]; const u16* wo = (const u16*)d_in[4];
  const u16* wg = (const u16*)d_in[6]; const u16* wu = (const u16*)d_in[7]; const u16* wd = (const u16*)d_in[8];
  const u16* c = (const u16*)d_in[9]; const u16* wmod = (const u16*)d_in[10];
  float* ws = (float*)d_ws;
  float* mod = ws; u16* q = (u16*)(ws + 4096); float* pm = ws + 8192; float* pl = pm + 1024; float* po = pl + 1024;
  float* part = po + 32 * 32 * 128; float* x1 = part + 32 * 512; float* g = x1 + 512;
  k_mod<<<48, 256, 0, stream>>>(c, wmod, mod);
  k_q<<<64, 256, 0, stream>>>(x, mod, wq, q);
  k_attn<<<256, 256, 0, stream>>>(q, K, V, pm, pl, po);
  k_wo<<<256, 256, 0, stream>>>(pm, pl, po, wo, part);
  k_gu<<<28, 256, 0, stream>>>(x, mod, part, wg, wu, x1, g);
  k_down<<<8, 256, 0, stream>>>(x1, mod, g, wd, (u16*)d_out);
}
```

```cpp
#include <hip/hip_runtime.h>
#include <stdint.h>

namespace {
typedef unsigned short u16;
typedef unsigned u32;
typedef unsigned long long u64;
typedef u32 v4u __attribute__((ext_vector_type(4)));
typedef u32 v2u __attribute__((ext_vector_type(2)));
typedef float f32x4 __attribute__((ext_vector_type(4)));
typedef float f32x2 __attribute__((ext_vector_type(2)));
typedef __bf16 bf16x2 __attribute__((ext_vector_type(2)));
typedef __bf16 bf16x8 __attribute__((ext_vector_type(8)));
#define GAS __attribute__((address_space(1)))
#define LAS __attribute__((address_space(3)))

#define KV_AUX 2
#define KV_SFX " nt"
#define W_AUX 2
#define WS_AUX 0

constexpr int NWG = 256, NTHR = 768;
constexpr u32 SENT = 0xFFFFFFFFu;
constexpr int SC1 = 16;
constexpr u32 SPIN_CAP = 1u << 17;
constexpr float LOG2E = 1.4426950408889634f;
constexpr int EPOCH_OFF = 0;
constexpr int H0_OFF = 4096;
constexpr int Q_OFF = 16384;
constexpr int S_OFF = 65536;
constexpr int T1_OFF = S_OFF;
constexpr int ML_OFF = T1_OFF + 524288;
constexpr int T2_OFF = ML_OFF + 65536;
constexpr int X1_OFF = T2_OFF + 131072;
constexpr int G_OFF = X1_OFF + 8192;
constexpr int MR_OFF = G_OFF + 8192;
constexpr int S_END = S_OFF + 64 * 12288;
static_assert(MR_OFF + 4096 <= S_END, "");
constexpr int WS_BYTES = S_END;
constexpr int L_K = 0, L_Q = 131072, L_ZERO = L_Q + 1024, L_FLAG = L_ZERO + 256, L_ML = L_FLAG + 64, L_RED = L_ML + 256, L_END = L_RED + 8192;

#define STAMP(k) do {} while (0)

struct Params {
  const u16 *x, *K, *V, *wq, *wo, *wg, *wu, *wd, *c, *wmod;
  u16* out;
  void* ws;
};

__device__ __forceinline__ float asf(u32 x) { return __builtin_bit_cast(float, x); }
__device__ __forceinline__ u32 asu(float x) { return __builtin_bit_cast(u32, x); }
__device__ __forceinline__ float blo(u32 w) { return asf(w << 16); }
__device__ __forceinline__ float bhi(u32 w) { return asf(w & 0xffff0000u); }
__device__ __forceinline__ u32 pk(float lo, float hi) { bf16x2 v = {(__bf16)lo, (__bf16)hi}; return __builtin_bit_cast(u32, v); }
__device__ __forceinline__ float rbf(float f) { return (float)(__bf16)f; }
__device__ __forceinline__ float dot2(u32 a, u32 b, float c) { return __builtin_amdgcn_fdot2_f32_bf16(__builtin_bit_cast(bf16x2, a), __builtin_bit_cast(bf16x2, b), c, false); }
__device__ __forceinline__ u32 plo(u32 rowA, u32 rowB) { return __builtin_amdgcn_perm(rowB, rowA, 0x05040100u); }
__device__ __forceinline__ u32 phi(u32 rowA, u32 rowB) { return __builtin_amdgcn_perm(rowB, rowA, 0x07060302u); }
template <int CTRL> __device__ __forceinline__ float dpp(float x) { return asf(__builtin_amdgcn_mov_dpp(asu(x), CTRL, 0xf, 0xf, true)); }
template <int CTRL> __device__ __forceinline__ u32 dppu(u32 x) { return __builtin_amdgcn_mov_dpp(x, CTRL, 0xf, 0xf, true); }
constexpr int XOR1 = 0xB1, XOR2 = 0x4E, XOR7 = 0x141, ROR8 = 0x128, ROR4 = 0x124, SHL1 = 0x101, SHL2 = 0x102;
__device__ __forceinline__ float mx(float a, float b) { return __builtin_amdgcn_fmed3f(a, b, __builtin_inff()); }
__device__ __forceinline__ float tr32(float a, float b) { auto s = __builtin_amdgcn_permlane32_swap(asu(a), asu(b), false, false); return asf(s[0]) + asf(s[1]); }
__device__ __forceinline__ float tr16(float a, float b) { auto s = __builtin_amdgcn_permlane16_swap(asu(a), asu(b), false, false); return asf(s[0]) + asf(s[1]); }
__device__ __forceinline__ float tm32(float a, float b) { auto s = __builtin_amdgcn_permlane32_swap(asu(a), asu(b), false, false); return mx(asf(s[0]), asf(s[1])); }
__device__ __forceinline__ float tm16(float a, float b) { auto s = __builtin_amdgcn_permlane16_swap(asu(a), asu(b), false, false); return mx(asf(s[0]), asf(s[1])); }
__device__ __forceinline__ float row_sum(float v) { v += dpp<ROR8>(v); v += dpp<XOR7>(v); v += dpp<XOR2>(v); v += dpp<XOR1>(v); return v; }
__device__ __forceinline__ float row_max(float v) { v = mx(v, dpp<ROR8>(v)); v = mx(v, dpp<XOR7>(v)); v = mx(v, dpp<XOR2>(v)); v = mx(v, dpp<XOR1>(v)); return v; }
__device__ __forceinline__ void tr8to2(const float* x, float& z0, float& z1) {
  const float y0 = tr32(x[0], x[4]), y1 = tr32(x[1], x[5]), y2 = tr32(x[2], x[6]), y3 = tr32(x[3], x[7]);
  z0 = tr16(y0, y2); z1 = tr16(y1, y3);
}
__device__ __forceinline__ u32 rdl(u32 v, int l) { return __builtin_amdgcn_readlane(v, l); }
__device__ __forceinline__ float rdlf(float v, int l) { return asf(__builtin_amdgcn_readlane(asu(v), l)); }
__device__ __forceinline__ u32 nosent(u32 w) { return w < 0xFFFFFFFEu ? w : 0xFFFFFFFEu; }
__device__ __forceinline__ bool valid4(v4u v) { return v.x != SENT && v.y != SENT && v.z != SENT && v.w != SENT; }
__device__ __forceinline__ bool valid2(v2u v) { return v.x != SENT && v.y != SENT; }
typedef __amdgpu_buffer_rsrc_t rsrc_t;
__device__ __forceinline__ rsrc_t mkr(const void* q, int bytes) { return __builtin_amdgcn_make_buffer_rsrc((void*)q, 0, bytes, 0x00020000); }
template <int AUX> __device__ __forceinline__ v4u ld4(rsrc_t r, int off, int so) { return __builtin_amdgcn_raw_buffer_load_b128(r, off, so, AUX); }
__device__ __forceinline__ v4u ldc(rsrc_t r, int off) { return __builtin_amdgcn_raw_buffer_load_b128(r, off, 0, SC1); }
__device__ __forceinline__ v2u ldc2(rsrc_t r, int off) { return __builtin_amdgcn_raw_buffer_load_b64(r, off, 0, SC1); }
__device__ __forceinline__ u32 ldc1(rsrc_t r, int off) { return __builtin_amdgcn_raw_buffer_load_b32(r, off, 0, SC1); }
__device__ __forceinline__ void stc(rsrc_t r, int off, v4u v) { __builtin_amdgcn_raw_buffer_store_b128(v, r, off, 0, SC1); }
__device__ __forceinline__ void stc2(rsrc_t r, int off, v2u v) { __builtin_amdgcn_raw_buffer_store_b64(v, r, off, 0, SC1); }
__device__ __forceinline__ void stc1(rsrc_t r, int off, u32 v) { __builtin_amdgcn_raw_buffer_store_b32(v, r, off, 0, SC1); }
#define MEMFENCE() asm volatile("" ::: "memory")
#define LGKM0() asm volatile("s_waitcnt lgkmcnt(0)" ::: "memory")
#define VM0() asm volatile("s_waitcnt vmcnt(0)" ::: "memory")
#define SCHED() __builtin_amdgcn_sched_barrier(0)
#define BARRIER() do { LGKM0(); SCHED(); __builtin_amdgcn_s_barrier(); MEMFENCE(); SCHED(); } while (0)
#define POLL(LOADS, COND) do { bool ok_ = false; _Pragma("nounroll") for (u32 s_ = 0; s_ < SPIN_CAP; ++s_) { if (!ok_) { LOADS; ok_ = (COND); } if (__all(ok_)) break; MEMFENCE(); } } while (0)
__device__ __forceinline__ void lds_wait_ge(volatile LAS u32* f, u32 want) {
  _Pragma("nounroll") for (u32 s = 0; s < (SPIN_CAP << 3); ++s) { if (*f >= want) break; __builtin_amdgcn_s_sleep(1); }
  MEMFENCE();
}
__device__ __forceinline__ float silu(float v) { return v * __builtin_amdgcn_rcpf(1.f + __expf(-v)); }
__device__ __forceinline__ u32 silu2(u32 c) { return pk(silu(blo(c)), silu(bhi(c))); }
__device__ __forceinline__ void ln_stats(v4u xv, float& mb, float& inv) {
  const float piv = blo(__builtin_amdgcn_readfirstlane(xv[0]));
  float s = 0.f, q = 0.f;
#pragma unroll
  for (int i = 0; i < 4; ++i) { const float a = blo(xv[i]) - piv, b = bhi(xv[i]) - piv; s += a + b; q = fmaf(a, a, q); q = fmaf(b, b, q); }
  float t = row_sum(tr32(s, q));
  t = tr16(t, t);
  const float sm = rdlf(t, 0) * (1.f / 512.f), sq = rdlf(t, 32) * (1.f / 512.f);
  const float var = fmaxf(sq - sm * sm, 0.f);
  mb = rbf(piv + sm);
  inv = __builtin_amdgcn_rcpf(rbf(__builtin_amdgcn_sqrtf(rbf(rbf(var) + 1e-5f))));
}
__device__ __forceinline__ u32 ln_pair(u32 x, float mb, float inv) { const u32 d = pk(blo(x) - mb, bhi(x) - mb); return pk(blo(d) * inv, bhi(d) * inv); }
__device__ __forceinline__ u32 one_plus(u32 c) { return pk(1.f + blo(c), 1.f + bhi(c)); }
__device__ __forceinline__ u32 modulate(u32 y, u32 c1, u32 sh) { const u32 z = pk(blo(y) * blo(c1), bhi(y) * bhi(c1)); return pk(blo(z) + blo(sh), bhi(z) + bhi(sh)); }
__device__ __forceinline__ void pairs8(v4u a, v4u b, u32* o) {
#pragma unroll
  for (int d = 0; d < 4; ++d) { o[2 * d] = plo(a[d], b[d]); o[2 * d + 1] = phi(a[d], b[d]); }
}

__global__ void __launch_bounds__(NTHR, 3) fused_kernel(Params p) {
  __shared__ __attribute__((aligned(16))) unsigned char lds_raw[L_END];
  LAS unsigned char* L = (LAS unsigned char*)lds_raw;
  volatile LAS u32* FL = (volatile LAS u32*)(L + L_FLAG);
  LAS float* RED = (LAS float*)(L + L_RED);
  const int tid = threadIdx.x, lane = tid & 63, wave = __builtin_amdgcn_readfirstlane(tid >> 6);
  const int bid = blockIdx.x, xc = bid & 7, ix = bid >> 3, ra = ix >> 3, rb = ix & 7;
  const rsrc_t ws = mkr(p.ws, WS_BYTES);
  if (wave == 0) { STAMP(0); ((LAS u32*)(L + L_ZERO))[lane] = 0u; if (lane < 16) FL[lane] = 0u; }
  BARRIER();

  if (wave >= 4) {
    const int aw = wave - 4, g = lane >> 4, n = lane & 15;
    if (ra == 0) {
      const int rho = lane >> 3, kap = lane & 7;
      const u32 tag = ldc1(ws, EPOCH_OFF) + 1u;
      const rsrc_t rx = mkr(p.x, 1024), rq = mkr(p.wq, 512 * 4096 * 2);
      const v4u xs = ld4<0>(rx, lane * 16, 0), xo = ld4<0>(rx, rho * 16, aw * 128);
      v4u w[8];
#pragma unroll
      for (int i = 0; i < 8; ++i) w[i] = ld4<W_AUX>(rq, rho * 65536 + kap * 16, (aw * 64 + i) * 8192 + xc * 1024 + rb * 128);
      BARRIER();
      float mb, inv;
      ln_stats(xs, mb, inv);
      u32 y[4], wp[4][8];
#pragma unroll
      for (int i = 0; i < 4; ++i) { y[i] = ln_pair(xo[i], mb, inv); pairs8(w[2 * i], w[2 * i + 1], wp[i]); }
      SCHED();
      v4u h0, h1, h2, h3;
      const int hb = H0_OFF + (4 * aw + (rho >> 1)) * 128 + (rho & 1) * 32;
      POLL((h0 = ldc(ws, hb), h1 = ldc(ws, hb + 16), h2 = ldc(ws, hb + 4096), h3 = ldc(ws, hb + 4112)),
           h0.x == tag && h0.z == ~tag && h1.x == tag && h1.z == ~tag && h2.x == tag && h2.z == ~tag && h3.x == tag && h3.z == ~tag);
      if (aw == 0) STAMP(3);
      const u32 sa[4] = {h0.y, h0.w, h1.y, h1.w}, ca[4] = {h2.y, h2.w, h3.y, h3.w};
      float acc[8];
#pragma unroll
      for (int k = 0; k < 8; ++k) acc[k] = 0.f;
#pragma unroll
      for (int i = 0; i < 4; ++i) {
        const u32 hp = modulate(y[i], one_plus(ca[i]), sa[i]);
#pragma unroll
        for (int k = 0; k < 8; ++k) acc[k] = dot2(wp[i][k], hp, acc[k]);
      }
      float z0, z1;
      tr8to2(acc, z0, z1);
      z0 += dpp<ROR8>(z0); z1 += dpp<ROR8>(z1);
      if (!(lane & 8)) *(LAS f32x2*)(RED + aw * 64 + kap * 8 + g * 2) = f32x2{z0, z1};
      BARRIER();
      if (aw == 0) {
        float s = 0.f;
#pragma unroll
        for (int w2 = 0; w2 < 8; ++w2) s += RED[w2 * 64 + lane];
        const u32 d = pk(s, dpp<SHL1>(s)), d2 = dppu<SHL2>(d);
        if ((lane & 3) == 0) stc(ws, Q_OFF + (xc * 8 + rb) * 256 + lane * 4, v4u{tag, d, ~tag, d2});
        STAMP(4);
      }
      SCHED();
    }
    const u32 key0 = (u32)(xc * 16384 + ix * 512 + aw * 64);
    {
      const GAS unsigned char* kb = (const GAS unsigned char*)p.K + (size_t)key0 * 256;
      const u32 b0 = g * 256 + ((n ^ g) << 4);
      const u32 o0 = b0, o1 = b0 ^ 64u, o2 = b0 ^ 128u, o3 = b0 ^ 192u;
      const u32 ldst = (u32)(uintptr_t)(L + L_K) + aw * 16384;
#pragma unroll
      for (int t = 0; t < 4; ++t) {
        u32 keep;
        asm volatile("s_mov_b32 %0, m0\n\ts_mov_b32 m0, %6\n\ts_nop 4\n\t"
                     "global_load_lds_dwordx4 %1, %5" KV_SFX "\n\t"
                     "global_load_lds_dwordx4 %2, %5 offset:1024" KV_SFX "\n\t"
                     "global_load_lds_dwordx4 %3, %5 offset:2048" KV_SFX "\n\t"
                     "global_load_lds_dwordx4 %4, %5 offset:3072" KV_SFX "\n\t"
                     "s_mov_b32 m0, %0"
                     : "=&s"(keep) : "v"(o0), "v"(o1), "v"(o2), "v"(o3), "s"(kb + t * 4096), "s"(ldst + t * 4096) : "memory");
      }
    }
    v4u vv[16];
    {
      const rsrc_t rv = mkr(p.V, 8 * 16384 * 256);
      const int vo = g * 4096 + n * 16;
#pragma unroll
      for (int i = 0; i < 16; ++i) vv[i] = ld4<KV_AUX>(rv, vo + i * 256, key0 * 256);
    }
    SCHED();
    if (lane == 0) __hip_atomic_fetch_add((LAS u32*)&FL[2], 1u, __ATOMIC_RELAXED, __HIP_MEMORY_SCOPE_WORKGROUP);
    lds_wait_ge(&FL[1], 1u);
    asm volatile("s_waitcnt vmcnt(16)" ::: "memory");
    SCHED();
    f32x4 sacc = {0.f, 0.f, 0.f, 0.f};
    {
      const LAS unsigned char* kbase = L + L_K + aw * 16384 + n * 256;
#pragma unroll
      for (int t = 0; t < 4; ++t) {
        const LAS unsigned char* qa = ((n >> 2) == t) ? (L + L_Q + (n & 3) * 256 + g * 16) : (L + L_ZERO + g * 16);
#pragma unroll
        for (int s = 0; s < 4; ++s) {
          const v4u a = *(const LAS v4u*)(qa + 64 * s);
          const v4u b = *(const LAS v4u*)(kbase + t * 4096 + (((4 * s + g) ^ n) << 4));
          sacc = __builtin_amdgcn_mfma_f32_16x16x32_bf16(__builtin_bit_cast(bf16x8, a), __builtin_bit_cast(bf16x8, b), sacc, 0, 0, 0);
        }
      }
    }
    float s2[4], pe[4];
    {
      const u32 a = pk(sacc[0], sacc[1]), b = pk(sacc[2], sacc[3]);
      constexpr float SC = 0.08838834764831845f;
      const u32 c = pk(blo(a) * SC, bhi(a) * SC), d = pk(blo(b) * SC, bhi(b) * SC);
      s2[0] = blo(c); s2[1] = bhi(c); s2[2] = blo(d); s2[3] = bhi(d);
    }
    const float mrow = row_max(tm16(tm32(s2[0], s2[2]), tm32(s2[1], s2[3])));
#pragma unroll
    for (int h = 0; h < 4; ++h) pe[h] = __builtin_amdgcn_exp2f((s2[h] - rdlf(mrow, 16 * h)) * LOG2E);
    const u32 w0 = pk(pe[0], pe[1]), w1 = pk(pe[2], pe[3]);
    const float lrow = row_sum(tr16(tr32(pe[0], pe[2]), tr32(pe[1], pe[3])));
    if (n == 0) *(LAS f32x2*)(L + L_ML + (g * 8 + aw) * 8) = f32x2{mrow, lrow};
    const int hh = n >> 2, e2 = n & 3;
    const u32 two = (u32)(2 * hh) | ((u32)(2 * hh + 1) << 8);
    const u32 selv = (e2 & 1) ? ((two << 16) | 0x0C0Cu) : (0x0C0C0000u | two);
    const u32 sel0 = (e2 >> 1) == 0 ? selv : 0x0C0C0C0Cu, sel1 = (e2 >> 1) == 1 ? selv : 0x0C0C0C0Cu;
    f32x4 o1 = {0.f, 0.f, 0.f, 0.f}, o2 = {0.f, 0.f, 0.f, 0.f};
#define PV_STEP(i) { const u32 b0_ = dppu<0x150 + (i)>(w0), b1_ = dppu<0x150 + (i)>(w1); \
      const u32 x0_ = __builtin_amdgcn_perm(b1_, b0_, sel0), x1_ = __builtin_amdgcn_perm(b1_, b0_, sel1); \
      o1 = __builtin_amdgcn_mfma_f32_16x16x32_bf16(__builtin_bit_cast(bf16x8, vv[i]), __builtin_bit_cast(bf16x8, (v4u{x0_, x1_, 0u, 0u})), o1, 0, 0, 0); \
      o2 = __builtin_amdgcn_mfma_f32_16x16x32_bf16(__builtin_bit_cast(bf16x8, vv[i]), __builtin_bit_cast(bf16x8, (v4u{0u, 0u, x0_, x1_})), o2, 0, 0, 0); }
    PV_STEP(0) PV_STEP(1) PV_STEP(2) PV_STEP(3) PV_STEP(4) PV_STEP(5) PV_STEP(6) PV_STEP(7)
    PV_STEP(8) PV_STEP(9) PV_STEP(10) PV_STEP(11) PV_STEP(12) PV_STEP(13) PV_STEP(14) PV_STEP(15)
#undef PV_STEP
    {
      LAS unsigned char* ob = L + L_K + aw * 16384 + (hh * 128 + e2 * 16 + 4 * g) * 4;
      *(LAS f32x4*)ob = o1; *(LAS f32x4*)(ob + 256) = o2;
    }
    BARRIER();
    if (aw < 2) {
      if (aw == 0) STAMP(12);
      const int tau = aw * 64 + lane, h = tau >> 5;
      float m[8], l[8];
#pragma unroll
      for (int w = 0; w < 4; ++w) { const f32x4 t = *(const LAS f32x4*)(L + L_ML + h * 64 + w * 16); m[2 * w] = t.x; l[2 * w] = t.y; m[2 * w + 1] = t.z; l[2 * w + 1] = t.w; }
      f32x4 part[8];
#pragma unroll
      for (int w = 0; w < 8; ++w) part[w] = *(const LAS f32x4*)(L + L_K + w * 16384 + tau * 16);
      const float M = mx(mx(mx(m[0], m[1]), mx(m[2], m[3])), mx(mx(m[4], m[5]), mx(m[6], m[7])));
      f32x4 acc = {0.f, 0.f, 0.f, 0.f}; float Ls = 0.f;
#pragma unroll
      for (int w = 0; w < 8; ++w) { const float f = __builtin_amdgcn_exp2f((m[w] - M) * LOG2E); acc += part[w] * f; Ls = fmaf(l[w], f, Ls); }
      stc(ws, T1_OFF + (((xc * 4 + h) * 4 + ((tau >> 3) & 3)) * 32 + ix) * 128 + (tau & 7) * 16, __builtin_bit_cast(v4u, acc));
      if ((lane & 31) == 0) stc2(ws, ML_OFF + ((xc * 32 + ix) * 2 + aw) * 128 + (lane >> 5) * 8, v2u{asu(M), asu(Ls)});
      if (aw == 0) STAMP(13);
    }
    SCHED();
    if (ra == 2) {
      const int lam = xc + 8 * (rb >> 1);
      if (lam >= 28) return;
      const int rho = lane >> 2, kap = lane & 3, col = lam * 64 + (rb & 1) * 32;
      const rsrc_t rg = mkr(p.wg, 512 * 1792 * 2), ru = mkr(p.wu, 512 * 1792 * 2);
      v4u w[8];
#pragma unroll
      for (int i = 0; i < 4; ++i) {
        w[i] = ld4<WS_AUX>(rg, rho * 4 * 3584 + kap * 16, (aw * 64 + i) * 3584 + col * 2);
        w[4 + i] = ld4<WS_AUX>(ru, rho * 4 * 3584 + kap * 16, (aw * 64 + i) * 3584 + col * 2);
      }
      v2u sb, cb;
      const int mo = MR_OFF + 1024 + aw * 128 + rho * 8;
      POLL((sb = ldc2(ws, mo), cb = ldc2(ws, mo + 1024)), valid2(sb) && valid2(cb));
      u32 wp[2][16];
#pragma unroll
      for (int i = 0; i < 2; ++i) { pairs8(w[2 * i], w[2 * i + 1], wp[i]); pairs8(w[4 + 2 * i], w[4 + 2 * i + 1], wp[i] + 8); }
      const u32 c1[2] = {one_plus(cb.x), one_plus(cb.y)};
      if (aw == 0) STAMP(23);
      SCHED();
      v4u xs; v2u xo;
      const int xb = X1_OFF + lane * 128, xb2 = X1_OFF + (8 * aw + (rho >> 1)) * 128 + (rho & 1) * 8;
      POLL((xs = ldc(ws, xb), xo = ldc2(ws, xb2)), valid4(xs) && valid2(xo));
      if (aw == 0) STAMP(18);
      float mb, inv;
      ln_stats(xs, mb, inv);
      const u32 hp0 = modulate(ln_pair(xo.x, mb, inv), c1[0], sb.x), hp1 = modulate(ln_pair(xo.y, mb, inv), c1[1], sb.y);
      float acc[16];
#pragma unroll
      for (int k = 0; k < 16; ++k) acc[k] = dot2(wp[1][k], hp1, dot2(wp[0][k], hp0, 0.f));
      float y[8];
#pragma unroll
      for (int k = 0; k < 8; ++k) y[k] = tr32(acc[k], acc[k + 8]);
      f32x4 z;
#pragma unroll
      for (int k = 0; k < 4; ++k) { float t = tr16(y[k], y[k + 4]); t += dpp<ROR4>(t); t += dpp<ROR8>(t); z[k] = t; }
      if (!(lane & 12)) *(LAS f32x4*)(RED + aw * 64 + (lane >> 5) * 32 + kap * 8 + (g & 1) * 4) = z;
      BARRIER();
      if (aw != 0) return;
      float s = 0.f;
#pragma unroll
      for (int w2 = 0; w2 < 8; ++w2) s += RED[w2 * 64 + lane];
      s = rbf(s);
      auto sw = __builtin_amdgcn_permlane32_swap(asu(s), asu(s), false, false);
      const float gt = asf(sw[0]), up = asf(sw[1]);
      const float u2 = 2.f * 0.7978845608028654f * (gt + 0.044715f * gt * gt * gt);
      const float gv = rbf(gt * __builtin_amdgcn_rcpf(1.f + __expf(-u2))) * up;
      const u32 d = nosent(pk(gv, dpp<SHL1>(gv)));
      if (lane < 32 && !(lane & 1)) stc1(ws, G_OFF + (col >> 5) * 128 + lane * 2, d);
      STAMP(19);
    } else if (ra == 3 && rb < 4) {
      const int rho = (lane >> 1) < 28 ? (lane >> 1) : 27, kap = lane & 1, col = xc * 64 + rb * 16;
      const rsrc_t rd = mkr(p.wd, 1792 * 512 * 2);
      v4u w[8];
#pragma unroll
      for (int i = 0; i < 8; ++i) w[i] = ld4<WS_AUX>(rd, rho * 8192 + kap * 16, (aw * 224 + i) * 1024 + col * 2);
      u32 gbw = 0, x1w = 0;
      if (aw == 0) {
        const int c2 = col + (lane & 14);
        POLL((gbw = ldc1(ws, MR_OFF + 3072 + c2 * 2), x1w = ldc1(ws, X1_OFF + (c2 >> 3) * 128 + (c2 & 7) * 2)), gbw != SENT && x1w != SENT);
      }
      u32 wp[4][8];
#pragma unroll
      for (int i = 0; i < 4; ++i) pairs8(w[2 * i], w[2 * i + 1], wp[i]);
      SCHED();
      v4u gv;
      const int e0 = aw * 224 + rho * 8;
      const int gb = G_OFF + (e0 >> 5) * 128 + (e0 & 31) * 2;
      POLL(gv = ldc(ws, gb), valid4(gv));
      if (aw == 0) STAMP(20);
      if (lane >= 56) gv = v4u{0u, 0u, 0u, 0u};
      float acc[8];
#pragma unroll
      for (int k = 0; k < 8; ++k) acc[k] = dot2(wp[3][k], gv[3], dot2(wp[2][k], gv[2], dot2(wp[1][k], gv[1], dot2(wp[0][k], gv[0], 0.f))));
      float z0, z1;
      tr8to2(acc, z0, z1);
      z0 += dpp<ROR8>(z0); z1 += dpp<ROR8>(z1); z0 += dpp<ROR4>(z0); z1 += dpp<ROR4>(z1); z0 += dpp<XOR2>(z0); z1 += dpp<XOR2>(z1);
      if (!(lane & 14)) *(LAS f32x2*)(RED + aw * 16 + kap * 8 + g * 2) = f32x2{z0, z1};
      BARRIER();
      if (aw != 0) return;
      if (lane < 16) {
        float s = 0.f;
#pragma unroll
        for (int w2 = 0; w2 < 8; ++w2) s += RED[w2 * 16 + lane];
        const float gbv = (lane & 1) ? bhi(gbw) : blo(gbw), x1v = (lane & 1) ? bhi(x1w) : blo(x1w);
        ((GAS u16*)p.out)[col + lane] = (u16)pk(x1v + rbf(gbv * rbf(s)), 0.f);
      }
      if (bid == 192 && lane == 0) stc1(ws, EPOCH_OFF, ldc1(ws, EPOCH_OFF) + 1u);
      STAMP(21);
    }
    return;
  }

  __builtin_amdgcn_s_setprio(3);
  const bool mcu = (ra == 3 && rb >= 4);
  if (wave == 1 && ra == 2 && rb == 7) asm volatile("buffer_wbl2 sc0 sc1\n\ts_waitcnt vmcnt(0)" ::: "memory");
  if (wave != 0 && !(ra <= 1 || mcu)) return;
  u32 tag = 0;
  if (wave == 0) tag = ldc1(ws, EPOCH_OFF) + 1u;
  if (ra == 0) {
    const int rho = lane >> 1, kap = lane & 1;
    const rsrc_t rm = mkr(p.wmod, 512 * 3072 * 2);
    const v2u cv = *(const GAS v2u*)((const GAS u16*)p.c + wave * 128 + rho * 4);
    v4u w[4];
#pragma unroll
    for (int i = 0; i < 4; ++i) w[i] = ld4<WS_AUX>(rm, rho * 4 * 6144 + kap * 16, (wave * 128 + i) * 6144 + xc * 256 + rb * 32);
    SCHED();
    const v4u sv = {SENT, SENT, SENT, SENT};
#pragma unroll
    for (int j = 0; j < 3; ++j) stc(ws, S_OFF + (xc * 8 + rb) * 12288 + (wave * 3 + j) * 1024 + lane * 16, sv);
    SCHED();
    u32 wp[2][8];
    pairs8(w[0], w[1], wp[0]); pairs8(w[2], w[3], wp[1]);
    const u32 s0 = silu2(cv.x), s1 = silu2(cv.y);
    float acc[8];
#pragma unroll
    for (int k = 0; k < 8; ++k) acc[k] = dot2(wp[1][k], s1, dot2(wp[0][k], s0, 0.f));
    if (wave == 0) STAMP(1);
    float z0, z1;
    tr8to2(acc, z0, z1);
    z0 += dpp<ROR8>(z0); z1 += dpp<ROR8>(z1); z0 += dpp<ROR4>(z0); z1 += dpp<ROR4>(z1); z0 += dpp<XOR2>(z0); z1 += dpp<XOR2>(z1);
    if (!(lane & 14)) *(LAS f32x2*)(RED + 1024 + wave * 16 + kap * 8 + (lane >> 4) * 2) = f32x2{z0, z1};
    VM0();
    BARRIER();
    if (wave == 0) {
      const int c = lane & 15;
      const float s = (RED[1024 + c] + RED[1040 + c]) + (RED[1056 + c] + RED[1072 + c]);
      const u32 d = pk(s, dpp<SHL1>(s)), d2 = dppu<SHL2>(d);
      if (lane < 16 && !(lane & 3)) stc(ws, H0_OFF + (xc * 8 + rb) * 128 + lane * 4, v4u{tag, d, ~tag, d2});
      STAMP(2);
    }
    BARRIER();
    if (wave != 0) return;
  }
  if (wave == 0) {
    v4u q0, q1;
    const int qb = Q_OFF + xc * 2048 + lane * 16;
    POLL((q0 = ldc(ws, qb), q1 = ldc(ws, qb + 1024)), q0.x == tag && q0.z == ~tag && q1.x == tag && q1.z == ~tag);
    *(LAS v2u*)(L + L_Q + lane * 8) = v2u{q0.y, q0.w};
    *(LAS v2u*)(L + L_Q + 512 + lane * 8) = v2u{q1.y, q1.w};
    LGKM0();
    if (lane == 0) FL[1] = 1u;
    STAMP(5);
  }
  if (ra == 0) {
    const int col = xc * 64 + rb * 8, g = lane >> 4;
    const u32 xin = *(const GAS u32*)((const GAS u16*)p.x + col + 2 * g);
    BARRIER();
    u32 ga;
    POLL(ga = ldc1(ws, MR_OFF + (col + 2 * g) * 2), ga != SENT);
    v4u a, b;
    const int tb = T2_OFF + lane * 2048 + col * 4;
    POLL((a = ldc(ws, tb), b = ldc(ws, tb + 16)), valid4(a) && valid4(b));
    STAMP(16);
    const float x8[8] = {asf(a.x), asf(a.y), asf(a.z), asf(a.w), asf(b.x), asf(b.y), asf(b.z), asf(b.w)};
    float z0, z1;
    tr8to2(x8, z0, z1);
    z0 = row_sum(z0); z1 = row_sum(z1);
    const u32 at = pk(z0, z1);
    const u32 pr = pk(blo(ga) * blo(at), bhi(ga) * bhi(at));
    const u32 d = nosent(pk(blo(xin) + blo(pr), bhi(xin) + bhi(pr)));
    const v4u o = {rdl(d, 0), rdl(d, 16), rdl(d, 32), rdl(d, 48)};
    if (lane == 0) stc(ws, X1_OFF + (xc * 8 + rb) * 128, o);
    STAMP(17);
  } else if (ra == 1) {
    const int hl = rb >> 1, hg = xc * 4 + hl, j = (rb & 1) * 4 + wave;
    const rsrc_t ro = mkr(p.wo, 4096 * 512 * 2);
    lds_wait_ge(&FL[2], 8u);
    v4u w[16];
#pragma unroll
    for (int c = 0; c < 16; ++c) w[c] = ld4<W_AUX>(ro, lane * 16, (hg * 128 + 8 * c + j) * 1024);
    BARRIER();
    u32 wp[8][8];
#pragma unroll
    for (int i = 0; i < 8; ++i) pairs8(w[2 * i], w[2 * i + 1], wp[i]);
    SCHED();
    v4u a, b; v2u ma, mb;
    const int ob = T1_OFF + ((hg * 4 + (j >> 1)) * 32 + (lane >> 2)) * 128 + (j & 1) * 64 + (lane & 3) * 16;
    const int mo = ML_OFF + ((xc * 32 + (lane >> 2)) * 2 + (hl >> 1)) * 128 + (hl & 1) * 8;
    POLL((a = ldc(ws, ob), b = ldc(ws, ob + 2048), ma = ldc2(ws, mo), mb = ldc2(ws, mo + 4096)), valid4(a) && valid4(b) && valid2(ma) && valid2(mb));
    if (wave == 0) STAMP(14);
    float M = mx(asf(ma.x), asf(mb.x));
    M = mx(M, dpp<ROR4>(M)); M = mx(M, dpp<ROR8>(M)); M = tm16(M, M); M = tm32(M, M);
    const float fa = __builtin_amdgcn_exp2f((asf(ma.x) - M) * LOG2E), fb = __builtin_amdgcn_exp2f((asf(mb.x) - M) * LOG2E);
    float Ls = fa * asf(ma.y) + fb * asf(mb.y);
    float o4[4];
#pragma unroll
    for (int k = 0; k < 4; ++k) o4[k] = fa * asf(a[k]) + fb * asf(b[k]);
    float u0 = tr32(o4[0], o4[2]), u1 = tr32(o4[1], o4[3]);
    Ls = tr32(Ls, Ls);
    u0 = tr16(u0, u0); u1 = tr16(u1, u1); Ls = tr16(Ls, Ls);
    u0 += dpp<ROR4>(u0); u1 += dpp<ROR4>(u1); Ls += dpp<ROR4>(Ls);
    u0 += dpp<ROR8>(u0); u1 += dpp<ROR8>(u1); Ls += dpp<ROR8>(Ls);
    const float inv = __builtin_amdgcn_rcpf(Ls);
    const u32 opk = pk(u0 * inv, u1 * inv);
    float acc[8];
#pragma unroll
    for (int k = 0; k < 8; ++k) acc[k] = 0.f;
#pragma unroll
    for (int i = 0; i < 8; ++i) {
      const u32 op = rdl(opk, (i >> 1) + 32 * (i & 1));
#pragma unroll
      for (int k = 0; k < 8; ++k) acc[k] = dot2(wp[i][k], op, acc[k]);
    }
    *(LAS f32x4*)(RED + wave * 512 + lane * 8) = f32x4{acc[0], acc[1], acc[2], acc[3]};
    *(LAS f32x4*)(RED + wave * 512 + lane * 8 + 4) = f32x4{acc[4], acc[5], acc[6], acc[7]};
    BARRIER();
    if (lane < 32) {
      const LAS f32x4* r = (const LAS f32x4*)(RED + wave * 128 + lane * 4);
      const f32x4 s = (r[0] + r[128]) + (r[256] + r[384]);
      stc(ws, T2_OFF + (xc * 8 + rb) * 2048 + wave * 512 + lane * 16, __builtin_bit_cast(v4u, s));
    }
    if (wave == 0) STAMP(15);
  } else if (mcu) {
    const int rho = lane >> 3, kap = lane & 7, mi = xc * 4 + (rb - 4);
    const rsrc_t rm = mkr(p.wmod, 512 * 3072 * 2);
    lds_wait_ge(&FL[1], 1u);
    const GAS v4u* cp = (const GAS v4u*)((const GAS u16*)p.c + wave * 128 + rho * 16);
    const v4u c0 = cp[0], c1 = cp[1];
    v4u w[16];
#pragma unroll
    for (int i = 0; i < 16; ++i) w[i] = ld4<W_AUX>(rm, rho * 16 * 6144 + kap * 16, (wave * 128 + i) * 6144 + 2048 + mi * 128);
    float acc[8];
#pragma unroll
    for (int k = 0; k < 8; ++k) acc[k] = 0.f;
#pragma unroll
    for (int i = 0; i < 8; ++i) {
      const u32 sp = silu2(i < 4 ? c0[i & 3] : c1[i & 3]);
      u32 wp[8];
      pairs8(w[2 * i], w[2 * i + 1], wp);
#pragma unroll
      for (int k = 0; k < 8; ++k) acc[k] = dot2(wp[k], sp, acc[k]);
    }
    float z0, z1;
    tr8to2(acc, z0, z1);
    z0 += dpp<ROR8>(z0); z1 += dpp<ROR8>(z1);
    if (!(lane & 8)) *(LAS f32x2*)(RED + wave * 64 + kap * 8 + (lane >> 4) * 2) = f32x2{z0, z1};
    LGKM0();
    u32 old = 0;
    if (lane == 0) old = __hip_atomic_fetch_add((LAS u32*)&FL[3], 1u, __ATOMIC_RELAXED, __HIP_MEMORY_SCOPE_WORKGROUP);
    old = __builtin_amdgcn_readfirstlane(old);
    MEMFENCE();
    if (old == 3u) {
      const float s = (RED[lane] + RED[64 + lane]) + (RED[128 + lane] + RED[192 + lane]);
      const u32 d = nosent(pk(s, dpp<SHL1>(s)));
      if (!(lane & 1)) stc1(ws, MR_OFF + mi * 128 + lane * 2, d);
      STAMP(22);
    }
  }
}
}

extern "C" void kernel_launch(void* const* d_in, const int* in_sizes, int n_in, void* d_out, int out_size, void* d_ws, size_t ws_size, hipStream_t stream) {
  Params p;
  p.x = (const u16*)d_in[0]; p.K = (const u16*)d_in[1]; p.V = (const u16*)d_in[2]; p.wq = (const u16*)d_in[3]; p.wo = (const u16*)d_in[4];
  p.wg = (const u16*)d_in[6]; p.wu = (const u16*)d_in[7]; p.wd = (const u16*)d_in[8]; p.c = (const u16*)d_in[9]; p.wmod = (const u16*)d_in[10];
  p.out = (u16*)d_out; p.ws = d_ws;
  hipLaunchKernelGGL(fused_kernel, dim3(NWG), dim3(NTHR), 0, stream, p);
}
```

```cpp
#include <hip/hip_runtime.h>
#include <stdint.h>

namespace {
typedef unsigned short u16;
typedef unsigned u32;
typedef unsigned long long u64;
typedef u32 v4u __attribute__((ext_vector_type(4)));
typedef u32 v2u __attribute__((ext_vector_type(2)));
typedef float f32x4 __attribute__((ext_vector_type(4)));
typedef float f32x2 __attribute__((ext_vector_type(2)));
typedef __bf16 bf16x2 __attribute__((ext_vector_type(2)));
typedef __bf16 bf16x8 __attribute__((ext_vector_type(8)));
typedef short s16x4 __attribute__((ext_vector_type(4)));
#define GAS __attribute__((address_space(1)))
#define LAS __attribute__((address_space(3)))

#define KV_AUX 2
#define KV_SFX " nt"
#define W_AUX 2
#define WS_AUX 0
#define THR_W 8
#define THR_L 32
#define N_CH 6
#define N_W 9
#define N_M 8
#define N_P 10

#define GU_FLAG 2
#define GU_CNT 4u

constexpr int NWG = 256, NTHR = 1024;
constexpr u32 SENT = 0xFFFFFFFFu;
constexpr int SC1 = 16;
constexpr u32 SPIN_CAP = 1u << 17;
constexpr float LOG2E = 1.4426950408889634f;
constexpr int EPOCH_OFF = 0;
constexpr int CF_OFF = 8192;
constexpr int Q_OFF = 16384;
constexpr int H0_OFF = 32768;
constexpr int S_OFF = 65536;
constexpr int T1_OFF = S_OFF;
constexpr int ML_OFF = T1_OFF + 524288;
constexpr int T2_OFF = ML_OFF + 65536;
constexpr int Y_OFF = T2_OFF + 131072;
constexpr int X1_OFF = Y_OFF + 114688;
constexpr int MR_OFF = X1_OFF + 8192;
constexpr int CLR_KB = 14;
constexpr int S_END = S_OFF + 64 * CLR_KB * 1024;
static_assert(MR_OFF + 4096 <= S_END, "");
constexpr int WS_BYTES = S_END;
constexpr int L_K = 0, L_Q = 8 * 16384, L_FLAG = L_Q + 1280, L_ML = L_FLAG + 64, L_X = L_ML + 256, L_RED = L_X + 1088, L_RED2 = L_RED + 4096, L_END = L_RED2 + 16384;
static_assert(L_END <= 163840 && L_RED % 16 == 0, "");
constexpr int GU_REST = 256 - 8 * N_CH - 8 * N_W - 4 * N_M - 5 * N_P, N_G = GU_REST / 7, GU_HI = GU_REST - 7 * N_G;
constexpr int ntask_of(int ix) { return ix < 8 ? N_CH : ix < 16 ? N_W : ix < 23 ? (ix - 16 < GU_HI ? N_G + 1 : N_G) : ix < 27 ? N_M : N_P; }
constexpr int toff_of(int ix) { int o = 0; for (int i = 0; i < ix; ++i) o += ntask_of(i); return o; }
constexpr u64 pack_off(int r) { u64 v = 0; for (int i = 0; i < 8; ++i) v |= (u64)toff_of(8 * r + i) << (8 * i); return v; }
constexpr u64 pack_cnt(int r) { u64 v = 0; for (int i = 0; i < 16; ++i) v |= (u64)ntask_of(16 * r + i) << (4 * i); return v; }
static_assert(toff_of(32) == 256 && N_G >= 4 && N_G + 1 <= 12 && N_CH >= 4 && N_CH <= 7 && N_P <= 12 && N_W <= 12, "");

#define STAMP(k) do {} while (0)
#define TNOW() 0ull
#define STAMPV(k, v) do { (void)(v); } while (0)

struct Params {
  const u16 *x, *K, *V, *wq, *wo, *wg, *wu, *wd, *c, *wmod;
  u16* out;
  void* ws;
};

__device__ __forceinline__ float asf(u32 x) { return __builtin_bit_cast(float, x); }
__device__ __forceinline__ u32 asu(float x) { return __builtin_bit_cast(u32, x); }
__device__ __forceinline__ float blo(u32 w) { return asf(w << 16); }
__device__ __forceinline__ float bhi(u32 w) { return asf(w & 0xffff0000u); }
__device__ __forceinline__ u32 pk(float lo, float hi) { bf16x2 v = {(__bf16)lo, (__bf16)hi}; return __builtin_bit_cast(u32, v); }
__device__ __forceinline__ float rbf(float f) { return (float)(__bf16)f; }
__device__ __forceinline__ float dot2(u32 a, u32 b, float c) { return __builtin_amdgcn_fdot2_f32_bf16(__builtin_bit_cast(bf16x2, a), __builtin_bit_cast(bf16x2, b), c, false); }
__device__ __forceinline__ u32 plo(u32 rowA, u32 rowB) { return __builtin_amdgcn_perm(rowB, rowA, 0x05040100u); }
__device__ __forceinline__ u32 phi(u32 rowA, u32 rowB) { return __builtin_amdgcn_perm(rowB, rowA, 0x07060302u); }
template <int CTRL> __device__ __forceinline__ float dpp(float x) { return asf(__builtin_amdgcn_mov_dpp(asu(x), CTRL, 0xf, 0xf, true)); }
template <int CTRL> __device__ __forceinline__ u32 dppu(u32 x) { return __builtin_amdgcn_mov_dpp(x, CTRL, 0xf, 0xf, true); }
constexpr int XOR1 = 0xB1, XOR2 = 0x4E, XOR7 = 0x141, ROR8 = 0x128, ROR4 = 0x124, SHL1 = 0x101, SHL2 = 0x102;
__device__ __forceinline__ float mx(float a, float b) { return __builtin_amdgcn_fmed3f(a, b, __builtin_inff()); }
__device__ __forceinline__ float tr32(float a, float b) { auto s = __builtin_amdgcn_permlane32_swap(asu(a), asu(b), false, false); return asf(s[0]) + asf(s[1]); }
__device__ __forceinline__ float tr16(float a, float b) { auto s = __builtin_amdgcn_permlane16_swap(asu(a), asu(b), false, false); return asf(s[0]) + asf(s[1]); }
__device__ __forceinline__ float tm32(float a, float b) { auto s = __builtin_amdgcn_permlane32_swap(asu(a), asu(b), false, false); return mx(asf(s[0]), asf(s[1])); }
__device__ __forceinline__ float tm16(float a, float b) { auto s = __builtin_amdgcn_permlane16_swap(asu(a), asu(b), false, false); return mx(asf(s[0]), asf(s[1])); }
__device__ __forceinline__ float row_sum(float v) { v += dpp<ROR8>(v); v += dpp<XOR7>(v); v += dpp<XOR2>(v); v += dpp<XOR1>(v); return v; }
__device__ __forceinline__ float row_max(float v) { v = mx(v, dpp<ROR8>(v)); v = mx(v, dpp<XOR7>(v)); v = mx(v, dpp<XOR2>(v)); v = mx(v, dpp<XOR1>(v)); return v; }
__device__ __forceinline__ void tr8to2(const float* x, float& z0, float& z1) {
  const float y0 = tr32(x[0], x[4]), y1 = tr32(x[1], x[5]), y2 = tr32(x[2], x[6]), y3 = tr32(x[3], x[7]);
  z0 = tr16(y0, y2); z1 = tr16(y1, y3);
}
__device__ __forceinline__ u32 rdl(u32 v, int l) { return __builtin_amdgcn_readlane(v, l); }
__device__ __forceinline__ float rdlf(float v, int l) { return asf(__builtin_amdgcn_readlane(asu(v), l)); }
__device__ __forceinline__ u32 nosent(u32 w) { return w < 0xFFFFFFFEu ? w : 0xFFFFFFFEu; }
__device__ __forceinline__ bool valid4(v4u v) { return max(max(v.x, v.y), max(v.z, v.w)) != SENT; }
__device__ __forceinline__ bool valid2(v2u v) { return max(v.x, v.y) != SENT; }
typedef __amdgpu_buffer_rsrc_t rsrc_t;
__device__ __forceinline__ rsrc_t mkr(const void* q, int bytes) { return __builtin_amdgcn_make_buffer_rsrc((void*)q, 0, bytes, 0x00020000); }
template <int AUX> __device__ __forceinline__ v4u ld4(rsrc_t r, int off, int so) { return __builtin_amdgcn_raw_buffer_load_b128(r, off, so, AUX); }
__device__ __forceinline__ v4u ldc(rsrc_t r, int off) { return __builtin_amdgcn_raw_buffer_load_b128(r, off, 0, SC1); }
__device__ __forceinline__ v2u ldc2(rsrc_t r, int off) { return __builtin_amdgcn_raw_buffer_load_b64(r, off, 0, SC1); }
__device__ __forceinline__ u32 ldc1(rsrc_t r, int off) { return __builtin_amdgcn_raw_buffer_load_b32(r, off, 0, SC1); }
__device__ __forceinline__ void stc(rsrc_t r, int off, v4u v) { __builtin_amdgcn_raw_buffer_store_b128(v, r, off, 0, SC1); }
__device__ __forceinline__ void stc2(rsrc_t r, int off, v2u v) { __builtin_amdgcn_raw_buffer_store_b64(v, r, off, 0, SC1); }
__device__ __forceinline__ void stc1(rsrc_t r, int off, u32 v) { __builtin_amdgcn_raw_buffer_store_b32(v, r, off, 0, SC1); }
#define MEMFENCE() asm volatile("" ::: "memory")
#define LGKM0() asm volatile("s_waitcnt lgkmcnt(0)" ::: "memory")
#define VM0() asm volatile("s_waitcnt vmcnt(0)" ::: "memory")
#define SCHED() __builtin_amdgcn_sched_barrier(0)
#define BARRIER() do { LGKM0(); SCHED(); __builtin_amdgcn_s_barrier(); MEMFENCE(); SCHED(); } while (0)
#define POLL(LOADS, COND) do { bool ok_ = false; _Pragma("nounroll") for (u32 s_ = 0; s_ < SPIN_CAP; ++s_) { if (!ok_) { LOADS; SCHED(); ok_ = (COND); } if (__all(ok_)) break; MEMFENCE(); } } while (0)
#define PIN8(a) asm volatile("" : "+v"((a)[0]), "+v"((a)[1]), "+v"((a)[2]), "+v"((a)[3]), "+v"((a)[4]), "+v"((a)[5]), "+v"((a)[6]), "+v"((a)[7]))
#define PIN1(a) asm volatile("" : "+v"(a))
__device__ __forceinline__ void lds_wait_ge(volatile LAS u32* f, u32 want) {
  _Pragma("nounroll") for (u32 s = 0; s < (SPIN_CAP << 3); ++s) { if (*f >= want) break; __builtin_amdgcn_s_sleep(1); }
  MEMFENCE();
}
__device__ __forceinline__ void lds_inc(volatile LAS u32* f, int lane) { if (lane == 0) __hip_atomic_fetch_add((LAS u32*)f, 1u, __ATOMIC_RELAXED, __HIP_MEMORY_SCOPE_WORKGROUP); }
__device__ __forceinline__ float silu(float v) { return v / (1.f + __expf(-v)); }
__device__ __forceinline__ u32 silu2(u32 c) { return pk(silu(blo(c)), silu(bhi(c))); }
__device__ __forceinline__ void ln_stats(v4u xv, float& mb, float& inv) {
  const float piv = blo(__builtin_amdgcn_readfirstlane(xv[0]));
  float s[4], q[4];
#pragma unroll
  for (int i = 0; i < 4; ++i) { const float a = blo(xv[i]) - piv, b = bhi(xv[i]) - piv; s[i] = a + b; q[i] = fmaf(a, a, b * b); }
  float t = row_sum(tr32((s[0] + s[1]) + (s[2] + s[3]), (q[0] + q[1]) + (q[2] + q[3])));
  t = tr16(t, t);
  const float sm = rdlf(t, 0) * (1.f / 512.f), sq = rdlf(t, 32) * (1.f / 512.f);
  const float var = fmaxf(sq - sm * sm, 0.f);
  mb = rbf(piv + sm);
  inv = __builtin_amdgcn_rcpf(rbf(__builtin_amdgcn_sqrtf(rbf(rbf(var) + 1e-5f))));
}
__device__ __forceinline__ u32 ln_pair(u32 x, float mb, float inv) { const u32 d = pk(blo(x) - mb, bhi(x) - mb); return pk(blo(d) * inv, bhi(d) * inv); }
__device__ __forceinline__ u32 one_plus(u32 c) { return pk(1.f + blo(c), 1.f + bhi(c)); }
__device__ __forceinline__ u32 modulate(u32 y, u32 c1, u32 sh) { const u32 z = pk(blo(y) * blo(c1), bhi(y) * bhi(c1)); return pk(blo(z) + blo(sh), bhi(z) + bhi(sh)); }
__device__ __forceinline__ void pairs8(v4u a, v4u b, u32* o) {
#pragma unroll
  for (int d = 0; d < 4; ++d) { o[2 * d] = plo(a[d], b[d]); o[2 * d + 1] = phi(a[d], b[d]); }
  PIN8(o);
}

__device__ __forceinline__ u32 lds_ticket(volatile LAS u32* f, int lane) {
  u32 old = 0;
  LGKM0();
  if (lane == 0) old = __hip_atomic_fetch_add((LAS u32*)f, 1u, __ATOMIC_RELAXED, __HIP_MEMORY_SCOPE_WORKGROUP);
  old = __builtin_amdgcn_readfirstlane(old);
  MEMFENCE();
  return old;
}
__device__ __forceinline__ float sum8(const LAS float* r, int stride) {
  float v[8];
#pragma unroll
  for (int i = 0; i < 8; ++i) v[i] = r[i * stride];
  return ((v[0] + v[1]) + (v[2] + v[3])) + ((v[4] + v[5]) + (v[6] + v[7]));
}

__global__ void __launch_bounds__(NTHR, 4) fused_kernel(Params p) {
  __shared__ __attribute__((aligned(16))) unsigned char lds_raw[L_END];
  LAS unsigned char* L = (LAS unsigned char*)lds_raw;
  volatile LAS u32* FL = (volatile LAS u32*)(L + L_FLAG);
  LAS float* RED = (LAS float*)(L + L_RED);
  LAS float* RED2 = (LAS float*)(L + L_RED2);
  const int tid = threadIdx.x, lane = tid & 63, wave = __builtin_amdgcn_readfirstlane(tid >> 6);
  const int bid = blockIdx.x, xc = bid & 7, ix = bid >> 3, rb = ix & 7;
  const bool chain = ix < 8;
  const int ntask = (int)(((ix < 16 ? pack_cnt(0) : pack_cnt(1)) >> (4 * (ix & 15))) & 15);
  const u32 nact = ntask < 8 ? ntask : 8;
#define MKWS() const rsrc_t ws = mkr(p.ws, WS_BYTES)
  const u64 t0 = TNOW();
  if (wave == 0 && lane < 16) FL[lane] = 0u;

  if (__builtin_expect(wave >= 8, 1)) {
    const int aw = wave - 8, g = lane >> 4, n = lane & 15;
    const bool early = aw < 4;
    if (__builtin_expect(chain, 0)) {
      BARRIER();
      if (aw == 7) {
        MKWS();
        u32 ep = ldc1(ws, EPOCH_OFF);
        const v4u sv = {SENT, SENT, SENT, SENT};
#pragma unroll
        for (int j = 0; j < CLR_KB; ++j) stc(ws, S_OFF + (xc * 8 + rb) * (CLR_KB * 1024) + j * 1024 + lane * 16, sv);
        BARRIER();
        VM0();
        PIN1(ep);
        if (lane == 0) stc2(ws, CF_OFF + (xc * 8 + rb) * 128, v2u{ep + 1u, ~(ep + 1u)});
        return;
      }
      if (aw >= ntask) return;
      BARRIER();
    } else if (!early) {
      BARRIER();
      if (aw >= ntask) return;
    }
    if (!early) lds_wait_ge(&FL[2], 4u);
    const int toff = (int)(((ix < 16 ? (ix < 8 ? pack_off(0) : pack_off(1)) : (ix < 24 ? pack_off(2) : pack_off(3))) >> (8 * rb)) & 255);
    const u32 b0 = g * 256 + ((n ^ g) << 4);
    const u32 ko0 = b0, ko1 = b0 ^ 64u, ko2 = b0 ^ 128u, ko3 = b0 ^ 192u;
    const u32 ldst = (u32)(uintptr_t)(L + L_K) + aw * 16384;
    const int hh = n >> 2, e2 = n & 3;
    float mrow = -__builtin_inff(), lrow = 0.f;
    f32x4 o1 = {0.f, 0.f, 0.f, 0.f}, o2 = {0.f, 0.f, 0.f, 0.f};
    u64 tI = 0, tK = 0, t31 = 0;
#pragma nounroll
    for (int task = aw; task < ntask; task += 8) {
      const bool first = task < 8, last = task + 8 >= ntask, thr = early && first;
      const u32 key0 = (u32)(xc * 16384 + (toff + task) * 64);
#define THROTTLE() do { if (thr) asm volatile("s_waitcnt vmcnt(%0)" :: "i"(THR_W - 4) : "memory"); else if (THR_L < 32) asm volatile("s_waitcnt vmcnt(%0)" :: "i"(THR_L - 4) : "memory"); SCHED(); } while (0)
      const GAS unsigned char* kb = (const GAS unsigned char*)p.K + (size_t)key0 * 256;
#pragma unroll
      for (int t = 0; t < 4; ++t) {
        u32 keep;
        asm volatile("s_mov_b32 %0, m0\n\ts_mov_b32 m0, %6\n\ts_nop 4\n\t"
                     "global_load_lds_dwordx4 %1, %5" KV_SFX "\n\t"
                     "global_load_lds_dwordx4 %2, %5 offset:1024" KV_SFX "\n\t"
                     "global_load_lds_dwordx4 %3, %5 offset:2048" KV_SFX "\n\t"
                     "global_load_lds_dwordx4 %4, %5 offset:3072" KV_SFX "\n\t"
                     "s_mov_b32 m0, %0"
                     : "=&s"(keep) : "v"(ko0), "v"(ko1), "v"(ko2), "v"(ko3), "s"(kb + t * 4096), "s"(ldst + t * 4096) : "memory");
        if (t == 0) { if (first) t31 = TNOW(); if (thr && !chain) BARRIER(); }
        THROTTLE();
      }
      v4u vv[16];
      const rsrc_t rv = mkr(p.V, 8 * 16384 * 256);
      const int vo = g * 4096 + n * 16;
#pragma unroll
      for (int i = 0; i < 16; ++i) { vv[i] = ld4<KV_AUX>(rv, vo + i * 256, key0 * 256); if ((i & 3) == 3 && i < 15) THROTTLE(); }
      SCHED();
      if (first) lds_inc(&FL[2], lane);
      if (last) lds_inc(&FL[6], lane);
      if (first) tI = TNOW();
      lds_wait_ge(&FL[1], 1u);
      asm volatile("s_waitcnt vmcnt(16)" ::: "memory");
      SCHED();
      if (first) tK = TNOW();
      float s2[4], pe[4];
      {
        const LAS unsigned char* kbase = L + L_K + aw * 16384 + n * 256;
        const LAS unsigned char* qa = L + L_Q + (n & 3) * 320 + g * 16;
        f32x4 sa[4];
#pragma unroll
        for (int t = 0; t < 4; ++t) sa[t] = f32x4{0.f, 0.f, 0.f, 0.f};
#pragma unroll
        for (int s = 0; s < 4; ++s) {
          const v4u a = *(const LAS v4u*)(qa + 64 * s);
#pragma unroll
          for (int t = 0; t < 4; ++t) {
            const v4u b = *(const LAS v4u*)(kbase + t * 4096 + (((4 * s + g) ^ n) << 4));
            sa[t] = __builtin_amdgcn_mfma_f32_16x16x32_bf16(__builtin_bit_cast(bf16x8, a), __builtin_bit_cast(bf16x8, b), sa[t], 0, 0, 0);
          }
        }
        const f32x4 lo = (g & 1) ? sa[1] : sa[0], hi = (g & 1) ? sa[3] : sa[2], sacc = (g & 2) ? hi : lo;
        const u32 a = pk(sacc[0], sacc[1]), b = pk(sacc[2], sacc[3]);
        constexpr float SC = 0.08838834764831845f;
        const u32 c = pk(blo(a) * SC, bhi(a) * SC), d = pk(blo(b) * SC, bhi(b) * SC);
        s2[0] = blo(c); s2[1] = bhi(c); s2[2] = blo(d); s2[3] = bhi(d);
      }
      const float mnew = mx(mrow, row_max(tm16(tm32(s2[0], s2[2]), tm32(s2[1], s2[3]))));
      const float alpha = __builtin_amdgcn_exp2f((mrow - mnew) * LOG2E);
      mrow = mnew;
#pragma unroll
      for (int h2 = 0; h2 < 4; ++h2) pe[h2] = __builtin_amdgcn_exp2f((s2[h2] - rdlf(mrow, 16 * h2)) * LOG2E);
      const u32 w0 = pk(pe[0], pe[1]), w1 = pk(pe[2], pe[3]);
      lrow = fmaf(lrow, alpha, row_sum(tr16(tr32(pe[0], pe[2]), tr32(pe[1], pe[3]))));
      if (last) {
        if (n == 0) *(LAS f32x2*)(L + L_ML + g * 64 + aw * 8) = f32x2{mrow, lrow};
        LGKM0();
        lds_inc(&FL[4], lane);
      }
      {
        const float a01 = (hh & 1) ? rdlf(alpha, 16) : rdlf(alpha, 0), a23 = (hh & 1) ? rdlf(alpha, 48) : rdlf(alpha, 32), ah = (hh & 2) ? a23 : a01;
        o1 *= ah; o2 *= ah;
      }
      const u32 two = (u32)(2 * hh) | ((u32)(2 * hh + 1) << 8);
      const u32 selv = (e2 & 1) ? ((two << 16) | 0x0C0Cu) : (0x0C0C0000u | two);
      const u32 sel0 = (e2 >> 1) == 0 ? selv : 0x0C0C0C0Cu, sel1 = (e2 >> 1) == 1 ? selv : 0x0C0C0C0Cu;
      v2u xb[16];
#define PV_PREP(i) { const u32 b0_ = dppu<0x150 + (i)>(w0), b1_ = dppu<0x150 + (i)>(w1); xb[i] = v2u{__builtin_amdgcn_perm(b1_, b0_, sel0), __builtin_amdgcn_perm(b1_, b0_, sel1)}; }
      PV_PREP(0) PV_PREP(1) PV_PREP(2) PV_PREP(3) PV_PREP(4) PV_PREP(5) PV_PREP(6) PV_PREP(7)
      PV_PREP(8) PV_PREP(9) PV_PREP(10) PV_PREP(11) PV_PREP(12) PV_PREP(13) PV_PREP(14) PV_PREP(15)
#undef PV_PREP
      SCHED();
#pragma unroll
      for (int i = 0; i < 16; ++i) {
        o1 = __builtin_amdgcn_mfma_f32_16x16x16bf16_1k(__builtin_bit_cast(s16x4, v2u{vv[i].x, vv[i].y}), __builtin_bit_cast(s16x4, xb[i]), o1, 0, 0, 0);
        o2 = __builtin_amdgcn_mfma_f32_16x16x16bf16_1k(__builtin_bit_cast(s16x4, v2u{vv[i].z, vv[i].w}), __builtin_bit_cast(s16x4, xb[i]), o2, 0, 0, 0);
      }
      SCHED();
    }
    {
      LAS unsigned char* ob = L + L_K + aw * 16384 + (hh * 128 + e2 * 16 + 4 * g) * 4;
      *(LAS f32x4*)ob = o1; *(LAS f32x4*)(ob + 256) = o2;
    }
    if (aw < 2 || aw > 3) { BARRIER(); return; }
    MKWS();
    const int tau = (aw - 2) * 64 + lane, h = tau >> 5;
    float f[8];
    lds_wait_ge(&FL[5], 1u);
    lds_wait_ge(&FL[4], nact);
    {
      float m[8], l[8];
#pragma unroll
      for (int w = 0; w < 4; ++w) { const f32x4 t = *(const LAS f32x4*)(L + L_ML + h * 64 + w * 16); m[2 * w] = t.x; l[2 * w] = t.y; m[2 * w + 1] = t.z; l[2 * w + 1] = t.w; }
#pragma unroll
      for (int w = 4; w < 8; ++w) { m[w] = w < (int)nact ? m[w] : -__builtin_inff(); l[w] = w < (int)nact ? l[w] : 0.f; }
      const float M = mx(mx(mx(m[0], m[1]), mx(m[2], m[3])), mx(mx(m[4], m[5]), mx(m[6], m[7])));
      float Ls = 0.f;
#pragma unroll
      for (int w = 0; w < 8; ++w) { f[w] = __builtin_amdgcn_exp2f((m[w] - M) * LOG2E); Ls = fmaf(l[w], f[w], Ls); PIN1(f[w]); }
      if ((lane & 31) == 0) stc2(ws, ML_OFF + ((xc * 32 + ix) * 2 + aw - 2) * 128 + (lane >> 5) * 8, v2u{asu(M), asu(Ls)});
    }
    int t1o = T1_OFF + (((xc * 4 + h) * 4 + ((tau >> 3) & 3)) * 32 + ix) * 128 + (tau & 7) * 16;
    u32 pa[8];
#pragma unroll
    for (int w = 0; w < 8; ++w) { pa[w] = (u32)(uintptr_t)(L + L_K) + tau * 16 + (w < (int)nact ? w : 0) * 16384; }
    PIN1(t1o); PIN8(pa);
    BARRIER();
    if (aw == 2) STAMP(chain ? 12 : ix < 16 ? 23 : ix < 23 ? 25 : ix < 27 ? 26 : 28);
    f32x4 part[8];
#pragma unroll
    for (int w = 0; w < 8; ++w) part[w] = *(const LAS f32x4*)(uintptr_t)pa[w];
    const f32x4 acc = ((part[0] * f[0] + part[1] * f[1]) + (part[2] * f[2] + part[3] * f[3])) + ((part[4] * f[4] + part[5] * f[5]) + (part[6] * f[6] + part[7] * f[7]));
    stc(ws, t1o, __builtin_bit_cast(v4u, acc));
    if (aw == 2) STAMP(13);
    return;
  }

  MKWS();
  const bool wcu = ix >= 8 && ix < 16, gcu = ix >= 16 && ix < 23, mcu = ix >= 23 && ix < 27;
  const int g = lane >> 4;
  u32 tag;
  if (chain) {
    const int rho = lane >> 3, kap = lane & 7;
    const rsrc_t rm = mkr(p.wmod, 512 * 3072 * 2), rx = mkr(p.x, 1024), rq = mkr(p.wq, 512 * 4096 * 2);
    const int cg = xc * 2 + (rb >> 2), ks = rb & 3;
    const int r1 = ks * 128 + wave * 32;
    const int dead = wave < 4 ? 0 : 0x40000000;
    const v2u cv = __builtin_amdgcn_raw_buffer_load_b64(mkr(p.c, 1024), rho * 8 + dead, r1 * 2, 0);
    v4u w1[4], w[8];
#pragma unroll
    for (int i = 0; i < 4; ++i) w1[i] = ld4<W_AUX>(rm, rho * 4 * 6144 + kap * 16 + dead, (r1 + i) * 6144 + cg * 128);
    u32 ep = ldc1(ws, EPOCH_OFF);
    SCHED();
    BARRIER();
    __builtin_amdgcn_s_setprio(3);
    const v4u xs = ld4<0>(rx, lane * 16, 0);
    const u32 xo = __builtin_amdgcn_raw_buffer_load_b32(rx, (lane & 31) * 4, wave * 128, 0);
#pragma unroll
    for (int i = 0; i < 8; ++i) w[i] = ld4<W_AUX>(rq, rho * 65536 + kap * 16, (wave * 64 + i) * 8192 + xc * 1024 + rb * 128);
    SCHED();
    const u64 t24 = TNOW();
    u64 t1 = 0;
    PIN1(ep);
    tag = ep + 1u;
    if (wave < 4) {
      u32 s0 = silu2(cv.x), s1 = silu2(cv.y);
      PIN1(s0); PIN1(s1);
      SCHED();
      u32 wp[2][8];
      pairs8(w1[0], w1[1], wp[0]); pairs8(w1[2], w1[3], wp[1]);
      t1 = TNOW();
      float acc[8];
#pragma unroll
      for (int k = 0; k < 8; ++k) acc[k] = dot2(wp[1][k], s1, dot2(wp[0][k], s0, 0.f));
      float z0, z1;
      tr8to2(acc, z0, z1);
      z0 += dpp<ROR8>(z0); z1 += dpp<ROR8>(z1);
      if (!(lane & 8)) *(LAS f32x2*)(RED + 512 + wave * 64 + kap * 8 + g * 2) = f32x2{z0, z1};
      if (lds_ticket(&FL[3], lane) == 3u) {
        const float s = (RED[512 + lane] + RED[576 + lane]) + (RED[640 + lane] + RED[704 + lane]);
        const float s_hi = dpp<SHL1>(s);
        if (!(lane & 1)) stc(ws, H0_OFF + (cg * 4 + ks) * 512 + lane * 8, v4u{tag, asu(s), ~tag, asu(s_hi)});
        STAMP(2);
      }
    }
    SCHED();
    float mb, inv;
    ln_stats(xs, mb, inv);
    u32 y = ln_pair(xo, mb, inv), wp[4][8];
    PIN1(y);
#pragma unroll
    for (int i = 0; i < 4; ++i) pairs8(w[2 * i], w[2 * i + 1], wp[i]);
    SCHED();
    const u64 t27 = TNOW();
    v4u h0, h1, h2, h3;
    const int hb = H0_OFF + (wave + (lane >> 5) * 8) * 2048 + (lane & 31) * 16;
    POLL((h0 = ldc(ws, hb), h1 = ldc(ws, hb + 512), h2 = ldc(ws, hb + 1024), h3 = ldc(ws, hb + 1536)),
         (((h0.x ^ tag) | (h1.x ^ tag) | (h2.x ^ tag) | (h3.x ^ tag)) | ((h0.z ^ ~tag) | (h1.z ^ ~tag) | (h2.z ^ ~tag) | (h3.z ^ ~tag))) == 0u);
    if (wave == 0) { STAMP(3); STAMPV(24, t24); STAMPV(27, t27); STAMPV(1, t1); }
    const u32 md = pk((asf(h0.y) + asf(h1.y)) + (asf(h2.y) + asf(h3.y)), (asf(h0.w) + asf(h1.w)) + (asf(h2.w) + asf(h3.w)));
    auto sw = __builtin_amdgcn_permlane32_swap(md, md, false, false);
    const u32 hp = modulate(y, one_plus(sw[1]), sw[0]);
    float acc[8];
#pragma unroll
    for (int k = 0; k < 8; ++k) acc[k] = 0.f;
    u32 hq[4];
#pragma unroll
    for (int i = 0; i < 4; ++i) hq[i] = __builtin_amdgcn_ds_bpermute(rho * 16 + i * 4, hp);
#pragma unroll
    for (int i = 0; i < 4; ++i)
#pragma unroll
      for (int k = 0; k < 8; ++k) acc[k] = dot2(wp[i][k], hq[i], acc[k]);
    float z0, z1;
    tr8to2(acc, z0, z1);
    z0 += dpp<ROR8>(z0); z1 += dpp<ROR8>(z1);
    if (!(lane & 8)) *(LAS f32x2*)(RED + wave * 64 + kap * 8 + g * 2) = f32x2{z0, z1};
    BARRIER();
    if (wave == 7) {
      const float s = sum8(RED + lane, 64);
      const u32 d = pk(s, dpp<SHL1>(s)), d2 = dppu<SHL2>(d);
      if ((lane & 3) == 0) stc(ws, Q_OFF + (xc * 8 + rb) * 256 + lane * 4, v4u{tag, d, ~tag, d2});
      STAMP(4);
    }
    if (wave != 0) return;
  } else {
    BARRIER();
    if (wave != 0 && !(wcu || gcu || mcu)) return;
    __builtin_amdgcn_s_setprio(3);
    tag = ldc1(ws, EPOCH_OFF) + 1u;
  }
  if (wave == 0) {
    v4u q0, q1; v2u cf;
    const int qb = Q_OFF + xc * 2048 + lane * 16;
    POLL((q0 = ldc(ws, qb), q1 = ldc(ws, qb + 1024)), (((q0.x ^ tag) | (q1.x ^ tag)) | ((q0.z ^ ~tag) | (q1.z ^ ~tag))) == 0u);
    LAS unsigned char* qd = L + L_Q + (lane >> 5) * 320 + (lane & 31) * 8;
    *(LAS v2u*)qd = v2u{q0.y, q0.w};
    *(LAS v2u*)(qd + 640) = v2u{q1.y, q1.w};
    LGKM0();
    if (lane == 0) FL[1] = 1u;
    STAMP(5); STAMPV(0, t0);
    POLL(cf = ldc2(ws, CF_OFF + lane * 128), ((cf.x ^ tag) | (cf.y ^ ~tag)) == 0u);
    if (lane == 0) FL[5] = 1u;
  }
  if (chain) {
    const int col = xc * 64 + rb * 8;
    const u32 xin = *(const GAS u32*)((const GAS u16*)p.x + col + 2 * g);
    BARRIER();
    u32 ga, gb;
    POLL((ga = ldc1(ws, MR_OFF + (col + 2 * g) * 2), gb = ldc1(ws, MR_OFF + 3072 + (col + 2 * g) * 2)), max(ga, gb) != SENT);
    v4u a, b;
    const int tb = lane * 2048 + col * 4;
    POLL((a = ldc(ws, T2_OFF + tb), b = ldc(ws, T2_OFF + tb + 16)), valid4(a) & valid4(b));
    STAMP(16);
    u32 x1;
    {
      const float x8[8] = {asf(a.x), asf(a.y), asf(a.z), asf(a.w), asf(b.x), asf(b.y), asf(b.z), asf(b.w)};
      float z0, z1;
      tr8to2(x8, z0, z1);
      z0 = row_sum(z0); z1 = row_sum(z1);
      const u32 at = pk(z0, z1);
      const u32 pr = pk(blo(ga) * blo(at), bhi(ga) * bhi(at));
      x1 = nosent(pk(blo(xin) + blo(pr), bhi(xin) + bhi(pr)));
      const v4u o = {rdl(x1, 0), rdl(x1, 16), rdl(x1, 32), rdl(x1, 48)};
      if (lane == 0) stc(ws, X1_OFF + (xc * 8 + rb) * 128, o);
    }
    STAMP(17);
    if (bid == 0 && lane == 0) stc1(ws, EPOCH_OFF, tag);
    const int yb = lane < 56 ? Y_OFF + tb : 0x40000000;
    POLL((a = ldc(ws, yb), b = ldc(ws, yb + 16)), valid4(a) & valid4(b));
    STAMP(20);
    {
      const float x8[8] = {asf(a.x), asf(a.y), asf(a.z), asf(a.w), asf(b.x), asf(b.y), asf(b.z), asf(b.w)};
      float z0, z1;
      tr8to2(x8, z0, z1);
      z0 = row_sum(z0); z1 = row_sum(z1);
      const u32 at = pk(z0, z1);
      const u32 pr = pk(blo(gb) * blo(at), bhi(gb) * bhi(at));
      const u32 d = pk(blo(x1) + blo(pr), bhi(x1) + bhi(pr));
      if (!(lane & 15)) *(GAS u32*)((GAS u16*)p.out + col + 2 * g) = d;
    }
    STAMP(21);
  } else if (wcu) {
    const int hl = rb >> 1, hg = xc * 4 + hl, j = (rb & 1) * 4 + (wave >> 1);
    const rsrc_t ro = mkr(p.wo, 4096 * 512 * 2);
    lds_wait_ge(&FL[6], nact);
    v4u w[8];
#pragma unroll
    for (int c = 0; c < 8; ++c) w[c] = ld4<W_AUX>(ro, lane * 16, (hg * 128 + 64 * (wave & 1) + 8 * c + j) * 1024);
    BARRIER();
    u32 wp[4][8];
#pragma unroll
    for (int i = 0; i < 4; ++i) pairs8(w[2 * i], w[2 * i + 1], wp[i]);
    if (wave == 0) STAMP(30);
    SCHED();
    v4u a; v2u ma;
    const int ob = T1_OFF + ((hg * 4 + (j >> 1)) * 32 + (lane >> 1)) * 128 + (j & 1) * 64 + (wave & 1) * 32 + (lane & 1) * 16;
    const int mo = ML_OFF + ((xc * 32 + (lane >> 1)) * 2 + (hl >> 1)) * 128 + (hl & 1) * 8;
    POLL(ma = ldc2(ws, mo), valid2(ma));
    float M = asf(ma.x);
    M = mx(M, dpp<XOR2>(M)); M = mx(M, dpp<ROR4>(M)); M = mx(M, dpp<ROR8>(M)); M = tm16(M, M); M = tm32(M, M);
    float fa = __builtin_amdgcn_exp2f((asf(ma.x) - M) * LOG2E);
    float Ls = fa * asf(ma.y);
    Ls = tr32(Ls, Ls); Ls = tr16(Ls, Ls); Ls += dpp<ROR8>(Ls); Ls += dpp<ROR4>(Ls); Ls += dpp<XOR2>(Ls);
    float inv = __builtin_amdgcn_rcpf(Ls);
    PIN1(fa); PIN1(inv);
    SCHED();
    POLL(a = ldc(ws, ob), valid4(a));
    if (wave == 0) STAMP(14);
    float u0 = tr32(fa * asf(a.x), fa * asf(a.z)), u1 = tr32(fa * asf(a.y), fa * asf(a.w));
    u0 = tr16(u0, u0); u1 = tr16(u1, u1);
    u0 += dpp<ROR8>(u0); u1 += dpp<ROR8>(u1);
    u0 += dpp<ROR4>(u0); u1 += dpp<ROR4>(u1);
    u0 += dpp<XOR2>(u0); u1 += dpp<XOR2>(u1);
    const u32 opk = pk(u0 * inv, u1 * inv);
    float acc[8];
#pragma unroll
    for (int k = 0; k < 8; ++k) acc[k] = 0.f;
#pragma unroll
    for (int i = 0; i < 4; ++i) {
      const u32 op = rdl(opk, (i >> 1) + 32 * (i & 1));
#pragma unroll
      for (int k = 0; k < 8; ++k) acc[k] = dot2(wp[i][k], op, acc[k]);
    }
    *(LAS f32x4*)(RED2 + wave * 512 + lane * 8) = f32x4{acc[0], acc[1], acc[2], acc[3]};
    *(LAS f32x4*)(RED2 + wave * 512 + lane * 8 + 4) = f32x4{acc[4], acc[5], acc[6], acc[7]};
    BARRIER();
    stc1(ws, T2_OFF + (xc * 8 + rb) * 2048 + wave * 256 + lane * 4, asu(sum8(RED2 + wave * 64 + lane, 512)));
    if (wave == 0) STAMP(15);
  } else if (gcu) {
    const int gi = xc * 7 + (ix - 16), rho = lane >> 2, kap = lane & 3;
    const rsrc_t rg = mkr(p.wg, 512 * 1792 * 2), ru = mkr(p.wu, 512 * 1792 * 2), rd = mkr(p.wd, 1792 * 512 * 2);
    lds_wait_ge(&FL[GU_FLAG], GU_CNT);
    v4u w[12];
#pragma unroll
    for (int i = 0; i < 4; ++i) {
      w[i] = ld4<WS_AUX>(rg, rho * 4 * 3584 + kap * 16, (wave * 64 + i) * 3584 + gi * 64);
      w[4 + i] = ld4<WS_AUX>(ru, rho * 4 * 3584 + kap * 16, (wave * 64 + i) * 3584 + gi * 64);
    }
#pragma unroll
    for (int i = 0; i < 4; ++i) w[8 + i] = ld4<W_AUX>(rd, lane * 16, (gi * 32 + wave * 4 + i) * 1024);
    BARRIER();
    v2u sb, cb;
    const int mo = MR_OFF + 1024 + wave * 128 + rho * 8;
    POLL((sb = ldc2(ws, mo), cb = ldc2(ws, mo + 1024)), valid2(sb) & valid2(cb));
    u32 wp[2][16], wd2[2][8];
#pragma unroll
    for (int i = 0; i < 2; ++i) { pairs8(w[2 * i], w[2 * i + 1], wp[i]); pairs8(w[4 + 2 * i], w[4 + 2 * i + 1], wp[i] + 8); pairs8(w[8 + 2 * i], w[9 + 2 * i], wd2[i]); }
    u32 c10 = one_plus(cb.x), c11 = one_plus(cb.y);
    PIN1(c10); PIN1(c11);
    if (wave == 0) STAMP(29);
    SCHED();
    if (wave == 0) {
      v4u xs;
      const int xb1 = X1_OFF + lane * 128;
      POLL(xs = ldc(ws, xb1), valid4(xs));
      STAMP(18);
      *(LAS v4u*)(L + L_X + lane * 16) = xs;
      float mb0, inv0;
      ln_stats(xs, mb0, inv0);
      if (lane == 0) *(LAS f32x2*)(L + L_X + 1024) = f32x2{mb0, inv0};
    }
    BARRIER();
    const v2u xo = *(const LAS v2u*)(L + L_X + wave * 128 + rho * 8);
    const f32x2 st = *(const LAS f32x2*)(L + L_X + 1024);
    const u32 hp0 = modulate(ln_pair(xo.x, st.x, st.y), c10, sb.x), hp1 = modulate(ln_pair(xo.y, st.x, st.y), c11, sb.y);
    float acc[16];
#pragma unroll
    for (int k = 0; k < 16; ++k) acc[k] = dot2(wp[1][k], hp1, dot2(wp[0][k], hp0, 0.f));
    float y[8];
#pragma unroll
    for (int k = 0; k < 8; ++k) y[k] = tr32(acc[k], acc[k + 8]);
    f32x4 z;
#pragma unroll
    for (int k = 0; k < 4; ++k) z[k] = tr16(y[k], y[k + 4]);
#pragma unroll
    for (int k = 0; k < 4; ++k) z[k] += dpp<ROR4>(z[k]);
#pragma unroll
    for (int k = 0; k < 4; ++k) z[k] += dpp<ROR8>(z[k]);
    if (!(lane & 12)) *(LAS f32x4*)(RED + wave * 64 + (lane >> 5) * 32 + kap * 8 + (g & 1) * 4) = z;
    BARRIER();
    float s = RED[(lane >> 3) * 64 + ((lane >> 2) & 1) * 32 + wave * 4 + (lane & 3)];
    s += dpp<ROR8>(s); s = tr16(s, s); s = rbf(tr32(s, s));
    const float gt = s, up = dpp<0x104>(s);
    const float u2 = (-2.f * 0.7978845608028654f * LOG2E) * (gt + 0.044715f * gt * gt * gt);
    const float gv = rbf(gt * __builtin_amdgcn_rcpf(1.f + __builtin_amdgcn_exp2f(u2))) * up;
    const u32 d = pk(gv, dpp<SHL1>(gv));
    const u32 g0 = rdl(d, 0), g1 = rdl(d, 2);
    float ya[8];
#pragma unroll
    for (int k = 0; k < 8; ++k) ya[k] = dot2(wd2[1][k], g1, dot2(wd2[0][k], g0, 0.f));
    *(LAS f32x4*)(RED2 + wave * 512 + lane * 8) = f32x4{ya[0], ya[1], ya[2], ya[3]};
    *(LAS f32x4*)(RED2 + wave * 512 + lane * 8 + 4) = f32x4{ya[4], ya[5], ya[6], ya[7]};
    BARRIER();
    stc1(ws, Y_OFF + gi * 2048 + wave * 256 + lane * 4, asu(sum8(RED2 + wave * 64 + lane, 512)));
    if (wave == 0) STAMP(19);
  } else if (mcu) {
    const int rho = lane >> 3, kap = lane & 7, mi = xc * 4 + (ix - 23);
    const rsrc_t rm = mkr(p.wmod, 512 * 3072 * 2);
    lds_wait_ge(&FL[1], 1u);
    const v4u c0 = *(const GAS v4u*)((const GAS u16*)p.c + wave * 64 + rho * 8);
    v4u w[8];
#pragma unroll
    for (int i = 0; i < 8; ++i) w[i] = ld4<W_AUX>(rm, rho * 8 * 6144 + kap * 16, (wave * 64 + i) * 6144 + 2048 + mi * 128);
    float acc[8];
#pragma unroll
    for (int k = 0; k < 8; ++k) acc[k] = 0.f;
#pragma unroll
    for (int i = 0; i < 4; ++i) {
      const u32 sp = silu2(c0[i]);
      u32 wp[8];
      pairs8(w[2 * i], w[2 * i + 1], wp);
#pragma unroll
      for (int k = 0; k < 8; ++k) acc[k] = dot2(wp[k], sp, acc[k]);
    }
    float z0, z1;
    tr8to2(acc, z0, z1);
    z0 += dpp<ROR8>(z0); z1 += dpp<ROR8>(z1);
    if (!(lane & 8)) *(LAS f32x2*)(RED + wave * 64 + kap * 8 + g * 2) = f32x2{z0, z1};
    if (lds_ticket(&FL[3], lane) == 7u) {
      const float s = sum8(RED + lane, 64);
      const u32 d = nosent(pk(s, dpp<SHL1>(s)));
      lds_wait_ge(&FL[5], 1u);
      if (!(lane & 1)) stc1(ws, MR_OFF + mi * 128 + lane * 2, d);
      STAMP(22);
    }
  }
}
}

extern "C" void kernel_launch(void* const* d_in, const int* in_sizes, int n_in, void* d_out, int out_size, void* d_ws, size_t ws_size, hipStream_t stream) {
  Params p;
  p.x = (const u16*)d_in[0]; p.K = (const u16*)d_in[1]; p.V = (const u16*)d_in[2]; p.wq = (const u16*)d_in[3]; p.wo = (const u16*)d_in[4];
  p.wg = (const u16*)d_in[6]; p.wu = (const u16*)d_in[7]; p.wd = (const u16*)d_in[8]; p.c = (const u16*)d_in[9]; p.wmod = (const u16*)d_in[10];
  p.out = (u16*)d_out; p.ws = d_ws;
  hipLaunchKernelGGL(fused_kernel, dim3(NWG), dim3(NTHR), 0, stream, p);
}
```

```cpp
#include <hip/hip_runtime.h>
#include <stdint.h>

namespace {
typedef unsigned short u16;
typedef unsigned u32;
typedef unsigned long long u64;
typedef u32 v4u __attribute__((ext_vector_type(4)));
typedef u32 v2u __attribute__((ext_vector_type(2)));
typedef u32 v8u __attribute__((ext_vector_type(8)));
typedef u32 v16u __attribute__((ext_vector_type(16)));
typedef float f32x4 __attribute__((ext_vector_type(4)));
typedef float f32x2 __attribute__((ext_vector_type(2)));
typedef __bf16 bf16x2 __attribute__((ext_vector_type(2)));
typedef __bf16 bf16x8 __attribute__((ext_vector_type(8)));
typedef short s16x4 __attribute__((ext_vector_type(4)));
#define GAS __attribute__((address_space(1)))
#define LAS __attribute__((address_space(3)))

#define KV_AUX 2
#define KV_SFX " nt"
#define W_AUX 2
#define THR_W 8
#define THR_Q 6
#define CH_EARLY 1
#define CODE_KB 19
#define QPOLL 1
#define XP 4
#define QSTAG 8
#define N_G 8
#define N_M 8

constexpr int NWG = 256, NTHR = 1024;
constexpr u32 SENT = 0xFFFFFFFFu;
constexpr int SC1 = 16;
constexpr u32 SPIN_CAP = 1u << 17;
constexpr float LOG2E = 1.4426950408889634f;
constexpr int EPOCH_OFF = 0;
constexpr int CF_OFF = 8192;
constexpr int Q_OFF = 16384;
constexpr int H0_OFF = 32768;
constexpr int S_OFF = 65536;
constexpr int T1_OFF = S_OFF;
constexpr int ML_OFF = T1_OFF + 524288;
constexpr int T2_OFF = ML_OFF + 65536;
constexpr int Y_OFF = T2_OFF + 262144;
constexpr int X1_OFF = Y_OFF + 114688;
constexpr int MR_OFF = X1_OFF + 8192;
constexpr int CLR_KB = 16;
constexpr int S_END = S_OFF + 64 * CLR_KB * 1024;
static_assert(MR_OFF + 4096 <= S_END, "");
constexpr int WS_BYTES = S_END;
constexpr int L_K = 0, L_TB = 8 * 16384, L_Q = 9 * 16384, L_FLAG = L_Q + 1280, L_ML = L_FLAG + 64, L_X = L_ML + 512, L_RED = L_X + 1024, L_END = L_RED + 4096;
static_assert(L_END <= 163840 && L_RED % 16 == 0 && L_TB % 16 == 0, "");
constexpr int CH_ALL = 256 - 7 * N_G - 4 * N_M - 13 * 9, N_CH = CH_ALL / 8, CH_HI = CH_ALL - 8 * N_CH;
constexpr int ntask_of(int ix) { return ix < 8 ? (ix < CH_HI ? N_CH + 1 : N_CH) : ix < 16 ? 9 : ix < 23 ? N_G : ix < 27 ? N_M : 9; }
constexpr int toff_of(int ix) { int o = 0; for (int i = 0; i < ix; ++i) o += ntask_of(i); return o; }
static_assert(toff_of(32) == 256 && N_G >= 4 && N_G <= 8 && N_CH >= 4 && N_CH + 1 <= 8 && N_M >= 4 && N_M <= 8, "");

#define STAMP(k) do {} while (0)
#define TNOW() 0ull
#define STAMPV(k, v) do { (void)(v); } while (0)

struct Ptrs {
  const u16 *x, *K, *V, *wq, *wo, *wg, *wu, *wd, *c, *wmod;
  u16* out;
  void* ws;
};
struct Params { Ptrs p; u32 tab[32]; };
static_assert(sizeof(Params) == 96 + 128 && L_K == 0, "");

__device__ __forceinline__ float asf(u32 x) { return __builtin_bit_cast(float, x); }
__device__ __forceinline__ u32 asu(float x) { return __builtin_bit_cast(u32, x); }
__device__ __forceinline__ float blo(u32 w) { return asf(w << 16); }
__device__ __forceinline__ float bhi(u32 w) { return asf(w & 0xffff0000u); }
__device__ __forceinline__ u32 pk2(f32x2 v) { return __builtin_bit_cast(u32, __builtin_convertvector(v, bf16x2)); }
__device__ __forceinline__ u32 pk(float lo, float hi) { return pk2(f32x2{lo, hi}); }
__device__ __forceinline__ f32x2 up2(u32 w) { return f32x2{blo(w), bhi(w)}; }
__device__ __forceinline__ float rbf(float f) { return (float)(__bf16)f; }
__device__ __forceinline__ float dot2(u32 a, u32 b, float c) { return __builtin_amdgcn_fdot2_f32_bf16(__builtin_bit_cast(bf16x2, a), __builtin_bit_cast(bf16x2, b), c, false); }
template <int CTRL> __device__ __forceinline__ float dpp(float x) { return asf(__builtin_amdgcn_mov_dpp(asu(x), CTRL, 0xf, 0xf, true)); }
template <int CTRL> __device__ __forceinline__ u32 dppu(u32 x) { return __builtin_amdgcn_mov_dpp(x, CTRL, 0xf, 0xf, true); }
constexpr int XOR1 = 0xB1, XOR2 = 0x4E, XOR7 = 0x141, ROR8 = 0x128, ROR4 = 0x124, SHL1 = 0x101, SHL2 = 0x102, BC = 0x150;
__device__ __forceinline__ float mx(float a, float b) { return __builtin_amdgcn_fmed3f(a, b, __builtin_inff()); }
__device__ __forceinline__ float tr32(float a, float b) { auto s = __builtin_amdgcn_permlane32_swap(asu(a), asu(b), false, false); return asf(s[0]) + asf(s[1]); }
__device__ __forceinline__ float tr16(float a, float b) { auto s = __builtin_amdgcn_permlane16_swap(asu(a), asu(b), false, false); return asf(s[0]) + asf(s[1]); }
__device__ __forceinline__ float tm32(float a, float b) { auto s = __builtin_amdgcn_permlane32_swap(asu(a), asu(b), false, false); return mx(asf(s[0]), asf(s[1])); }
__device__ __forceinline__ float tm16(float a, float b) { auto s = __builtin_amdgcn_permlane16_swap(asu(a), asu(b), false, false); return mx(asf(s[0]), asf(s[1])); }
__device__ __forceinline__ float row_sum(float v) { v += dpp<ROR8>(v); v += dpp<XOR7>(v); v += dpp<XOR2>(v); v += dpp<XOR1>(v); return v; }
__device__ __forceinline__ float row_max(float v) { v = mx(v, dpp<ROR8>(v)); v = mx(v, dpp<XOR7>(v)); v = mx(v, dpp<XOR2>(v)); v = mx(v, dpp<XOR1>(v)); return v; }
__device__ __forceinline__ f32x2 sum8x64(v4u a, v4u b) {
  const float y0 = tr32(asf(a.x), asf(b.x)), y1 = tr32(asf(a.y), asf(b.y)), y2 = tr32(asf(a.z), asf(b.z)), y3 = tr32(asf(a.w), asf(b.w));
  return f32x2{row_sum(tr16(y0, y2)), row_sum(tr16(y1, y3))};
}
__device__ __forceinline__ u32 rdl(u32 v, int l) { return __builtin_amdgcn_readlane(v, l); }
__device__ __forceinline__ float rdlf(float v, int l) { return asf(__builtin_amdgcn_readlane(asu(v), l)); }
__device__ __forceinline__ u32 nosent(u32 w) { return w < 0xFFFFFFFEu ? w : 0xFFFFFFFEu; }
typedef __amdgpu_buffer_rsrc_t rsrc_t;
__device__ __forceinline__ rsrc_t mkr(const void* q, int bytes) { return __builtin_amdgcn_make_buffer_rsrc((void*)q, 0, bytes, 0x00020000); }
template <int AUX> __device__ __forceinline__ v4u ld4(rsrc_t r, int off, int so) { return __builtin_amdgcn_raw_buffer_load_b128(r, off, so, AUX); }
__device__ __forceinline__ u32 ld1(rsrc_t r, int off, int so) { return __builtin_amdgcn_raw_buffer_load_b32(r, off, so, 0); }
__device__ __forceinline__ v4u ldc(rsrc_t r, int off) { return __builtin_amdgcn_raw_buffer_load_b128(r, off, 0, SC1); }
__device__ __forceinline__ u32 ldc1(rsrc_t r, int off) { return __builtin_amdgcn_raw_buffer_load_b32(r, off, 0, SC1); }
__device__ __forceinline__ void stc(rsrc_t r, int off, v4u v) { __builtin_amdgcn_raw_buffer_store_b128(v, r, off, 0, SC1); }
__device__ __forceinline__ void stc2(rsrc_t r, int off, v2u v) { __builtin_amdgcn_raw_buffer_store_b64(v, r, off, 0, SC1); }
__device__ __forceinline__ void stc_row(rsrc_t r, int off, float v, int lane) {
  const v4u o = {asu(v), asu(dpp<0x55>(v)), asu(dpp<0xAA>(v)), asu(dpp<0xFF>(v))};
  if (!(lane & 3)) stc(r, off, o);
}
__device__ __forceinline__ void stc1(rsrc_t r, int off, u32 v) { __builtin_amdgcn_raw_buffer_store_b32(v, r, off, 0, SC1); }
#define MEMFENCE() asm volatile("" ::: "memory")
#define LGKM0() asm volatile("s_waitcnt lgkmcnt(0)" ::: "memory")
#define VM0() asm volatile("s_waitcnt vmcnt(0)" ::: "memory")
#define SCHED() __builtin_amdgcn_sched_barrier(0)
#define BARRIER() do { LGKM0(); SCHED(); __builtin_amdgcn_s_barrier(); MEMFENCE(); SCHED(); } while (0)
#define PIN1(a) asm volatile("" : "+v"(a))
#define PIN4(a) asm volatile("" : "+v"((a)[0]), "+v"((a)[1]), "+v"((a)[2]), "+v"((a)[3]))
#define PIN8(a) asm volatile("" : "+v"((a)[0]), "+v"((a)[1]), "+v"((a)[2]), "+v"((a)[3]), "+v"((a)[4]), "+v"((a)[5]), "+v"((a)[6]), "+v"((a)[7]))

#define POLL_HEAD "s_mov_b32 %[n], %[cap]\n\t1:\n\t"
#define POLL_TAIL "s_cbranch_execz 2f\n\ts_sub_u32 %[n], %[n], 1\n\ts_cbranch_scc0 1b\n\t2:\n\ts_mov_b64 exec, -1\n\ts_nop 2"
__device__ __forceinline__ v4u poll4(rsrc_t r, int off) {
  v4u a; u32 n;
  asm volatile(POLL_HEAD "buffer_load_dwordx4 v[124:127], %[o], %[r], 0 offen sc1\n\ts_waitcnt vmcnt(0)\n\t"
               "v_max_u32 v123, v124, v125\n\tv_max3_u32 v123, v123, v126, v127\n\tv_cmpx_eq_u32 vcc, -1, v123\n\t" POLL_TAIL
               : "=&{v[124:127]}"(a), [n] "=&s"(n) : [o] "v"(off), [r] "s"(r), [cap] "i"(SPIN_CAP) : "memory", "vcc", "scc", "v123");
  return a;
}
template <int D> __device__ __forceinline__ void poll4x2(rsrc_t r, int off, v4u& a, v4u& b) {
  u32 n;
  asm volatile(POLL_HEAD "buffer_load_dwordx4 v[120:123], %[o], %[r], 0 offen sc1\n\tbuffer_load_dwordx4 v[124:127], %[o], %[r], 0 offen offset:%[d] sc1\n\ts_waitcnt vmcnt(1)\n\t"
               "v_max_u32 v119, v120, v121\n\tv_max3_u32 v119, v119, v122, v123\n\ts_waitcnt vmcnt(0)\n\tv_max3_u32 v119, v119, v124, v125\n\tv_max3_u32 v119, v119, v126, v127\n\t"
               "v_cmpx_eq_u32 vcc, -1, v119\n\t" POLL_TAIL
               : "=&{v[120:123]}"(a), "=&{v[124:127]}"(b), [n] "=&s"(n) : [o] "v"(off), [r] "s"(r), [cap] "i"(SPIN_CAP), [d] "i"(D) : "memory", "vcc", "scc", "v119");
}
__device__ __forceinline__ void poll4x4(rsrc_t r, int off, int off2, v4u& a, v4u& b, v4u& c, v4u& d) {
  u32 n;
  asm volatile(POLL_HEAD "buffer_load_dwordx4 v[112:115], %[o], %[r], 0 offen sc1\n\tbuffer_load_dwordx4 v[116:119], %[o], %[r], 0 offen offset:16 sc1\n\t"
               "buffer_load_dwordx4 v[120:123], %[o2], %[r], 0 offen sc1\n\tbuffer_load_dwordx4 v[124:127], %[o2], %[r], 0 offen offset:16 sc1\n\t"
               "s_waitcnt vmcnt(3)\n\tv_max_u32 v110, v112, v113\n\tv_max3_u32 v110, v110, v114, v115\n\t"
               "s_waitcnt vmcnt(2)\n\tv_max_u32 v111, v116, v117\n\tv_max3_u32 v111, v111, v118, v119\n\t"
               "s_waitcnt vmcnt(1)\n\tv_max3_u32 v110, v110, v120, v121\n\tv_max3_u32 v111, v111, v122, v123\n\t"
               "s_waitcnt vmcnt(0)\n\tv_max3_u32 v110, v110, v124, v125\n\tv_max3_u32 v111, v111, v126, v127\n\t"
               "v_max_u32 v110, v110, v111\n\tv_cmpx_eq_u32 vcc, -1, v110\n\t" POLL_TAIL
               : "=&{v[112:115]}"(a), "=&{v[116:119]}"(b), "=&{v[120:123]}"(c), "=&{v[124:127]}"(d), [n] "=&s"(n)
               : [o] "v"(off), [o2] "v"(off2), [r] "s"(r), [cap] "i"(SPIN_CAP) : "memory", "vcc", "scc", "v110", "v111");
}
__device__ __forceinline__ v2u poll2(rsrc_t r, int off) {
  v2u a; u32 n;
  asm volatile(POLL_HEAD "buffer_load_dwordx2 v[126:127], %[o], %[r], 0 offen sc1\n\ts_waitcnt vmcnt(0)\n\t"
               "v_max_u32 v125, v126, v127\n\tv_cmpx_eq_u32 vcc, -1, v125\n\ts_cbranch_execz 2f\n\ts_sleep 1\n\ts_sub_u32 %[n], %[n], 1\n\ts_cbranch_scc0 1b\n\t2:\n\ts_mov_b64 exec, -1\n\ts_nop 2"
               : "=&{v[126:127]}"(a), [n] "=&s"(n) : [o] "v"(off), [r] "s"(r), [cap] "i"(SPIN_CAP) : "memory", "vcc", "scc", "v125");
  return a;
}
template <int D> __device__ __forceinline__ void poll1x2(rsrc_t r, int off, u32& a, u32& b) {
  u32 n;
  asm volatile(POLL_HEAD "buffer_load_dword v126, %[o], %[r], 0 offen sc1\n\tbuffer_load_dword v127, %[o], %[r], 0 offen offset:%[d] sc1\n\ts_waitcnt vmcnt(0)\n\t"
               "v_max_u32 v125, v126, v127\n\tv_cmpx_eq_u32 vcc, -1, v125\n\t" POLL_TAIL
               : "=&{v126}"(a), "=&{v127}"(b), [n] "=&s"(n) : [o] "v"(off), [r] "s"(r), [cap] "i"(SPIN_CAP), [d] "i"(D) : "memory", "vcc", "scc", "v125");
}
__device__ __forceinline__ void pollT4(rsrc_t r, int off, u32 tag, u32 ntag, v4u& a, v4u& b, v4u& c, v4u& d) {
  u32 n;
  asm volatile(POLL_HEAD "buffer_load_dwordx4 v[112:115], %[o], %[r], 0 offen sc1\n\tbuffer_load_dwordx4 v[116:119], %[o], %[r], 0 offen offset:512 sc1\n\t"
               "buffer_load_dwordx4 v[120:123], %[o], %[r], 0 offen offset:1024 sc1\n\tbuffer_load_dwordx4 v[124:127], %[o], %[r], 0 offen offset:1536 sc1\n\t"
               "s_waitcnt vmcnt(3)\n\tv_xor_b32 v110, v112, %[t]\n\tv_xor_b32 v111, v114, %[u]\n\t"
               "s_waitcnt vmcnt(2)\n\tv_bitop3_b32 v110, v116, %[t], v110 bitop3:0xbe\n\tv_bitop3_b32 v111, v118, %[u], v111 bitop3:0xbe\n\t"
               "s_waitcnt vmcnt(1)\n\tv_bitop3_b32 v110, v120, %[t], v110 bitop3:0xbe\n\tv_bitop3_b32 v111, v122, %[u], v111 bitop3:0xbe\n\t"
               "s_waitcnt vmcnt(0)\n\tv_bitop3_b32 v110, v124, %[t], v110 bitop3:0xbe\n\tv_bitop3_b32 v111, v126, %[u], v111 bitop3:0xbe\n\t"
               "v_or_b32 v110, v110, v111\n\tv_cmpx_ne_u32 vcc, 0, v110\n\t" POLL_TAIL
               : "=&{v[112:115]}"(a), "=&{v[116:119]}"(b), "=&{v[120:123]}"(c), "=&{v[124:127]}"(d), [n] "=&s"(n)
               : [o] "v"(off), [r] "s"(r), [cap] "i"(SPIN_CAP), [t] "v"(tag), [u] "v"(ntag) : "memory", "vcc", "scc", "v110", "v111");
}
__device__ __forceinline__ void lds_wait_ge(volatile LAS u32* f, u32 want) {
  _Pragma("nounroll") for (u32 s = 0; s < (SPIN_CAP << 3); ++s) { if (*f >= want) break; __builtin_amdgcn_s_sleep(1); }
  MEMFENCE();
}
__device__ __forceinline__ void lds_inc(volatile LAS u32* f, int lane) {
  asm volatile("s_mov_b64 exec, 1\n\tds_add_u32 %0, %1\n\ts_mov_b64 exec, -1" :: "v"((u32)(uintptr_t)f), "v"(1u) : "memory");
}
__device__ __forceinline__ u32 lds_ticket(volatile LAS u32* f, int lane) {
  u32 old;
  asm volatile("s_mov_b64 exec, 1\n\tds_add_rtn_u32 %0, %1, %2\n\ts_mov_b64 exec, -1\n\ts_waitcnt lgkmcnt(0)" : "=&v"(old) : "v"((u32)(uintptr_t)f), "v"(1u) : "memory");
  return __builtin_amdgcn_readfirstlane(old);
}
__device__ __forceinline__ float sum8(const LAS float* r, int stride) {
  float v[8];
#pragma unroll
  for (int i = 0; i < 8; ++i) v[i] = r[i * stride];
  return ((v[0] + v[1]) + (v[2] + v[3])) + ((v[4] + v[5]) + (v[6] + v[7]));
}
__device__ __forceinline__ float silu(float v) { return v / (1.f + __expf(-v)); }
__device__ __forceinline__ u32 silu2(u32 c) { return pk(silu(blo(c)), silu(bhi(c))); }
__device__ __forceinline__ void ln_stats(v4u xv, float& mb, float& inv) {
  constexpr u32 ONE2 = 0x3f803f80u;
  const float s = dot2(xv[0], ONE2, dot2(xv[1], ONE2, 0.f)) + dot2(xv[2], ONE2, dot2(xv[3], ONE2, 0.f));
  const float q = dot2(xv[0], xv[0], dot2(xv[1], xv[1], 0.f)) + dot2(xv[2], xv[2], dot2(xv[3], xv[3], 0.f));
  float t = row_sum(tr32(s, q));
  t = tr16(t, t);
  const float mean = rdlf(t, 0) * (1.f / 512.f), sq = rdlf(t, 32) * (1.f / 512.f);
  float var = fmaf(-mean, mean, sq);
  if (__builtin_expect(!(var * 64.f >= sq), 0)) {
    float d = 0.f;
#pragma unroll
    for (int i = 0; i < 4; ++i) { const float a = blo(xv[i]) - mean, b = bhi(xv[i]) - mean; d = fmaf(a, a, fmaf(b, b, d)); }
    d = row_sum(tr32(d, d));
    var = tr16(d, d) * (1.f / 512.f);
  }
  mb = rbf(mean);
  inv = __builtin_amdgcn_rcpf(rbf(__builtin_amdgcn_sqrtf(rbf(rbf(var) + 1e-5f))));
}
__device__ __forceinline__ u32 ln_pair(u32 x, float mb, float inv) { return pk2(up2(pk2(up2(x) - mb)) * inv); }
__device__ __forceinline__ f32x2 one_plus(u32 c) { return up2(pk2(up2(c) + 1.f)); }
__device__ __forceinline__ u32 modulate(u32 y, f32x2 c1, f32x2 sh) { return pk2(up2(pk2(up2(y) * c1)) + sh); }
__device__ __forceinline__ int tile_voff(int lane, int stride) { const int rho = lane >> 3; return (8 * (rho >> 2) + (rho & 3)) * stride + (lane & 7) * 16; }
template <int AUX> __device__ __forceinline__ void tile_load(rsrc_t r, int voff, int soff, int stride, v4u* w) {
#pragma unroll
  for (int i = 0; i < 4; ++i) w[i] = ld4<AUX>(r, voff, soff + (16 * (i & 1) + 4 * (i >> 1)) * stride);
}
__device__ __forceinline__ void tile_frags(LAS unsigned char* tb, int lane, const v4u* w, v4u* f) {
  const int rho = lane >> 3;
  LAS unsigned char* wr = tb + rho * 128 + (((lane & 7) ^ (rho & 6)) << 4);
  const int row = 4 * (lane >> 4) + ((lane >> 2) & 3);
  const LAS unsigned char* rd = tb + row * 128 + (lane & 3) * 8;
  const int sw = (row >> 1) & 3;
  v2u h[2][4];
#pragma unroll
  for (int t = 0; t < 2; ++t) {
    *(LAS v4u*)wr = w[2 * t]; *(LAS v4u*)(wr + 1024) = w[2 * t + 1];
    MEMFENCE();
#pragma unroll
    for (int ct = 0; ct < 4; ++ct) h[t][ct] = __builtin_bit_cast(v2u, __builtin_amdgcn_ds_read_tr16_b64_v4i16((LAS s16x4*)(rd + ((ct ^ sw) << 5))));
    MEMFENCE();
  }
#pragma unroll
  for (int ct = 0; ct < 4; ++ct) f[ct] = v4u{h[0][ct].x, h[0][ct].y, h[1][ct].x, h[1][ct].y};
  PIN4(f);
}
__device__ __forceinline__ void lds_frags(const LAS unsigned char* tile, int lane, v4u* f) {
  const int row = 4 * (lane >> 4) + ((lane >> 2) & 3);
  const LAS unsigned char* rd = tile + row * 128 + (lane & 3) * 8;
  const int sw = (row >> 1) & 3;
#pragma unroll
  for (int ct = 0; ct < 4; ++ct) {
    const v2u lo = __builtin_bit_cast(v2u, __builtin_amdgcn_ds_read_tr16_b64_v4i16((LAS s16x4*)(rd + ((ct ^ sw) << 5))));
    const v2u hi = __builtin_bit_cast(v2u, __builtin_amdgcn_ds_read_tr16_b64_v4i16((LAS s16x4*)(rd + 2048 + ((ct ^ sw) << 5))));
    f[ct] = v4u{lo.x, lo.y, hi.x, hi.y};
  }
  PIN4(f);
}
__device__ __forceinline__ f32x4 mfma32(v4u a, v4u b, f32x4 c) { return __builtin_amdgcn_mfma_f32_16x16x32_bf16(__builtin_bit_cast(bf16x8, a), __builtin_bit_cast(bf16x8, b), c, 0, 0, 0); }
template <int NS> __device__ __forceinline__ float gemv(const v4u* a, const v4u* f, int g) {
  f32x4 acc[4];
#pragma unroll
  for (int ct = 0; ct < 4; ++ct) acc[ct] = mfma32(a[0], f[ct], f32x4{0.f, 0.f, 0.f, 0.f});
#pragma unroll
  for (int s = 1; s < NS; ++s)
#pragma unroll
    for (int ct = 0; ct < 4; ++ct) acc[ct] = mfma32(a[s], f[4 * s + ct], acc[ct]);
  const float lo = (g & 1) ? acc[1][0] : acc[0][0], hi = (g & 1) ? acc[3][0] : acc[2][0];
  return (g & 2) ? hi : lo;
}
template <int B> __device__ __forceinline__ v4u bcast4(u32 v) { return v4u{dppu<BC + B>(v), dppu<BC + B + 1>(v), dppu<BC + B + 2>(v), dppu<BC + B + 3>(v)}; }

__global__ void __launch_bounds__(NTHR, 4) fused_kernel(Params) {
  __shared__ __attribute__((aligned(128))) unsigned char lds_raw[L_END];
  LAS unsigned char* L = (LAS unsigned char*)lds_raw;
  v16u ka; v8u kb8;
  int wave; u32 tab, ko0, ko1, ko2, ko3, ep, d1; v4u xs;
  const u64 t0 = TNOW();
  {
    u32 sa, sb, sc, keep, va, vb; u64 pa, pc;
#define DMA4(P) "global_load_lds_dwordx4 %[k0], " P KV_SFX "\n\tglobal_load_lds_dwordx4 %[k1], " P " offset:1024" KV_SFX "\n\t" \
                "global_load_lds_dwordx4 %[k2], " P " offset:2048" KV_SFX "\n\tglobal_load_lds_dwordx4 %[k3], " P " offset:3072" KV_SFX "\n\t"
    asm volatile("s_getpc_b64 s[48:49]\n\ts_load_dwordx16 s[16:31], %[kp], 0x0\n\ts_load_dwordx8 s[36:43], %[kp], 0x40\n\t"
                 "s_lshr_b32 s46, %[bid], 3\n\ts_lshl_b32 s46, s46, 2\n\ts_add_u32 s46, s46, 0x60\n\ts_load_dword s46, %[kp], s46"
                 : "=&{s[16:31]}"(ka), "=&{s[36:43]}"(kb8), "=&{s46}"(tab), "=&{s[48:49]}"(pc) : [kp] "s"(__builtin_amdgcn_kernarg_segment_ptr()), [bid] "s"(blockIdx.x) : "scc");
    asm volatile(
      "s_lshr_b32 %[sa], %[bid], 3\n\t"
      "v_readfirstlane_b32 %[w], %[tid]\n\ts_mov_b32 %[keep], m0\n\ts_and_b32 %[sc], %[bid], 7\n\t"
      "s_lshr_b32 %[w], %[w], 6\n\ts_cmp_lt_u32 %[w], 8\n\ts_cbranch_scc1 1f\n\t"
      "v_bfe_u32 %[va], %[tid], 4, 2\n\tv_and_b32 %[vb], 15, %[tid]\n\tv_xor_b32 %[vb], %[vb], %[va]\n\tv_lshlrev_b32 %[vb], 4, %[vb]\n\t"
      "v_lshl_or_b32 %[k0], %[va], 8, %[vb]\n\tv_xor_b32 %[k1], 64, %[k0]\n\tv_xor_b32 %[k2], 0x80, %[k0]\n\tv_xor_b32 %[k3], 0xc0, %[k0]\n\t"
      "s_sub_u32 %[sb], %[w], 8\n\ts_lshl_b32 m0, %[sb], 14\n\ts_lshl_b32 %[sc], %[sc], 8\n\ts_add_u32 %[sb], %[sb], %[sc]\n\t"
      "s_cmp_gt_u32 %[w], 11\n\ts_cselect_b32 %[sa], 0, %[sa]\n\ts_cmp_lt_u32 %[sa], 8\n\t"
      "s_waitcnt lgkmcnt(0)\n\ts_cbranch_scc1 9f\n\t"
      "s_and_b32 %[sc], s46, 0xff\n\ts_add_u32 %[sb], %[sb], %[sc]\n\ts_lshl_b32 %[sb], %[sb], 14\n\t"
      "s_add_u32 s44, s18, %[sb]\n\ts_addc_u32 s45, s19, 0\n\t" DMA4("s[44:45]")
      "s_branch 9f\n\t"
      "1:\n\tv_bfe_u32 %[va], %[tid], 3, 3\n\tv_and_b32 %[vb], 6, %[va]\n\tv_xor_b32 %[vb], %[vb], %[tid]\n\tv_and_b32 %[vb], 7, %[vb]\n\tv_lshlrev_b32 %[vb], 4, %[vb]\n\t"
      "v_and_b32 %[k1], 4, %[va]\n\tv_add_u32 %[va], %[va], %[k1]\n\t"
      "v_and_b32 v106, 63, %[tid]\n\tv_lshlrev_b32 v106, 4, v106\n\tv_mov_b32 v104, 0\n\t"
      "s_cmp_eq_u32 %[w], 3\n\ts_cbranch_scc0 4f\n\t"
      "s_and_b32 s44, s48, 0xffffff00\n\ts_and_b32 %[sb], %[sa], 7\n\ts_lshl_b32 s45, %[sb], 10\n\ts_add_u32 s44, s44, s45\n\ts_addc_u32 s45, s49, 0\n\t"
      "global_load_dwordx4 v[110:113], v106, s[44:45]\n\ts_add_u32 s44, s44, 0x2000\n\ts_addc_u32 s45, s45, 0\n\t"
      "global_load_dwordx4 v[114:117], v106, s[44:45]\n\ts_add_u32 s44, s44, 0x2000\n\ts_addc_u32 s45, s45, 0\n\t"
      "s_cmp_lt_u32 %[sb], %[ck]\n\ts_cbranch_scc0 4f\n\tglobal_load_dwordx4 v[118:121], v106, s[44:45]\n\t4:\n\t"
      "v_and_b32 v105, 3, %[tid]\n\tv_lshlrev_b32 v105, 2, v105\n\tv_and_or_b32 v105, %[tid], 48, v105\n\t"
      "s_cmp_lt_u32 %[sa], 8\n\ts_cbranch_scc0 2f\n\t"
      "v_lshl_or_b32 %[k0], %[va], 13, %[vb]\n\tv_add_u32 %[k1], 0x1fc00, %[k0]\n\tv_add_u32 %[k2], 0x7800, %[k0]\n\tv_add_u32 %[k3], 0x27400, %[k0]\n\t"
      "v_bfe_u32 %[va], %[tid], 2, 1\n\tv_lshl_or_b32 v105, %[va], 6, v105\n\ts_lshl_b32 %[sb], %[w], 7\n\tv_add_u32 v105, %[sb], v105\n\t"
      "s_lshl_b32 m0, %[w], 13\n\ts_lshl_b32 %[sb], %[w], 9\n\ts_add_u32 %[sb], %[sb], %[sc]\n\ts_lshl_b32 %[sb], %[sb], 3\n\ts_add_u32 %[sb], %[sb], %[sa]\n\ts_lshl_b32 %[sb], %[sb], 7\n\t"
      "s_waitcnt lgkmcnt(0)\n\t"
      "global_load_dword v105, v105, s[16:17]\n\tglobal_load_dwordx4 v[106:109], v106, s[16:17]\n\tglobal_load_dword v104, v104, s[42:43] sc1\n\t"
      "s_add_u32 s44, s22, %[sb]\n\ts_addc_u32 s45, s23, 0\n\t" DMA4("s[44:45]")
      "s_add_u32 s44, s44, 0x40000\n\ts_addc_u32 s45, s45, 0\n\ts_add_u32 m0, m0, 0x1000\n\ts_nop 0\n\t" DMA4("s[44:45]")
      "s_branch 9f\n\t"
      "2:\n\tv_mul_u32_u24 %[k0], 0x1800, %[va]\n\tv_add_u32 %[k0], %[k0], %[vb]\n\tv_add_u32 %[k1], 0x17c00, %[k0]\n\tv_add_u32 %[k2], 0x5800, %[k0]\n\tv_add_u32 %[k3], 0x1d400, %[k0]\n\t"
      "s_sub_u32 %[sb], %[sa], 23\n\ts_cmp_lt_u32 %[sb], 4\n\ts_cbranch_scc0 3f\n\t"
      "v_bfe_u32 %[va], %[tid], 2, 1\n\tv_lshl_or_b32 v105, %[va], 6, v105\n\ts_lshl_b32 s44, %[w], 7\n\tv_add_u32 v105, s44, v105\n\t"
      "s_lshl_b32 s44, %[w], 13\n\ts_add_u32 m0, s44, 0x10000\n\ts_lshl_b32 %[sc], %[sc], 2\n\ts_add_u32 %[sb], %[sb], %[sc]\n\ts_add_u32 %[sb], %[sb], 16\n\ts_lshl_b32 %[sb], %[sb], 7\n\t"
      "s_mul_i32 %[sc], %[w], 0x60000\n\ts_add_u32 %[sb], %[sb], %[sc]\n\t"
      "s_waitcnt lgkmcnt(0)\n\t"
      "global_load_dword v105, v105, s[36:37]\n\tglobal_load_dword v104, v104, s[42:43] sc1\n\t"
      "s_add_u32 s44, s38, %[sb]\n\ts_addc_u32 s45, s39, 0\n\t" DMA4("s[44:45]")
      "s_add_u32 s44, s44, 0x30000\n\ts_addc_u32 s45, s45, 0\n\ts_add_u32 m0, m0, 0x1000\n\ts_nop 0\n\t" DMA4("s[44:45]")
      "s_branch 9f\n\t3:\n\t"
      "s_and_b32 %[sb], %[sa], 3\n\ts_lshl_b32 %[sb], %[sb], 2\n\ts_and_b32 s44, %[w], 3\n\ts_add_u32 %[sb], %[sb], s44\n\t"
      "s_lshl_b32 s44, s44, 12\n\ts_add_u32 m0, s44, %[tb]\n\ts_lshl_b32 s44, %[sb], 6\n\tv_add_u32 v105, s44, v105\n\t"
      "s_mul_i32 %[sb], %[sb], 0x30000\n\ts_lshl_b32 %[sc], %[sc], 8\n\ts_add_u32 %[sb], %[sb], %[sc]\n\ts_bfe_u32 %[sc], %[sa], 0x10002\n\ts_lshl_b32 %[sc], %[sc], 7\n\ts_add_u32 %[sb], %[sb], %[sc]\n\t"
      "s_lshr_b32 %[sc], %[w], 2\n\ts_lshr_b32 %[sa], %[sa], 3\n\ts_and_b32 %[sa], %[sa], %[sc]\n\ts_lshr_b32 %[sc], %[bid], 6\n\ts_cmp_eq_u32 %[sc], 1\n\ts_cselect_b32 %[sa], %[sa], 0\n\ts_cmp_eq_u32 %[sa], 1\n\t"
      "s_waitcnt lgkmcnt(0)\n\t"
      "global_load_dword v105, v105, s[36:37]\n\tglobal_load_dword v104, v104, s[42:43] sc1\n\ts_cbranch_scc0 9f\n\t"
      "s_add_u32 s44, s38, %[sb]\n\ts_addc_u32 s45, s39, 0\n\t" DMA4("s[44:45]")
      "9:\n\ts_mov_b32 m0, %[keep]"
      : "=&{s[44:45]}"(pa), [w] "=&s"(wave), [k0] "=&v"(ko0), [k1] "=&v"(ko1), [k2] "=&v"(ko2), [k3] "=&v"(ko3),
        "=&{v104}"(ep), "=&{v105}"(d1), "=&{v[106:109]}"(xs), [sa] "=&s"(sa), [sb] "=&s"(sb), [sc] "=&s"(sc), [keep] "=&s"(keep), [va] "=&v"(va), [vb] "=&v"(vb)
      : "{s[16:31]}"(ka), "{s[36:43]}"(kb8), "{s46}"(tab), [bid] "s"(blockIdx.x), [tid] "v"(threadIdx.x), [tb] "i"(L_TB), "{s[48:49]}"(pc), [ck] "i"(CODE_KB > 16 ? CODE_KB - 16 : 0)
      : "memory", "scc", "v110", "v111", "v112", "v113", "v114", "v115", "v116", "v117", "v118", "v119", "v120", "v121");
    asm volatile("" : "+{s[16:31]}"(ka), "+{s[36:43]}"(kb8), "+{s46}"(tab));
  }
  const u64 t31 = TNOW();
  volatile LAS u32* FL = (volatile LAS u32*)(L + L_FLAG);
  LAS float* RED = (LAS float*)(L + L_RED);
  const int lane = threadIdx.x & 63;
  const int bid = blockIdx.x, xc = bid & 7, ix = bid >> 3, rb = ix & 7;
  const int g = lane >> 4, n = lane & 15;
  const bool chain = ix < 8, wcu = (ix >> 3) == 1;
  const int toff = tab & 255, ntask = (tab >> 8) & 15;
  const bool ninth = wave == 2 && ntask == 9;
  const int aw = ninth ? 8 : wave - 8;
  const bool head = (wave >> 2) == 2 && !chain, p1 = wcu && (wave >> 2) == 1;
  const u32 ldst = (u32)(uintptr_t)L + L_K + aw * 16384;
#define KGROUP(kb, lo) do { u32 keep_; \
    asm volatile("s_mov_b32 %0, m0\n\ts_mov_b32 m0, %6\n\ts_nop 0\n\t" \
                 "global_load_lds_dwordx4 %1, %5" KV_SFX "\n\t" \
                 "global_load_lds_dwordx4 %2, %5 offset:1024" KV_SFX "\n\t" \
                 "global_load_lds_dwordx4 %3, %5 offset:2048" KV_SFX "\n\t" \
                 "global_load_lds_dwordx4 %4, %5 offset:3072" KV_SFX "\n\t" \
                 "s_mov_b32 m0, %0" \
                 : "=&s"(keep_) : "v"(ko0), "v"(ko1), "v"(ko2), "v"(ko3), "s"(kb), "s"(ldst + (lo)) : "memory"); } while (0)
#define PTR(v, i) (((u64)(v)[2 * (i) + 1] << 32) | (v)[2 * (i)])
  Ptrs p;
  p.x = (const u16*)PTR(ka, 0); p.K = (const u16*)PTR(ka, 1); p.V = (const u16*)PTR(ka, 2); p.wq = (const u16*)PTR(ka, 3); p.wo = (const u16*)PTR(ka, 4);
  p.wg = (const u16*)PTR(ka, 5); p.wu = (const u16*)PTR(ka, 6); p.wd = (const u16*)PTR(ka, 7);
  p.c = (const u16*)PTR(kb8, 0); p.wmod = (const u16*)PTR(kb8, 1); p.out = (u16*)PTR(kb8, 2); p.ws = (void*)PTR(kb8, 3);
#define MKWS() const rsrc_t ws = mkr(p.ws, WS_BYTES)
  if (wave == 0 && lane < 16) FL[lane] = (lane == 0 && ix >= 16) ? 1u : 0u;
  BARRIER();

  if (__builtin_expect(wave < 8 && !ninth, 1)) {
  MKWS();
  const bool gcu = ix >= 16 && ix < 23, mcu = ix >= 23 && ix < 27;
  LAS unsigned char* tb = L + L_TB + wave * 2048;
  if (wave >= QPOLL && !(chain || wcu || gcu || mcu)) return;
  __builtin_amdgcn_s_setprio(3);
  asm volatile("s_waitcnt vmcnt(8)\n\ts_cmp_eq_u32 %3, 2\n\ts_cbranch_scc1 1f\n\ts_waitcnt vmcnt(4)\n\ts_cmp_eq_u32 %3, 1\n\ts_cbranch_scc1 1f\n\ts_waitcnt vmcnt(0)\n\t1:"
               : "+{v104}"(ep), "+{v105}"(d1), "+{v[106:109]}"(xs) : "s"(__builtin_amdgcn_readfirstlane((chain || mcu) ? 2 : p1 ? 1 : 0)) : "memory", "scc");
  const int cg = xc * 2 + (rb >> 2), ks = rb & 3;
  const int pi = ((n >> 2) & 1) * 16 + g * 4 + (n & 3);
  const u32 tag = ep + 1u;
  if (__builtin_expect(p1, 1)) {
    v4u a = bcast4<0>(silu2(d1)), f[4];
    int ho = H0_OFF + (cg * 4 + ks) * 512 + lane * 8;
    PIN1(a); PIN1(ho);
    VM0();
    const u64 t1 = TNOW();
    lds_frags(L + L_TB + (wave & 3) * 4096, lane, f);
    RED[(wave & 3) * 64 + lane] = gemv<1>(&a, f, g);
    if (lds_ticket(&FL[3], lane) == 3u) {
      const float s = (RED[lane] + RED[64 + lane]) + (RED[128 + lane] + RED[192 + lane]);
      const float s_hi = dpp<SHL1>(s);
      if (!(lane & 1)) stc(ws, ho, v4u{tag, asu(s), ~tag, asu(s_hi)});
      MEMFENCE();
      if (lane == 0) FL[0] = 1u;
      STAMP(2);
    }
    if (wave == 4) STAMPV(1, t1);
  }
  if (__builtin_expect(chain, 1)) {
    float mb, inv;
    ln_stats(xs, mb, inv);
    f32x2 y = up2(ln_pair(d1, mb, inv));
    PIN1(y);
    VM0();
    v4u f[8];
    lds_frags(L + L_K + wave * 8192, lane, f); lds_frags(L + L_K + wave * 8192 + 4096, lane, f + 4);
    lds_inc(&FL[3], lane);
    SCHED();
    const u64 t27 = TNOW();
    v4u h0, h1, h2, h3;
    pollT4(ws, H0_OFF + (wave + (n >> 3) * 8) * 2048 + pi * 16, tag, ~tag, h0, h1, h2, h3);
    if (wave == 0) { STAMP(3); STAMPV(24, t31); STAMPV(27, t27); }
    const u32 md = pk((asf(h0.y) + asf(h1.y)) + (asf(h2.y) + asf(h3.y)), (asf(h0.w) + asf(h1.w)) + (asf(h2.w) + asf(h3.w)));
    const u32 ot = dppu<ROR8>(md), sa = (n & 8) ? ot : md, ca = (n & 8) ? md : ot;
    const u32 hp = pk2(up2(pk2(y * one_plus(ca))) + up2(sa));
    const v4u a[2] = {bcast4<0>(hp), bcast4<4>(hp)};
    RED[wave * 64 + lane] = gemv<2>(a, f, g);
    if (lds_ticket(&FL[7], lane) == 7u) {
      const float s = sum8(RED + lane, 64);
      const u32 d = pk(s, dpp<SHL1>(s)), d2 = dppu<SHL2>(d);
      if ((lane & 3) == 0) stc(ws, Q_OFF + (xc * 8 + rb) * 256 + lane * 4, v4u{tag, d, ~tag, d2});
      MEMFENCE();
      if (lane == 0) FL[0] = 1u;
      STAMP(4);
    }
  }
  if (mcu) {
    const u32 sp = silu2(d1);
    const v4u a[2] = {bcast4<0>(sp), bcast4<4>(sp)};
    VM0();
    v4u f[8];
    lds_frags(L + 65536 + wave * 8192, lane, f); lds_frags(L + 65536 + wave * 8192 + 4096, lane, f + 4);
    RED[wave * 64 + lane] = gemv<2>(a, f, g);
    const u32 tk = lds_ticket(&FL[3], lane);
    if (wave == 1) {
      lds_wait_ge(&FL[3], 8u);
      const float s = sum8(RED + lane, 64);
      const u32 d = nosent(pk(s, dpp<SHL1>(s)));
      lds_wait_ge(&FL[5], 1u + (tk & 0u));
      if (!(lane & 1)) stc1(ws, MR_OFF + (xc * 4 + (ix - 23)) * 128 + lane * 2, d);
      STAMP(22);
    }
  }
  if (wcu && wave == 1) {
    lds_wait_ge(&FL[0], 1u);
    const v4u sv = {SENT, SENT, SENT, SENT};
#pragma unroll
    for (int j = 0; j < CLR_KB; ++j) stc(ws, S_OFF + (xc * 8 + rb) * (CLR_KB * 1024) + j * 1024 + lane * 16, sv);
    VM0();
    if (lane == 0) stc2(ws, CF_OFF + (xc * 8 + rb) * 128, v2u{tag, ~tag});
  }
  if (wave < QPOLL) {
    const int qb = Q_OFF + xc * 2048 + lane * 16;
    for (int i = 0; i < wave; ++i) __builtin_amdgcn_s_sleep(QSTAG);
    _Pragma("nounroll") for (u32 s = 0; s < SPIN_CAP; ++s) {
      const v4u q0 = ldc(ws, qb), q1 = ldc(ws, qb + 1024);
      SCHED();
      if (__all((((q0.x ^ tag) | (q1.x ^ tag)) | ((q0.z ^ ~tag) | (q1.z ^ ~tag))) == 0u)) {
        LAS unsigned char* qd = L + L_Q + (lane >> 5) * 320 + (lane & 31) * 8;
        *(LAS v2u*)qd = v2u{q0.y, q0.w};
        *(LAS v2u*)(qd + 640) = v2u{q1.y, q1.w};
        LGKM0();
        if (lane == 0) FL[1] = 1u;
        STAMP(5);
        break;
      }
      if (FL[1] != 0u) break;
      MEMFENCE();
    }
    if (wave == 0) {
      STAMPV(0, t0);
      _Pragma("nounroll") for (u32 s = 0; s < SPIN_CAP; ++s) {
        const v2u cf = __builtin_amdgcn_raw_buffer_load_b64(ws, CF_OFF + lane * 128, 0, SC1);
        if (__all(((cf.x ^ tag) | (cf.y ^ ~tag)) == 0u)) break;
        __builtin_amdgcn_s_sleep(4);
        MEMFENCE();
      }
      if (lane == 0) FL[5] = 1u;
    }
  }
  if (ix < 16) {
    if (wave < 4) return;
    const int x = wave & 3, hl = ix >> 2, hg = xc * 4 + hl, qd = ix & 3;
    const rsrc_t ro = mkr(p.wo, 4096 * 512 * 2);
    const int col = xc * 64 + rb * 8;
    lds_wait_ge(&FL[2], (u32)ntask);
    v4u w[8];
    {
      const int rho = lane >> 3, vo = (64 * (rho >> 2) + 8 * (rho & 3)) * 1024 + (lane & 7) * 16;
#pragma unroll
      for (int i = 0; i < 8; ++i) w[i] = ld4<W_AUX>(ro, vo, (hg * 128 + 32 * ((i >> 1) & 1) + 2 * qd + (i & 1)) * 1024 + x * 256 + (i >> 2) * 128);
    }
    BARRIER();
    const v2u ma = poll2(ws, ML_OFF + ((xc * 32 + (lane >> 1)) * 2 + (hl >> 1)) * 128 + (hl & 1) * 8);
    v4u f[8];
    tile_frags(tb, lane, w, f); tile_frags(tb, lane, w + 4, f + 4);
    if (wave == 4) STAMP(30);
    int ob = T1_OFF + ((hg * 4 + qd) * 32 + (lane >> 1)) * 128 + x * 32 + (lane & 1) * 16;
    int t2o = T2_OFF + (xc * 16 + ix) * 2048 + x * 512 + lane * 4;
    float M = asf(ma.x);
    M = mx(M, dpp<XOR2>(M)); M = mx(M, dpp<ROR4>(M)); M = mx(M, dpp<ROR8>(M)); M = tm16(M, M); M = tm32(M, M);
    float fa = __builtin_amdgcn_exp2f((asf(ma.x) - M) * LOG2E);
    float Ls = fa * asf(ma.y);
    Ls = tr32(Ls, Ls); Ls = tr16(Ls, Ls); Ls += dpp<ROR8>(Ls); Ls += dpp<ROR4>(Ls); Ls += dpp<XOR2>(Ls);
    float inv = __builtin_amdgcn_rcpf(Ls);
    u32 oa = (u32)(uintptr_t)(L + L_X) + x * 16 + (lane & 1) * 8 + (lane >> 5) * 4;
    PIN1(fa); PIN1(inv); PIN1(ob); PIN1(t2o); PIN1(oa);
    SCHED();
    const v4u a = poll4(ws, ob);
    if (wave == 4) STAMP(14);
    float u0 = tr32(fa * asf(a.x), fa * asf(a.z)), u1 = tr32(fa * asf(a.y), fa * asf(a.w));
    u0 = tr16(u0, u0); u1 = tr16(u1, u1);
    u0 += dpp<ROR8>(u0); u1 += dpp<ROR8>(u1);
    u0 += dpp<ROR4>(u0); u1 += dpp<ROR4>(u1);
    u0 += dpp<XOR2>(u0); u1 += dpp<XOR2>(u1);
    if (!(lane & 30)) *(LAS u32*)(uintptr_t)oa = pk(u0 * inv, u1 * inv);
    BARRIER();
    const v4u av = *(const LAS v4u*)(L + L_X + g * 16);
    const float y0 = gemv<1>(&av, f, g), y1 = gemv<1>(&av, f + 4, g);
    stc_row(ws, t2o, y0, lane); stc_row(ws, t2o + 256, y1, lane);
    if (wave == 4) STAMP(15);
    if (!chain || wave != 4) return;
    f32x2 xin = up2(*(const GAS u32*)((const GAS u16*)p.x + col + 2 * g));
    u32 gaw, gbw;
    poll1x2<3072>(ws, MR_OFF + (col + 2 * g) * 2, gaw, gbw);
    f32x2 ga = up2(gaw), gb = up2(gbw);
    int tio = T2_OFF + lane * 2048 + col * 4, tio2 = tio + 131072, yo = lane < 56 ? Y_OFF + lane * 2048 + col * 4 : 0x40000000;
    PIN1(ga); PIN1(gb); PIN1(xin); PIN1(tio); PIN1(tio2); PIN1(yo);
    v4u pa, pb, pc, pd;
    poll4x4(ws, tio, tio2, pa, pb, pc, pd);
    STAMP(16);
    pa = __builtin_bit_cast(v4u, __builtin_bit_cast(f32x4, pa) + __builtin_bit_cast(f32x4, pc));
    pb = __builtin_bit_cast(v4u, __builtin_bit_cast(f32x4, pb) + __builtin_bit_cast(f32x4, pd));
    const u32 x1 = nosent(pk2(xin + up2(pk2(ga * up2(pk2(sum8x64(pa, pb)))))));
    const v4u o = {rdl(x1, 0), rdl(x1, 16), rdl(x1, 32), rdl(x1, 48)};
    if (lane == 0) stc(ws, X1_OFF + (xc * 8 + rb) * 128, o);
    STAMP(17);
    if (bid == 0 && lane == 0) stc1(ws, EPOCH_OFF, tag);
    f32x2 x1f = up2(x1);
    PIN1(x1f);
    poll4x2<16>(ws, yo, pa, pb);
    STAMP(20);
    const u32 d = pk2(x1f + up2(pk2(gb * up2(pk2(sum8x64(pa, pb))))));
    if (!n) *(GAS u32*)((GAS u16*)p.out + col + 2 * g) = d;
    STAMP(21);
  } else if (gcu) {
    const int gi = xc * 7 + (ix - 16);
    const rsrc_t rd = mkr(p.wd, 1792 * 512 * 2);
    BARRIER();
    lds_wait_ge(&FL[8], 2u);
    v4u w[12];
    {
      const int rho = lane >> 3;
      const GAS unsigned char* src = (const GAS unsigned char*)((lane & 4) ? p.wu : p.wg) + (size_t)(wave * 64 + 8 * (rho >> 2) + (rho & 3)) * 3584 + gi * 64 + (lane & 3) * 16;
#pragma unroll
      for (int i = 0; i < 8; ++i) w[i] = *(const GAS v4u*)(src + (32 * (i >> 2) + 16 * (i & 1) + 4 * ((i >> 1) & 1)) * 3584);
    }
    tile_load<W_AUX>(rd, tile_voff(lane, 1024), gi * 32 * 1024 + wave * 128, 1024, w + 8);
    v4u f[12];
    tile_frags(tb, lane, w, f); tile_frags(tb, lane, w + 4, f + 4); tile_frags(tb, lane, w + 8, f + 8);
    int yo = Y_OFF + gi * 2048 + wave * 256 + lane * 4;
    PIN1(yo);
    if (wave == 0) STAMP(29);
    SCHED();
    if (wave < XP) {
      constexpr int NP = 4 / XP;
      v4u sbw, cbw;
      poll4x2<1024>(ws, MR_OFF + 1024 + lane * 16, sbw, cbw);
      f32x2 sb[NP], c1[NP];
#pragma unroll
      for (int i = 0; i < NP; ++i) {
        const u32 s_ = XP == 1 ? sbw[i] : XP == 2 ? (wave ? sbw[2 + i] : sbw[i]) : ((wave & 2) ? ((wave & 1) ? sbw.w : sbw.z) : ((wave & 1) ? sbw.y : sbw.x));
        const u32 c_ = XP == 1 ? cbw[i] : XP == 2 ? (wave ? cbw[2 + i] : cbw[i]) : ((wave & 2) ? ((wave & 1) ? cbw.w : cbw.z) : ((wave & 1) ? cbw.y : cbw.x));
        sb[i] = up2(s_); c1[i] = one_plus(c_); PIN1(sb[i]); PIN1(c1[i]);
      }
      int xb1 = X1_OFF + lane * 128;
      PIN1(xb1);
      const v4u xs = poll4(ws, xb1);
      if (wave == 0) STAMP(18);
      float mb, inv;
      ln_stats(xs, mb, inv);
#pragma unroll
      for (int i = 0; i < NP; ++i) {
        const u32 xm = XP == 1 ? xs[i] : XP == 2 ? (wave ? xs[2 + i] : xs[i]) : ((wave & 2) ? ((wave & 1) ? xs.w : xs.z) : ((wave & 1) ? xs.y : xs.x));
        *(LAS u32*)(L + L_X + lane * 16 + (wave * NP + i) * 4) = modulate(ln_pair(xm, mb, inv), c1[i], sb[i]);
      }
    }
    BARRIER();
    const v4u av[2] = {*(const LAS v4u*)(L + L_X + wave * 128 + g * 16), *(const LAS v4u*)(L + L_X + wave * 128 + 64 + g * 16)};
    RED[wave * 64 + lane] = gemv<2>(av, f, g);
    BARRIER();
    const float s = rbf(sum8(RED + (n >> 3) * 32 + g * 8 + (n & 7), 64));
    const float gt = s, up = dpp<ROR8>(s);
    const float u2 = (-2.f * 0.7978845608028654f * LOG2E) * (gt + 0.044715f * gt * gt * gt);
    const float gv = rbf(gt * __builtin_amdgcn_rcpf(1.f + __builtin_amdgcn_exp2f(u2))) * up;
    const u32 d = pk(gv, dpp<SHL1>(gv));
    const v4u ag = {dppu<BC + 0>(d), dppu<BC + 2>(d), dppu<BC + 4>(d), dppu<BC + 6>(d)};
    stc_row(ws, yo, gemv<1>(&ag, f + 8, g), lane);
    if (wave == 0) STAMP(19);
  }
  return;
  }
  {
    const bool early = aw < 4;
    if (ninth) {
      VM0();
      const u32 kv0 = g * 256 + ((n ^ g) << 4);
      ko0 = kv0; ko1 = kv0 ^ 64u; ko2 = kv0 ^ 128u; ko3 = kv0 ^ 192u;
    }
    if (aw >= ntask) return;
    if (chain) lds_wait_ge(CH_EARLY ? &FL[3] : &FL[0], CH_EARLY ? 8u : 1u);
    if (!early) {
      lds_wait_ge(&FL[3], (ix >= 23 && ix < 27) ? 8u : (wcu && ninth) ? 4u : 0u);
      _Pragma("nounroll") for (u32 s = 0; s < (SPIN_CAP << 3); ++s) { if (FL[2] >= 4u || FL[1] != 0u) break; __builtin_amdgcn_s_sleep(1); }
      MEMFENCE();
    }
    const int hh = n >> 2, e2 = n & 3;
    u32 qseen = 0;
    const u32 key0 = (u32)(xc * 16384 + (toff + aw) * 64);
#define THROTTLE() do { if (!qseen) { const v2u fl_ = *(volatile LAS v2u*)FL; qseen = __builtin_amdgcn_readfirstlane(fl_.y); \
      if (!qseen) { if (__builtin_amdgcn_readfirstlane(fl_.x)) asm volatile("s_waitcnt vmcnt(%0)" :: "i"(THR_W - 4) : "memory"); else asm volatile("s_waitcnt vmcnt(%0)" :: "i"(THR_Q - 4) : "memory"); } } SCHED(); } while (0)
    const u64 kb = (u64)p.K + (u64)key0 * 256;
    if (!head) KGROUP(kb, 0);
    THROTTLE(); KGROUP(kb + 4096, 4096);
    THROTTLE(); KGROUP(kb + 8192, 8192); THROTTLE(); KGROUP(kb + 12288, 12288); THROTTLE();
    v4u vv[16];
    {
      const rsrc_t rv = mkr(p.V, 8 * 16384 * 256);
      const int vo = g * 4096 + n * 16;
#pragma unroll
      for (int i = 0; i < 16; ++i) { vv[i] = ld4<KV_AUX>(rv, vo + i * 256, key0 * 256); if ((i & 3) == 3 && i < 15) THROTTLE(); }
    }
    SCHED();
    lds_inc(&FL[2], lane);
    const u64 tI = TNOW();
    lds_wait_ge(&FL[1], 1u);
    asm volatile("s_waitcnt vmcnt(16)" ::: "memory");
    SCHED();
    const u64 tK = TNOW();
    float s2[4], pe[4];
    {
      const LAS unsigned char* kbase = L + L_K + aw * 16384 + n * 256;
      const LAS unsigned char* qa = L + L_Q + (n & 3) * 320 + g * 16;
      f32x4 sa[4];
#pragma unroll
      for (int t = 0; t < 4; ++t) sa[t] = f32x4{0.f, 0.f, 0.f, 0.f};
#pragma unroll
      for (int s = 0; s < 4; ++s) {
        const v4u a = *(const LAS v4u*)(qa + 64 * s);
#pragma unroll
        for (int t = 0; t < 4; ++t) sa[t] = mfma32(a, *(const LAS v4u*)(kbase + t * 4096 + (((4 * s + g) ^ n) << 4)), sa[t]);
      }
      const f32x4 lo = (g & 1) ? sa[1] : sa[0], hi = (g & 1) ? sa[3] : sa[2], sacc = (g & 2) ? hi : lo;
      constexpr float SC = 0.08838834764831845f;
      const f32x2 c = up2(pk2(up2(pk(sacc[0], sacc[1])) * SC)), d = up2(pk2(up2(pk(sacc[2], sacc[3])) * SC));
      s2[0] = c.x; s2[1] = c.y; s2[2] = d.x; s2[3] = d.y;
    }
    const float mrow = row_max(tm16(tm32(s2[0], s2[2]), tm32(s2[1], s2[3])));
#pragma unroll
    for (int h2 = 0; h2 < 4; ++h2) pe[h2] = __builtin_amdgcn_exp2f((s2[h2] - rdlf(mrow, 16 * h2)) * LOG2E);
    const u32 w0 = pk(pe[0], pe[1]), w1 = pk(pe[2], pe[3]);
    const float lrow = row_sum(tr16(tr32(pe[0], pe[2]), tr32(pe[1], pe[3])));
    if (n == 0) *(LAS f32x2*)(L + L_ML + g * 128 + aw * 8) = f32x2{mrow, lrow};
    MEMFENCE();
    lds_inc(&FL[4], lane);
    const u32 two = (u32)(2 * hh) | ((u32)(2 * hh + 1) << 8);
    const u32 selv = (e2 & 1) ? ((two << 16) | 0x0C0Cu) : (0x0C0C0000u | two);
    const u32 sel0 = (e2 >> 1) == 0 ? selv : 0x0C0C0C0Cu, sel1 = (e2 >> 1) == 1 ? selv : 0x0C0C0C0Cu;
    v2u xb[16];
#define PV_PREP(i) { const u32 b0_ = dppu<BC + (i)>(w0), b1_ = dppu<BC + (i)>(w1); xb[i] = v2u{__builtin_amdgcn_perm(b1_, b0_, sel0), __builtin_amdgcn_perm(b1_, b0_, sel1)}; }
    PV_PREP(0) PV_PREP(1) PV_PREP(2) PV_PREP(3) PV_PREP(4) PV_PREP(5) PV_PREP(6) PV_PREP(7)
    PV_PREP(8) PV_PREP(9) PV_PREP(10) PV_PREP(11) PV_PREP(12) PV_PREP(13) PV_PREP(14) PV_PREP(15)
#undef PV_PREP
    u32 obw = (u32)(uintptr_t)L + L_K + aw * 16384 + (hh * 128 + e2 * 16 + 4 * g) * 4;
    PIN1(obw);
    SCHED();
    f32x4 o1 = {0.f, 0.f, 0.f, 0.f}, o2 = {0.f, 0.f, 0.f, 0.f};
#pragma unroll
    for (int i = 0; i < 16; ++i) {
      o1 = __builtin_amdgcn_mfma_f32_16x16x16bf16_1k(__builtin_bit_cast(s16x4, v2u{vv[i].x, vv[i].y}), __builtin_bit_cast(s16x4, xb[i]), o1, 0, 0, 0);
      o2 = __builtin_amdgcn_mfma_f32_16x16x16bf16_1k(__builtin_bit_cast(s16x4, v2u{vv[i].z, vv[i].w}), __builtin_bit_cast(s16x4, xb[i]), o2, 0, 0, 0);
    }
    *(LAS f32x4*)(uintptr_t)obw = o1; *(LAS f32x4*)(uintptr_t)(obw + 256) = o2;
    if (aw < 2 || aw > 3) { BARRIER(); return; }
    MKWS();
    const int tau = (aw - 2) * 64 + lane, h = tau >> 5;
    float f[9];
    lds_wait_ge(&FL[5], 1u);
    lds_wait_ge(&FL[4], (u32)ntask);
    {
      float m[10], l[10];
#pragma unroll
      for (int w = 0; w < 5; ++w) { const f32x4 t = *(const LAS f32x4*)(L + L_ML + h * 128 + w * 16); m[2 * w] = t.x; l[2 * w] = t.y; m[2 * w + 1] = t.z; l[2 * w + 1] = t.w; }
#pragma unroll
      for (int w = 4; w < 9; ++w) { m[w] = w < ntask ? m[w] : -__builtin_inff(); l[w] = w < ntask ? l[w] : 0.f; }
      const float M = mx(mx(mx(mx(m[0], m[1]), mx(m[2], m[3])), mx(mx(m[4], m[5]), mx(m[6], m[7]))), m[8]);
      float Ls = 0.f;
#pragma unroll
      for (int w = 0; w < 9; ++w) { f[w] = __builtin_amdgcn_exp2f((m[w] - M) * LOG2E); Ls = fmaf(l[w], f[w], Ls); PIN1(f[w]); }
      if ((lane & 31) == 0) stc2(ws, ML_OFF + ((xc * 32 + ix) * 2 + aw - 2) * 128 + (lane >> 5) * 8, v2u{asu(M), asu(Ls)});
    }
    int t1o = T1_OFF + (((xc * 4 + h) * 4 + ((tau >> 3) & 3)) * 32 + ix) * 128 + (tau & 7) * 16;
    u32 pa[9];
#pragma unroll
    for (int w = 0; w < 9; ++w) { pa[w] = (u32)(uintptr_t)(L + L_K) + tau * 16 + (w < ntask ? w : 0) * 16384; PIN1(pa[w]); }
    PIN1(t1o);
    BARRIER();
    if (aw == 2) STAMP(chain ? 12 : ix < 16 ? 23 : ix < 23 ? 25 : ix < 27 ? 26 : 28);
    f32x4 part[9];
#pragma unroll
    for (int w = 0; w < 9; ++w) part[w] = *(const LAS f32x4*)(uintptr_t)pa[w];
    const f32x4 acc = (((part[0] * f[0] + part[1] * f[1]) + (part[2] * f[2] + part[3] * f[3])) + ((part[4] * f[4] + part[5] * f[5]) + (part[6] * f[6] + part[7] * f[7]))) + part[8] * f[8];
    stc(ws, t1o, __builtin_bit_cast(v4u, acc));
    lds_inc(&FL[8], lane);
    if (aw == 2) STAMP(13);
    return;
  }

}
}

extern "C" void kernel_launch(void* const* d_in, const int* in_sizes, int n_in, void* d_out, int out_size, void* d_ws, size_t ws_size, hipStream_t stream) {
  Params a;
  Ptrs& p = a.p;
  p.x = (const u16*)d_in[0]; p.K = (const u16*)d_in[1]; p.V = (const u16*)d_in[2]; p.wq = (const u16*)d_in[3]; p.wo = (const u16*)d_in[4];
  p.wg = (const u16*)d_in[6]; p.wu = (const u16*)d_in[7]; p.wd = (const u16*)d_in[8]; p.c = (const u16*)d_in[9]; p.wmod = (const u16*)d_in[10];
  p.out = (u16*)d_out; p.ws = d_ws;
  for (int i = 0; i < 32; ++i) a.tab[i] = (u32)toff_of(i) | ((u32)ntask_of(i) << 8);
  hipLaunchKernelGGL(fused_kernel, dim3(NWG), dim3(NTHR), 0, stream, a);
}
```

```cpp
#include <hip/hip_runtime.h>
#include <stdint.h>

namespace {
typedef unsigned short u16;
typedef unsigned u32;
typedef unsigned long long u64;
typedef u32 v4u __attribute__((ext_vector_type(4)));
typedef u32 v2u __attribute__((ext_vector_type(2)));
typedef u32 v8u __attribute__((ext_vector_type(8)));
typedef u32 v16u __attribute__((ext_vector_type(16)));
typedef float f32x4 __attribute__((ext_vector_type(4)));
typedef float f32x2 __attribute__((ext_vector_type(2)));
typedef __bf16 bf16x2 __attribute__((ext_vector_type(2)));
typedef __bf16 bf16x8 __attribute__((ext_vector_type(8)));
typedef short s16x4 __attribute__((ext_vector_type(4)));
#define GAS __attribute__((address_space(1)))
#define LAS __attribute__((address_space(3)))

#define KV_AUX 2
#define KV_SFX " nt"
#define W_AUX 2
#define THR_W 8
#define THR_Q 6
#define CH_EARLY 0
#define CODE_PF 1
#define CF_SLEEP 100
#define CF_STRIDE 16
#define Q_DELAY 100
#define LATE_FREE 1
#define MAPL 0
#define IXOP "s_lshr_b32 "
#define IXC "3"
#define XCOP "s_and_b32 "
#define XCC "7"
#define PFR "%[sa]"
#define QPOLL 1
#define XP 4
#define QSTAG 24
#define H_W 18
#define H_G 18
#define H_M 16
#define H_P 18

constexpr int NWG = 256, NTHR = 1024;
constexpr u32 SENT = 0xFFFFFFFFu;
constexpr int SC1 = 16;
constexpr u32 SPIN_CAP = 1u << 17;
constexpr float LOG2E = 1.4426950408889634f;
constexpr int EPOCH_OFF = 0;
constexpr int CF_OFF = 8192;
constexpr int Q_OFF = 16384;
constexpr int H0_OFF = 32768;
constexpr int S_OFF = 65536;
constexpr int T1_OFF = S_OFF;
constexpr int ML_OFF = T1_OFF + 524288;
constexpr int T2_OFF = ML_OFF + 65536;
constexpr int Y_OFF = T2_OFF + 262144;
constexpr int X1_OFF = Y_OFF + 114688;
constexpr int MR_OFF = X1_OFF + 8192;
constexpr int CLR_KB = 16;
constexpr int S_END = S_OFF + 64 * CLR_KB * 1024;
static_assert(MR_OFF + 4096 <= S_END, "");
constexpr int WS_BYTES = S_END;
constexpr int L_K = 0, L_TB = 8 * 16384, L_Q = 9 * 16384, L_FLAG = L_Q + 1280, L_ML = L_FLAG + 64, L_X = L_ML + 512, L_RED = L_X + 1024, L_END = L_RED + 4096;
static_assert(L_END <= 163840 && L_RED % 16 == 0 && L_TB % 16 == 0, "");
constexpr int CH_ALL = 512 - 8 * H_W - 7 * H_G - 4 * H_M - 5 * H_P, H_CH = CH_ALL / 8, CH_HI = CH_ALL - 8 * H_CH;
constexpr int nhalf_of(int ix) { return ix < 8 ? (ix < CH_HI ? H_CH + 1 : H_CH) : ix < 16 ? H_W : ix < 23 ? H_G : ix < 27 ? H_M : H_P; }
constexpr int toff_of(int ix) { int o = 0; for (int i = 0; i < ix; ++i) o += nhalf_of(i); return o; }
static_assert(toff_of(32) == 512 && H_G >= 8 && H_G <= 18 && H_CH >= 8 && H_CH + 1 <= 16 && H_M >= 8 && H_M <= 16 && H_W >= 8 && H_W <= 18 && H_P >= 8 && H_P <= 18, "");

#define STAMP(k) do {} while (0)
#define TNOW() 0ull
#define STAMPV(k, v) do { (void)(v); } while (0)

struct Ptrs {
  const u16 *x, *K, *V, *wq, *wo, *wg, *wu, *wd, *c, *wmod;
  u16* out;
  void* ws;
};
struct Params { Ptrs p; u32 tab[32]; };
static_assert(sizeof(Params) == 96 + 128 && L_K == 0, "");

__device__ __forceinline__ float asf(u32 x) { return __builtin_bit_cast(float, x); }
__device__ __forceinline__ u32 asu(float x) { return __builtin_bit_cast(u32, x); }
__device__ __forceinline__ float blo(u32 w) { return asf(w << 16); }
__device__ __forceinline__ float bhi(u32 w) { return asf(w & 0xffff0000u); }
__device__ __forceinline__ u32 pk2(f32x2 v) { return __builtin_bit_cast(u32, __builtin_convertvector(v, bf16x2)); }
__device__ __forceinline__ u32 pk(float lo, float hi) { return pk2(f32x2{lo, hi}); }
__device__ __forceinline__ f32x2 up2(u32 w) { return f32x2{blo(w), bhi(w)}; }
__device__ __forceinline__ float rbf(float f) { return (float)(__bf16)f; }
__device__ __forceinline__ float dot2(u32 a, u32 b, float c) { return __builtin_amdgcn_fdot2_f32_bf16(__builtin_bit_cast(bf16x2, a), __builtin_bit_cast(bf16x2, b), c, false); }
template <int CTRL> __device__ __forceinline__ float dpp(float x) { return asf(__builtin_amdgcn_mov_dpp(asu(x), CTRL, 0xf, 0xf, true)); }
template <int CTRL> __device__ __forceinline__ u32 dppu(u32 x) { return __builtin_amdgcn_mov_dpp(x, CTRL, 0xf, 0xf, true); }
constexpr int XOR1 = 0xB1, XOR2 = 0x4E, XOR7 = 0x141, ROR8 = 0x128, ROR4 = 0x124, SHL1 = 0x101, SHL2 = 0x102, BC = 0x150;
__device__ __forceinline__ float mx(float a, float b) { return __builtin_amdgcn_fmed3f(a, b, __builtin_inff()); }
__device__ __forceinline__ float tr32(float a, float b) { auto s = __builtin_amdgcn_permlane32_swap(asu(a), asu(b), false, false); return asf(s[0]) + asf(s[1]); }
__device__ __forceinline__ float tr16(float a, float b) { auto s = __builtin_amdgcn_permlane16_swap(asu(a), asu(b), false, false); return asf(s[0]) + asf(s[1]); }
__device__ __forceinline__ float tm32(float a, float b) { auto s = __builtin_amdgcn_permlane32_swap(asu(a), asu(b), false, false); return mx(asf(s[0]), asf(s[1])); }
__device__ __forceinline__ float tm16(float a, float b) { auto s = __builtin_amdgcn_permlane16_swap(asu(a), asu(b), false, false); return mx(asf(s[0]), asf(s[1])); }
__device__ __forceinline__ float row_sum(float v) { v += dpp<ROR8>(v); v += dpp<XOR7>(v); v += dpp<XOR2>(v); v += dpp<XOR1>(v); return v; }
__device__ __forceinline__ float row_max(float v) { v = mx(v, dpp<ROR8>(v)); v = mx(v, dpp<XOR7>(v)); v = mx(v, dpp<XOR2>(v)); v = mx(v, dpp<XOR1>(v)); return v; }
__device__ __forceinline__ f32x2 sum8x64(v4u a, v4u b) {
  const float y0 = tr32(asf(a.x), asf(b.x)), y1 = tr32(asf(a.y), asf(b.y)), y2 = tr32(asf(a.z), asf(b.z)), y3 = tr32(asf(a.w), asf(b.w));
  return f32x2{row_sum(tr16(y0, y2)), row_sum(tr16(y1, y3))};
}
__device__ __forceinline__ u32 rdl(u32 v, int l) { return __builtin_amdgcn_readlane(v, l); }
__device__ __forceinline__ float rdlf(float v, int l) { return asf(__builtin_amdgcn_readlane(asu(v), l)); }
__device__ __forceinline__ u32 nosent(u32 w) { return w < 0xFFFFFFFEu ? w : 0xFFFFFFFEu; }
typedef __amdgpu_buffer_rsrc_t rsrc_t;
__device__ __forceinline__ rsrc_t mkr(const void* q, int bytes) { return __builtin_amdgcn_make_buffer_rsrc((void*)q, 0, bytes, 0x00020000); }
template <int AUX> __device__ __forceinline__ v4u ld4(rsrc_t r, int off, int so) { return __builtin_amdgcn_raw_buffer_load_b128(r, off, so, AUX); }
__device__ __forceinline__ u32 ld1(rsrc_t r, int off, int so) { return __builtin_amdgcn_raw_buffer_load_b32(r, off, so, 0); }
__device__ __forceinline__ v4u ldc(rsrc_t r, int off) { return __builtin_amdgcn_raw_buffer_load_b128(r, off, 0, SC1); }
__device__ __forceinline__ u32 ldc1(rsrc_t r, int off) { return __builtin_amdgcn_raw_buffer_load_b32(r, off, 0, SC1); }
__device__ __forceinline__ void stc(rsrc_t r, int off, v4u v) { __builtin_amdgcn_raw_buffer_store_b128(v, r, off, 0, SC1); }
__device__ __forceinline__ void stc2(rsrc_t r, int off, v2u v) { __builtin_amdgcn_raw_buffer_store_b64(v, r, off, 0, SC1); }
__device__ __forceinline__ void stc_row(rsrc_t r, int off, float v, int lane) {
  const v4u o = {asu(v), asu(dpp<0x55>(v)), asu(dpp<0xAA>(v)), asu(dpp<0xFF>(v))};
  if (!(lane & 3)) stc(r, off, o);
}
__device__ __forceinline__ void stc1(rsrc_t r, int off, u32 v) { __builtin_amdgcn_raw_buffer_store_b32(v, r, off, 0, SC1); }
#define MEMFENCE() asm volatile("" ::: "memory")
#define LGKM0() asm volatile("s_waitcnt lgkmcnt(0)" ::: "memory")
#define VM0() asm volatile("s_waitcnt vmcnt(0)" ::: "memory")
#define SCHED() __builtin_amdgcn_sched_barrier(0)
#define BARRIER() do { LGKM0(); SCHED(); __builtin_amdgcn_s_barrier(); MEMFENCE(); SCHED(); } while (0)
#define PIN1(a) asm volatile("" : "+v"(a))
#define PIN4(a) asm volatile("" : "+v"((a)[0]), "+v"((a)[1]), "+v"((a)[2]), "+v"((a)[3]))
#define PIN8(a) asm volatile("" : "+v"((a)[0]), "+v"((a)[1]), "+v"((a)[2]), "+v"((a)[3]), "+v"((a)[4]), "+v"((a)[5]), "+v"((a)[6]), "+v"((a)[7]))

#define POLL_HEAD "s_mov_b32 %[n], %[cap]\n\t1:\n\t"
#define POLL_TAIL "s_cbranch_execz 2f\n\ts_sub_u32 %[n], %[n], 1\n\ts_cbranch_scc0 1b\n\t2:\n\ts_mov_b64 exec, -1\n\ts_nop 2"
__device__ __forceinline__ v4u poll4(rsrc_t r, int off) {
  v4u a; u32 n;
  asm volatile(POLL_HEAD "buffer_load_dwordx4 v[124:127], %[o], %[r], 0 offen sc1\n\ts_waitcnt vmcnt(0)\n\t"
               "v_max_u32 v123, v124, v125\n\tv_max3_u32 v123, v123, v126, v127\n\tv_cmpx_eq_u32 vcc, -1, v123\n\t" POLL_TAIL
               : "=&{v[124:127]}"(a), [n] "=&s"(n) : [o] "v"(off), [r] "s"(r), [cap] "i"(SPIN_CAP) : "memory", "vcc", "scc", "v123");
  return a;
}
template <int D> __device__ __forceinline__ void poll4x2(rsrc_t r, int off, v4u& a, v4u& b) {
  u32 n;
  asm volatile(POLL_HEAD "buffer_load_dwordx4 v[120:123], %[o], %[r], 0 offen sc1\n\tbuffer_load_dwordx4 v[124:127], %[o], %[r], 0 offen offset:%[d] sc1\n\ts_waitcnt vmcnt(1)\n\t"
               "v_max_u32 v119, v120, v121\n\tv_max3_u32 v119, v119, v122, v123\n\ts_waitcnt vmcnt(0)\n\tv_max3_u32 v119, v119, v124, v125\n\tv_max3_u32 v119, v119, v126, v127\n\t"
               "v_cmpx_eq_u32 vcc, -1, v119\n\t" POLL_TAIL
               : "=&{v[120:123]}"(a), "=&{v[124:127]}"(b), [n] "=&s"(n) : [o] "v"(off), [r] "s"(r), [cap] "i"(SPIN_CAP), [d] "i"(D) : "memory", "vcc", "scc", "v119");
}
__device__ __forceinline__ void poll4x4(rsrc_t r, int off, int off2, v4u& a, v4u& b, v4u& c, v4u& d) {
  u32 n;
  asm volatile(POLL_HEAD "buffer_load_dwordx4 v[112:115], %[o], %[r], 0 offen sc1\n\tbuffer_load_dwordx4 v[116:119], %[o], %[r], 0 offen offset:16 sc1\n\t"
               "buffer_load_dwordx4 v[120:123], %[o2], %[r], 0 offen sc1\n\tbuffer_load_dwordx4 v[124:127], %[o2], %[r], 0 offen offset:16 sc1\n\t"
               "s_waitcnt vmcnt(3)\n\tv_max_u32 v110, v112, v113\n\tv_max3_u32 v110, v110, v114, v115\n\t"
               "s_waitcnt vmcnt(2)\n\tv_max_u32 v111, v116, v117\n\tv_max3_u32 v111, v111, v118, v119\n\t"
               "s_waitcnt vmcnt(1)\n\tv_max3_u32 v110, v110, v120, v121\n\tv_max3_u32 v111, v111, v122, v123\n\t"
               "s_waitcnt vmcnt(0)\n\tv_max3_u32 v110, v110, v124, v125\n\tv_max3_u32 v111, v111, v126, v127\n\t"
               "v_max_u32 v110, v110, v111\n\tv_cmpx_eq_u32 vcc, -1, v110\n\t" POLL_TAIL
               : "=&{v[112:115]}"(a), "=&{v[116:119]}"(b), "=&{v[120:123]}"(c), "=&{v[124:127]}"(d), [n] "=&s"(n)
               : [o] "v"(off), [o2] "v"(off2), [r] "s"(r), [cap] "i"(SPIN_CAP) : "memory", "vcc", "scc", "v110", "v111");
}
__device__ __forceinline__ v2u poll2(rsrc_t r, int off) {
  v2u a; u32 n;
  asm volatile(POLL_HEAD "buffer_load_dwordx2 v[126:127], %[o], %[r], 0 offen sc1\n\ts_waitcnt vmcnt(0)\n\t"
               "v_max_u32 v125, v126, v127\n\tv_cmpx_eq_u32 vcc, -1, v125\n\ts_cbranch_execz 2f\n\ts_sleep 1\n\ts_sub_u32 %[n], %[n], 1\n\ts_cbranch_scc0 1b\n\t2:\n\ts_mov_b64 exec, -1\n\ts_nop 2"
               : "=&{v[126:127]}"(a), [n] "=&s"(n) : [o] "v"(off), [r] "s"(r), [cap] "i"(SPIN_CAP) : "memory", "vcc", "scc", "v125");
  return a;
}
template <int D> __device__ __forceinline__ void poll1x2(rsrc_t r, int off, u32& a, u32& b) {
  u32 n;
  asm volatile(POLL_HEAD "buffer_load_dword v126, %[o], %[r], 0 offen sc1\n\tbuffer_load_dword v127, %[o], %[r], 0 offen offset:%[d] sc1\n\ts_waitcnt vmcnt(0)\n\t"
               "v_max_u32 v125, v126, v127\n\tv_cmpx_eq_u32 vcc, -1, v125\n\t" POLL_TAIL
               : "=&{v126}"(a), "=&{v127}"(b), [n] "=&s"(n) : [o] "v"(off), [r] "s"(r), [cap] "i"(SPIN_CAP), [d] "i"(D) : "memory", "vcc", "scc", "v125");
}
__device__ __forceinline__ void pollT4(rsrc_t r, int off, u32 tag, u32 ntag, v4u& a, v4u& b, v4u& c, v4u& d) {
  u32 n;
  asm volatile(POLL_HEAD "buffer_load_dwordx4 v[112:115], %[o], %[r], 0 offen sc1\n\tbuffer_load_dwordx4 v[116:119], %[o], %[r], 0 offen offset:512 sc1\n\t"
               "buffer_load_dwordx4 v[120:123], %[o], %[r], 0 offen offset:1024 sc1\n\tbuffer_load_dwordx4 v[124:127], %[o], %[r], 0 offen offset:1536 sc1\n\t"
               "s_waitcnt vmcnt(3)\n\tv_xor_b32 v110, v112, %[t]\n\tv_xor_b32 v111, v114, %[u]\n\t"
               "s_waitcnt vmcnt(2)\n\tv_bitop3_b32 v110, v116, %[t], v110 bitop3:0xbe\n\tv_bitop3_b32 v111, v118, %[u], v111 bitop3:0xbe\n\t"
               "s_waitcnt vmcnt(1)\n\tv_bitop3_b32 v110, v120, %[t], v110 bitop3:0xbe\n\tv_bitop3_b32 v111, v122, %[u], v111 bitop3:0xbe\n\t"
               "s_waitcnt vmcnt(0)\n\tv_bitop3_b32 v110, v124, %[t], v110 bitop3:0xbe\n\tv_bitop3_b32 v111, v126, %[u], v111 bitop3:0xbe\n\t"
               "v_or_b32 v110, v110, v111\n\tv_cmpx_ne_u32 vcc, 0, v110\n\t" POLL_TAIL
               : "=&{v[112:115]}"(a), "=&{v[116:119]}"(b), "=&{v[120:123]}"(c), "=&{v[124:127]}"(d), [n] "=&s"(n)
               : [o] "v"(off), [r] "s"(r), [cap] "i"(SPIN_CAP), [t] "v"(tag), [u] "v"(ntag) : "memory", "vcc", "scc", "v110", "v111");
}
__device__ __forceinline__ void lds_wait_ge(volatile LAS u32* f, u32 want) {
  _Pragma("nounroll") for (u32 s = 0; s < (SPIN_CAP << 3); ++s) { if (*f >= want) break; __builtin_amdgcn_s_sleep(1); }
  MEMFENCE();
}
__device__ __forceinline__ void lds_inc(volatile LAS u32* f, int lane) {
  asm volatile("s_mov_b64 exec, 1\n\tds_add_u32 %0, %1\n\ts_mov_b64 exec, -1" :: "v"((u32)(uintptr_t)f), "v"(1u) : "memory");
}
__device__ __forceinline__ u32 lds_ticket(volatile LAS u32* f, int lane) {
  u32 old;
  asm volatile("s_mov_b64 exec, 1\n\tds_add_rtn_u32 %0, %1, %2\n\ts_mov_b64 exec, -1\n\ts_waitcnt lgkmcnt(0)" : "=&v"(old) : "v"((u32)(uintptr_t)f), "v"(1u) : "memory");
  return __builtin_amdgcn_readfirstlane(old);
}
__device__ __forceinline__ float sum8(const LAS float* r, int stride) {
  float v[8];
#pragma unroll
  for (int i = 0; i < 8; ++i) v[i] = r[i * stride];
  return ((v[0] + v[1]) + (v[2] + v[3])) + ((v[4] + v[5]) + (v[6] + v[7]));
}
__device__ __forceinline__ float silu(float v) { return v / (1.f + __expf(-v)); }
__device__ __forceinline__ u32 silu2(u32 c) { return pk(silu(blo(c)), silu(bhi(c))); }
__device__ __forceinline__ void ln_stats(v4u xv, float& mb, float& inv) {
  constexpr u32 ONE2 = 0x3f803f80u;
  const float s = dot2(xv[0], ONE2, dot2(xv[1], ONE2, 0.f)) + dot2(xv[2], ONE2, dot2(xv[3], ONE2, 0.f));
  const float q = dot2(xv[0], xv[0], dot2(xv[1], xv[1], 0.f)) + dot2(xv[2], xv[2], dot2(xv[3], xv[3], 0.f));
  float t = row_sum(tr32(s, q));
  t = tr16(t, t);
  const float mean = rdlf(t, 0) * (1.f / 512.f), sq = rdlf(t, 32) * (1.f / 512.f);
  float var = fmaf(-mean, mean, sq);
  if (__builtin_expect(!(var * 64.f >= sq), 0)) {
    float d = 0.f;
#pragma unroll
    for (int i = 0; i < 4; ++i) { const float a = blo(xv[i]) - mean, b = bhi(xv[i]) - mean; d = fmaf(a, a, fmaf(b, b, d)); }
    d = row_sum(tr32(d, d));
    var = tr16(d, d) * (1.f / 512.f);
  }
  mb = rbf(mean);
  inv = __builtin_amdgcn_rcpf(rbf(__builtin_amdgcn_sqrtf(rbf(rbf(var) + 1e-5f))));
}
__device__ __forceinline__ u32 ln_pair(u32 x, float mb, float inv) { return pk2(up2(pk2(up2(x) - mb)) * inv); }
__device__ __forceinline__ f32x2 one_plus(u32 c) { return up2(pk2(up2(c) + 1.f)); }
__device__ __forceinline__ u32 modulate(u32 y, f32x2 c1, f32x2 sh) { return pk2(up2(pk2(up2(y) * c1)) + sh); }
__device__ __forceinline__ int tile_voff(int lane, int stride) { const int rho = lane >> 3; return (8 * (rho >> 2) + (rho & 3)) * stride + (lane & 7) * 16; }
template <int AUX> __device__ __forceinline__ void tile_load(rsrc_t r, int voff, int soff, int stride, v4u* w) {
#pragma unroll
  for (int i = 0; i < 4; ++i) w[i] = ld4<AUX>(r, voff, soff + (16 * (i & 1) + 4 * (i >> 1)) * stride);
}
__device__ __forceinline__ void tile_frags(LAS unsigned char* tb, int lane, const v4u* w, v4u* f) {
  const int rho = lane >> 3;
  LAS unsigned char* wr = tb + rho * 128 + (((lane & 7) ^ (rho & 6)) << 4);
  const int row = 4 * (lane >> 4) + ((lane >> 2) & 3);
  const LAS unsigned char* rd = tb + row * 128 + (lane & 3) * 8;
  const int sw = (row >> 1) & 3;
  v2u h[2][4];
#pragma unroll
  for (int t = 0; t < 2; ++t) {
    *(LAS v4u*)wr = w[2 * t]; *(LAS v4u*)(wr + 1024) = w[2 * t + 1];
    MEMFENCE();
#pragma unroll
    for (int ct = 0; ct < 4; ++ct) h[t][ct] = __builtin_bit_cast(v2u, __builtin_amdgcn_ds_read_tr16_b64_v4i16((LAS s16x4*)(rd + ((ct ^ sw) << 5))));
    MEMFENCE();
  }
#pragma unroll
  for (int ct = 0; ct < 4; ++ct) f[ct] = v4u{h[0][ct].x, h[0][ct].y, h[1][ct].x, h[1][ct].y};
  PIN4(f);
}
__device__ __forceinline__ void lds_frags(const LAS unsigned char* tile, int lane, v4u* f) {
  const int row = 4 * (lane >> 4) + ((lane >> 2) & 3);
  const LAS unsigned char* rd = tile + row * 128 + (lane & 3) * 8;
  const int sw = (row >> 1) & 3;
#pragma unroll
  for (int ct = 0; ct < 4; ++ct) {
    const v2u lo = __builtin_bit_cast(v2u, __builtin_amdgcn_ds_read_tr16_b64_v4i16((LAS s16x4*)(rd + ((ct ^ sw) << 5))));
    const v2u hi = __builtin_bit_cast(v2u, __builtin_amdgcn_ds_read_tr16_b64_v4i16((LAS s16x4*)(rd + 2048 + ((ct ^ sw) << 5))));
    f[ct] = v4u{lo.x, lo.y, hi.x, hi.y};
  }
  PIN4(f);
}
__device__ __forceinline__ f32x4 mfma32(v4u a, v4u b, f32x4 c) { return __builtin_amdgcn_mfma_f32_16x16x32_bf16(__builtin_bit_cast(bf16x8, a), __builtin_bit_cast(bf16x8, b), c, 0, 0, 0); }
template <int NS> __device__ __forceinline__ float gemv(const v4u* a, const v4u* f, int g) {
  f32x4 acc[4];
#pragma unroll
  for (int ct = 0; ct < 4; ++ct) acc[ct] = mfma32(a[0], f[ct], f32x4{0.f, 0.f, 0.f, 0.f});
#pragma unroll
  for (int s = 1; s < NS; ++s)
#pragma unroll
    for (int ct = 0; ct < 4; ++ct) acc[ct] = mfma32(a[s], f[4 * s + ct], acc[ct]);
  const float lo = (g & 1) ? acc[1][0] : acc[0][0], hi = (g & 1) ? acc[3][0] : acc[2][0];
  return (g & 2) ? hi : lo;
}
template <int B> __device__ __forceinline__ v4u bcast4(u32 v) { return v4u{dppu<BC + B>(v), dppu<BC + B + 1>(v), dppu<BC + B + 2>(v), dppu<BC + B + 3>(v)}; }

__global__ void __launch_bounds__(NTHR, 4) fused_kernel(Params) {
  __shared__ __attribute__((aligned(128))) unsigned char lds_raw[L_END];
  LAS unsigned char* L = (LAS unsigned char*)lds_raw;
  v16u ka; v8u kb8;
  int wave; u32 tab, ko0, ko1, ko2, ko3, ep, d1; v4u xs;
  const u64 t0 = TNOW();
  {
    u32 sa, sb, sc, keep, va, vb; u64 pa, pc;
#define DMA4(P) "global_load_lds_dwordx4 %[k0], " P KV_SFX "\n\tglobal_load_lds_dwordx4 %[k1], " P " offset:1024" KV_SFX "\n\t" \
                "global_load_lds_dwordx4 %[k2], " P " offset:2048" KV_SFX "\n\tglobal_load_lds_dwordx4 %[k3], " P " offset:3072" KV_SFX "\n\t"
    asm volatile("s_getpc_b64 s[48:49]\n\t8:\n\ts_load_dwordx16 s[16:31], %[kp], 0x0\n\ts_load_dwordx8 s[36:43], %[kp], 0x40\n\t"
                 IXOP "s46, %[bid], " IXC "\n\ts_lshl_b32 s46, s46, 2\n\ts_add_u32 s46, s46, 0x60\n\ts_load_dword s46, %[kp], s46"
                 : "=&{s[16:31]}"(ka), "=&{s[36:43]}"(kb8), "=&{s46}"(tab), "=&{s[48:49]}"(pc) : [kp] "s"(__builtin_amdgcn_kernarg_segment_ptr()), [bid] "s"(blockIdx.x) : "scc");
    asm volatile(
      IXOP "%[sa], %[bid], " IXC "\n\t"
      "v_readfirstlane_b32 %[w], %[tid]\n\ts_mov_b32 %[keep], m0\n\t" XCOP "%[sc], %[bid], " XCC "\n\t"
      "s_lshr_b32 %[w], %[w], 6\n\ts_cmp_lt_u32 %[w], 8\n\ts_cbranch_scc1 1f\n\t"
      "v_bfe_u32 %[va], %[tid], 4, 2\n\tv_and_b32 %[vb], 15, %[tid]\n\tv_xor_b32 %[vb], %[vb], %[va]\n\tv_lshlrev_b32 %[vb], 4, %[vb]\n\t"
      "v_lshl_or_b32 %[k0], %[va], 8, %[vb]\n\tv_xor_b32 %[k1], 64, %[k0]\n\tv_xor_b32 %[k2], 0x80, %[k0]\n\tv_xor_b32 %[k3], 0xc0, %[k0]\n\t"
      "s_sub_u32 %[sb], %[w], 8\n\ts_lshl_b32 m0, %[sb], 14\n\ts_lshl_b32 %[sc], %[sc], 8\n\ts_add_u32 %[sb], %[sb], %[sc]\n\ts_lshl_b32 %[sb], %[sb], 1\n\t"
      "s_cmp_gt_u32 %[w], 11\n\ts_cselect_b32 %[sa], 0, %[sa]\n\ts_cmp_lt_u32 %[sa], 8\n\t"
      "s_waitcnt lgkmcnt(0)\n\ts_cbranch_scc1 9f\n\t"
      "s_and_b32 %[sc], s46, 0xffff\n\ts_add_u32 %[sb], %[sb], %[sc]\n\ts_lshl_b32 %[sb], %[sb], 13\n\t"
      "s_add_u32 s44, s18, %[sb]\n\ts_addc_u32 s45, s19, 0\n\t" DMA4("s[44:45]")
      "s_branch 9f\n\t"
      "1:\n\tv_bfe_u32 %[va], %[tid], 3, 3\n\tv_and_b32 %[vb], 6, %[va]\n\tv_xor_b32 %[vb], %[vb], %[tid]\n\tv_and_b32 %[vb], 7, %[vb]\n\tv_lshlrev_b32 %[vb], 4, %[vb]\n\t"
      "v_and_b32 %[k1], 4, %[va]\n\tv_add_u32 %[va], %[va], %[k1]\n\t"
      "v_and_b32 v106, 63, %[tid]\n\tv_lshlrev_b32 v106, 4, v106\n\tv_mov_b32 v104, 0\n\t"
      "s_cmp_eq_u32 %[w], 3\n\ts_cbranch_scc0 4f\n\t"
      "s_and_b32 s49, s49, 0xffff\n\ts_mov_b32 s50, 71f-8b\n\ts_max_u32 s50, s50, 72f-8b\n\ts_max_u32 s50, s50, 73f-8b\n\ts_mov_b32 s51, 0x20000\n\ts_and_b32 %[sb], " PFR ", 7\n\ts_lshl_b32 %[sb], %[sb], 10\n\t"
      "buffer_load_dwordx4 v[110:113], v106, s[48:51], %[sb] offen\n\ts_add_u32 %[sb], %[sb], 0x2000\n\t"
      "buffer_load_dwordx4 v[114:117], v106, s[48:51], %[sb] offen\n\ts_add_u32 %[sb], %[sb], 0x2000\n\t"
      "buffer_load_dwordx4 v[118:121], v106, s[48:51], %[sb] offen\n\t4:\n\t"
      "v_and_b32 v105, 3, %[tid]\n\tv_lshlrev_b32 v105, 2, v105\n\tv_and_or_b32 v105, %[tid], 48, v105\n\t"
      "s_cmp_lt_u32 %[sa], 8\n\ts_cbranch_scc0 2f\n\t"
      "v_lshl_or_b32 %[k0], %[va], 13, %[vb]\n\tv_add_u32 %[k1], 0x1fc00, %[k0]\n\tv_add_u32 %[k2], 0x7800, %[k0]\n\tv_add_u32 %[k3], 0x27400, %[k0]\n\t"
      "v_bfe_u32 %[va], %[tid], 2, 1\n\tv_lshl_or_b32 v105, %[va], 6, v105\n\ts_lshl_b32 %[sb], %[w], 7\n\tv_add_u32 v105, %[sb], v105\n\t"
      "s_lshl_b32 m0, %[w], 13\n\ts_lshl_b32 %[sb], %[w], 9\n\ts_add_u32 %[sb], %[sb], %[sc]\n\ts_lshl_b32 %[sb], %[sb], 3\n\ts_add_u32 %[sb], %[sb], %[sa]\n\ts_lshl_b32 %[sb], %[sb], 7\n\t"
      "s_waitcnt lgkmcnt(0)\n\t"
      "global_load_dword v105, v105, s[16:17]\n\tglobal_load_dwordx4 v[106:109], v106, s[16:17]\n\tglobal_load_dword v104, v104, s[42:43] sc1\n\t"
      "s_add_u32 s44, s22, %[sb]\n\ts_addc_u32 s45, s23, 0\n\t" DMA4("s[44:45]")
      "s_add_u32 s44, s44, 0x40000\n\ts_addc_u32 s45, s45, 0\n\ts_add_u32 m0, m0, 0x1000\n\ts_nop 0\n\t" DMA4("s[44:45]")
      "s_branch 9f\n\t"
      "2:\n\tv_mul_u32_u24 %[k0], 0x1800, %[va]\n\tv_add_u32 %[k0], %[k0], %[vb]\n\tv_add_u32 %[k1], 0x17c00, %[k0]\n\tv_add_u32 %[k2], 0x5800, %[k0]\n\tv_add_u32 %[k3], 0x1d400, %[k0]\n\t"
      "s_sub_u32 %[sb], %[sa], 23\n\ts_cmp_lt_u32 %[sb], 4\n\ts_cbranch_scc0 3f\n\t"
      "v_bfe_u32 %[va], %[tid], 2, 1\n\tv_lshl_or_b32 v105, %[va], 6, v105\n\ts_lshl_b32 s44, %[w], 7\n\tv_add_u32 v105, s44, v105\n\t"
      "s_lshl_b32 s44, %[w], 13\n\ts_add_u32 m0, s44, 0x10000\n\ts_lshl_b32 %[sc], %[sc], 2\n\ts_add_u32 %[sb], %[sb], %[sc]\n\ts_add_u32 %[sb], %[sb], 16\n\ts_lshl_b32 %[sb], %[sb], 7\n\t"
      "s_mul_i32 %[sc], %[w], 0x60000\n\ts_add_u32 %[sb], %[sb], %[sc]\n\t"
      "s_waitcnt lgkmcnt(0)\n\t"
      "global_load_dword v105, v105, s[36:37]\n\tglobal_load_dword v104, v104, s[42:43] sc1\n\t"
      "s_add_u32 s44, s38, %[sb]\n\ts_addc_u32 s45, s39, 0\n\t" DMA4("s[44:45]")
      "s_add_u32 s44, s44, 0x30000\n\ts_addc_u32 s45, s45, 0\n\ts_add_u32 m0, m0, 0x1000\n\ts_nop 0\n\t" DMA4("s[44:45]")
      "s_branch 9f\n\t3:\n\t"
      "s_and_b32 %[sb], %[sa], 3\n\ts_lshl_b32 %[sb], %[sb], 2\n\ts_and_b32 s44, %[w], 3\n\ts_add_u32 %[sb], %[sb], s44\n\t"
      "s_lshl_b32 s44, s44, 12\n\ts_add_u32 m0, s44, %[tb]\n\ts_lshl_b32 s44, %[sb], 6\n\tv_add_u32 v105, s44, v105\n\t"
      "s_mul_i32 %[sb], %[sb], 0x30000\n\ts_lshl_b32 %[sc], %[sc], 8\n\ts_add_u32 %[sb], %[sb], %[sc]\n\ts_bfe_u32 %[sc], %[sa], 0x10002\n\ts_lshl_b32 %[sc], %[sc], 7\n\ts_add_u32 %[sb], %[sb], %[sc]\n\t"
      "s_lshr_b32 %[sc], %[w], 2\n\ts_lshr_b32 %[sa], %[sa], 3\n\ts_cmp_eq_u32 %[sa], 1\n\ts_cselect_b32 %[sa], %[sc], 0\n\ts_cmp_eq_u32 %[sa], 1\n\t"
      "s_waitcnt lgkmcnt(0)\n\t"
      "global_load_dword v105, v105, s[36:37]\n\tglobal_load_dword v104, v104, s[42:43] sc1\n\ts_cbranch_scc0 9f\n\t"
      "s_add_u32 s44, s38, %[sb]\n\ts_addc_u32 s45, s39, 0\n\t" DMA4("s[44:45]")
      "9:\n\ts_mov_b32 m0, %[keep]"
      : "=&{s[44:45]}"(pa), "+{s[48:49]}"(pc), [w] "=&s"(wave), [k0] "=&v"(ko0), [k1] "=&v"(ko1), [k2] "=&v"(ko2), [k3] "=&v"(ko3),
        "=&{v104}"(ep), "=&{v105}"(d1), "=&{v[106:109]}"(xs), [sa] "=&s"(sa), [sb] "=&s"(sb), [sc] "=&s"(sc), [keep] "=&s"(keep), [va] "=&v"(va), [vb] "=&v"(vb)
      : "{s[16:31]}"(ka), "{s[36:43]}"(kb8), "{s46}"(tab), [bid] "s"(blockIdx.x), [tid] "v"(threadIdx.x), [tb] "i"(L_TB)
      : "memory", "scc", "s50", "s51", "v110", "v111", "v112", "v113", "v114", "v115", "v116", "v117", "v118", "v119", "v120", "v121");
    asm volatile("" : "+{s[16:31]}"(ka), "+{s[36:43]}"(kb8), "+{s46}"(tab));
  }
  const u64 t31 = TNOW();
  volatile LAS u32* FL = (volatile LAS u32*)(L + L_FLAG);
  LAS float* RED = (LAS float*)(L + L_RED);
  const int lane = threadIdx.x & 63;
  const int bid = blockIdx.x, xc = MAPL ? bid >> 5 : bid & 7, ix = MAPL ? bid & 31 : bid >> 3, rb = ix & 7;
  const int g = lane >> 4, n = lane & 15;
  const bool chain = ix < 8, wcu = (ix >> 3) == 1;
  const int toff = tab & 0xffff, nhalf = tab >> 16, ntask = (nhalf + 1) >> 1;
  const bool ninth = wave == 2 && ntask == 9;
  const int aw = ninth ? 8 : wave - 8;
  const bool head = (wave >> 2) == 2 && !chain, p1 = wcu && (wave >> 2) == 1;
  const u32 ldst = (u32)(uintptr_t)L + L_K + aw * 16384;
#define KGROUP(kb, lo) do { u32 keep_; \
    asm volatile("s_mov_b32 %0, m0\n\ts_mov_b32 m0, %6\n\ts_nop 0\n\t" \
                 "global_load_lds_dwordx4 %1, %5" KV_SFX "\n\t" \
                 "global_load_lds_dwordx4 %2, %5 offset:1024" KV_SFX "\n\t" \
                 "global_load_lds_dwordx4 %3, %5 offset:2048" KV_SFX "\n\t" \
                 "global_load_lds_dwordx4 %4, %5 offset:3072" KV_SFX "\n\t" \
                 "s_mov_b32 m0, %0" \
                 : "=&s"(keep_) : "v"(ko0), "v"(ko1), "v"(ko2), "v"(ko3), "s"(kb), "s"(ldst + (lo)) : "memory"); } while (0)
#define PTR(v, i) (((u64)(v)[2 * (i) + 1] << 32) | (v)[2 * (i)])
  Ptrs p;
  p.x = (const u16*)PTR(ka, 0); p.K = (const u16*)PTR(ka, 1); p.V = (const u16*)PTR(ka, 2); p.wq = (const u16*)PTR(ka, 3); p.wo = (const u16*)PTR(ka, 4);
  p.wg = (const u16*)PTR(ka, 5); p.wu = (const u16*)PTR(ka, 6); p.wd = (const u16*)PTR(ka, 7);
  p.c = (const u16*)PTR(kb8, 0); p.wmod = (const u16*)PTR(kb8, 1); p.out = (u16*)PTR(kb8, 2); p.ws = (void*)PTR(kb8, 3);
#define MKWS() const rsrc_t ws = mkr(p.ws, WS_BYTES)
  if (wave == 0 && lane < 16) FL[lane] = (lane == 0 && ix >= 16) ? 1u : 0u;
  BARRIER();

  MKWS();
  const bool gcu = ix >= 16 && ix < 23, mcu = ix >= 23 && ix < 27;
  u32 tag = 0;
  if (__builtin_expect(wave < 8 && !ninth, 1)) {
  if (wave >= 4 && !(chain || wcu || gcu || mcu)) return;
  __builtin_amdgcn_s_setprio(3);
  asm volatile("s_waitcnt vmcnt(8)\n\ts_cmp_eq_u32 %3, 2\n\ts_cbranch_scc1 1f\n\ts_waitcnt vmcnt(4)\n\ts_cmp_eq_u32 %3, 1\n\ts_cbranch_scc1 1f\n\ts_waitcnt vmcnt(0)\n\t1:"
               : "+{v104}"(ep), "+{v105}"(d1), "+{v[106:109]}"(xs) : "s"(__builtin_amdgcn_readfirstlane((chain || mcu) ? 2 : p1 ? 1 : 0)) : "memory", "scc");
  const int cg = xc * 2 + (rb >> 2), ks = rb & 3;
  const int pi = ((n >> 2) & 1) * 16 + g * 4 + (n & 3);
  tag = ep + 1u;
  if (__builtin_expect(p1, 1)) {
    v4u a = bcast4<0>(silu2(d1)), f[4];
    int ho = H0_OFF + (cg * 4 + ks) * 512 + lane * 8;
    PIN1(a); PIN1(ho);
    VM0();
    const u64 t1 = TNOW();
    lds_frags(L + L_TB + (wave & 3) * 4096, lane, f);
    RED[(wave & 3) * 64 + lane] = gemv<1>(&a, f, g);
    if (lds_ticket(&FL[3], lane) == 3u) {
      const float s = (RED[lane] + RED[64 + lane]) + (RED[128 + lane] + RED[192 + lane]);
      const float s_hi = dpp<SHL1>(s);
      if (!(lane & 1)) stc(ws, ho, v4u{tag, asu(s), ~tag, asu(s_hi)});
      MEMFENCE();
      if (lane == 0) FL[0] = 1u;
      STAMP(2);
    }
    if (wave == 4) STAMPV(1, t1);
  }
  if (__builtin_expect(chain, 1)) {
    float mb, inv;
    ln_stats(xs, mb, inv);
    f32x2 y = up2(ln_pair(d1, mb, inv));
    PIN1(y);
    VM0();
    v4u f[8];
    lds_frags(L + L_K + wave * 8192, lane, f); lds_frags(L + L_K + wave * 8192 + 4096, lane, f + 4);
    lds_inc(&FL[3], lane);
    SCHED();
    const u64 t27 = TNOW();
    v4u h0, h1, h2, h3;
    pollT4(ws, H0_OFF + (wave + (n >> 3) * 8) * 2048 + pi * 16, tag, ~tag, h0, h1, h2, h3);
    if (wave == 0) { STAMP(3); STAMPV(24, t31); STAMPV(27, t27); }
    const u32 md = pk((asf(h0.y) + asf(h1.y)) + (asf(h2.y) + asf(h3.y)), (asf(h0.w) + asf(h1.w)) + (asf(h2.w) + asf(h3.w)));
    const u32 ot = dppu<ROR8>(md), sa = (n & 8) ? ot : md, ca = (n & 8) ? md : ot;
    const u32 hp = pk2(up2(pk2(y * one_plus(ca))) + up2(sa));
    const v4u a[2] = {bcast4<0>(hp), bcast4<4>(hp)};
    RED[wave * 64 + lane] = gemv<2>(a, f, g);
    if (lds_ticket(&FL[7], lane) == 7u) {
      const float s = sum8(RED + lane, 64);
      const u32 d = pk(s, dpp<SHL1>(s)), d2 = dppu<SHL2>(d);
      if ((lane & 3) == 0) stc(ws, Q_OFF + (xc * 8 + rb) * 256 + lane * 4, v4u{tag, d, ~tag, d2});
      MEMFENCE();
      if (lane == 0) FL[0] = 1u;
      STAMP(4);
    }
  }
  if (mcu) {
    const u32 sp = silu2(d1);
    const v4u a[2] = {bcast4<0>(sp), bcast4<4>(sp)};
    VM0();
    v4u f[8];
    lds_frags(L + 65536 + wave * 8192, lane, f); lds_frags(L + 65536 + wave * 8192 + 4096, lane, f + 4);
    RED[wave * 64 + lane] = gemv<2>(a, f, g);
    const u32 tk = lds_ticket(&FL[3], lane);
    if (wave == 1) {
      lds_wait_ge(&FL[3], 8u);
      const float s = sum8(RED + lane, 64);
      const u32 d = nosent(pk(s, dpp<SHL1>(s)));
      lds_wait_ge(&FL[5], 1u + (tk & 0u));
      if (!(lane & 1)) stc1(ws, MR_OFF + (xc * 4 + (ix - 23)) * 128 + lane * 2, d);
      STAMP(22);
    }
  }
  if (wcu && wave == 1) {
    lds_wait_ge(&FL[0], 1u);
    const v4u sv = {SENT, SENT, SENT, SENT};
#pragma unroll
    for (int j = 0; j < CLR_KB; ++j) stc(ws, S_OFF + (xc * 8 + rb) * (CLR_KB * 1024) + j * 1024 + lane * 16, sv);
    VM0();
    if (lane == 0) stc2(ws, CF_OFF + (xc * 8 + rb) * CF_STRIDE, v2u{tag, ~tag});
  }
  if (wave < QPOLL) {
    const int qb = Q_OFF + xc * 2048 + lane * 16;
    if (!chain && !mcu) __builtin_amdgcn_s_sleep(Q_DELAY);
    for (int i = 0; i < wave; ++i) __builtin_amdgcn_s_sleep(QSTAG);
    _Pragma("nounroll") for (u32 s = 0; s < SPIN_CAP; ++s) {
      const v4u q0 = ldc(ws, qb), q1 = ldc(ws, qb + 1024);
      SCHED();
      if (__all((((q0.x ^ tag) | (q1.x ^ tag)) | ((q0.z ^ ~tag) | (q1.z ^ ~tag))) == 0u)) {
        LAS unsigned char* qd = L + L_Q + (lane >> 5) * 320 + (lane & 31) * 8;
        *(LAS v2u*)qd = v2u{q0.y, q0.w};
        *(LAS v2u*)(qd + 640) = v2u{q1.y, q1.w};
        LGKM0();
        if (lane == 0) FL[1] = 1u;
        STAMP(5);
        break;
      }
      if (FL[1] != 0u) break;
      MEMFENCE();
    }
    if (wave == 0) STAMPV(0, t0);
  }
  if (wave == 3) {
    {
      __builtin_amdgcn_s_sleep(CF_SLEEP);
      _Pragma("nounroll") for (u32 s = 0; s < SPIN_CAP; ++s) {
        const v2u cf = __builtin_amdgcn_raw_buffer_load_b64(ws, CF_OFF + lane * CF_STRIDE, 0, SC1);
        if (__all(((cf.x ^ tag) | (cf.y ^ ~tag)) == 0u)) break;
        __builtin_amdgcn_s_sleep(16);
        MEMFENCE();
      }
      if (lane == 0) FL[5] = 1u;
    }
  }
  }
  else {
    const bool early = aw < 4;
    if (ninth) {
      VM0();
      const u32 kv0 = g * 256 + ((n ^ g) << 4);
      ko0 = kv0; ko1 = kv0 ^ 64u; ko2 = kv0 ^ 128u; ko3 = kv0 ^ 192u;
    }
    if (aw >= ntask) return;
    if (chain) lds_wait_ge(CH_EARLY ? &FL[3] : &FL[0], CH_EARLY ? 8u : 1u);
    if (!early) {
      lds_wait_ge(&FL[3], (ix >= 23 && ix < 27) ? 8u : (wcu && ninth) ? 4u : 0u);
      _Pragma("nounroll") for (u32 s = 0; s < (SPIN_CAP << 3); ++s) { if (FL[2] >= 4u || FL[1] != 0u) break; __builtin_amdgcn_s_sleep(1); }
      MEMFENCE();
    }
    const int hh = n >> 2, e2 = n & 3;
    u32 qseen = (LATE_FREE && !early && ix >= 16) ? 1u : 0u;
    const u32 key0 = (u32)(xc * 16384 + toff * 32 + aw * 64);
    const bool halft = 2 * aw + 1 == nhalf;
#define THROTTLE() do { if (!qseen) { const v2u fl_ = *(volatile LAS v2u*)FL; qseen = __builtin_amdgcn_readfirstlane(fl_.y); \
      if (!qseen) { if (__builtin_amdgcn_readfirstlane(fl_.x)) asm volatile("s_waitcnt vmcnt(%0)" :: "i"(THR_W - 4) : "memory"); else asm volatile("s_waitcnt vmcnt(%0)" :: "i"(THR_Q - 4) : "memory"); } } SCHED(); } while (0)
    const u64 kb = (u64)p.K + (u64)key0 * 256;
    if (!head) KGROUP(kb, 0);
    THROTTLE(); KGROUP(kb + 4096, 4096);
    THROTTLE();
    if (!halft) { KGROUP(kb + 8192, 8192); THROTTLE(); KGROUP(kb + 12288, 12288); THROTTLE(); }
    v4u vv[16];
    {
      const rsrc_t rv = mkr(p.V, 8 * 16384 * 256);
      const int vo = (halft && g >= 2) ? 0x40000000 : g * 4096 + n * 16;
#pragma unroll
      for (int i = 0; i < 16; ++i) { vv[i] = ld4<KV_AUX>(rv, vo + i * 256, key0 * 256); if ((i & 3) == 3 && i < 15) THROTTLE(); }
    }
    SCHED();
    lds_inc(&FL[2], lane);
    const u64 tI = TNOW();
    lds_wait_ge(&FL[1], 1u);
    asm volatile("s_waitcnt vmcnt(16)" ::: "memory");
    SCHED();
    const u64 tK = TNOW();
    float s2[4], pe[4];
    {
      const LAS unsigned char* kbase = L + L_K + aw * 16384 + n * 256;
      const LAS unsigned char* qa = L + L_Q + (n & 3) * 320 + g * 16;
      f32x4 sa[4];
#pragma unroll
      for (int t = 0; t < 4; ++t) sa[t] = f32x4{0.f, 0.f, 0.f, 0.f};
#pragma unroll
      for (int s = 0; s < 4; ++s) {
        const v4u a = *(const LAS v4u*)(qa + 64 * s);
#pragma unroll
        for (int t = 0; t < 4; ++t) sa[t] = mfma32(a, *(const LAS v4u*)(kbase + t * 4096 + (((4 * s + g) ^ n) << 4)), sa[t]);
      }
      const f32x4 lo = (g & 1) ? sa[1] : sa[0], hi = (g & 1) ? sa[3] : sa[2], sacc = (g & 2) ? hi : lo;
      constexpr float SC = 0.08838834764831845f;
      const f32x2 c = up2(pk2(up2(pk(sacc[0], sacc[1])) * SC)), d = up2(pk2(up2(pk(sacc[2], sacc[3])) * SC));
      const bool dead = halft && g >= 2;
      s2[0] = dead ? -__builtin_inff() : c.x; s2[1] = dead ? -__builtin_inff() : c.y; s2[2] = dead ? -__builtin_inff() : d.x; s2[3] = dead ? -__builtin_inff() : d.y;
    }
    const float mrow = row_max(tm16(tm32(s2[0], s2[2]), tm32(s2[1], s2[3])));
#pragma unroll
    for (int h2 = 0; h2 < 4; ++h2) pe[h2] = __builtin_amdgcn_exp2f((s2[h2] - rdlf(mrow, 16 * h2)) * LOG2E);
    const u32 w0 = pk(pe[0], pe[1]), w1 = pk(pe[2], pe[3]);
    const float lrow = row_sum(tr16(tr32(pe[0], pe[2]), tr32(pe[1], pe[3])));
    if (n == 0) *(LAS f32x2*)(L + L_ML + g * 128 + aw * 8) = f32x2{mrow, lrow};
    MEMFENCE();
    lds_inc(&FL[4], lane);
    const u32 two = (u32)(2 * hh) | ((u32)(2 * hh + 1) << 8);
    const u32 selv = (e2 & 1) ? ((two << 16) | 0x0C0Cu) : (0x0C0C0000u | two);
    const u32 sel0 = (e2 >> 1) == 0 ? selv : 0x0C0C0C0Cu, sel1 = (e2 >> 1) == 1 ? selv : 0x0C0C0C0Cu;
    v2u xb[16];
#define PV_PREP(i) { const u32 b0_ = dppu<BC + (i)>(w0), b1_ = dppu<BC + (i)>(w1); xb[i] = v2u{__builtin_amdgcn_perm(b1_, b0_, sel0), __builtin_amdgcn_perm(b1_, b0_, sel1)}; }
    PV_PREP(0) PV_PREP(1) PV_PREP(2) PV_PREP(3) PV_PREP(4) PV_PREP(5) PV_PREP(6) PV_PREP(7)
    PV_PREP(8) PV_PREP(9) PV_PREP(10) PV_PREP(11) PV_PREP(12) PV_PREP(13) PV_PREP(14) PV_PREP(15)
#undef PV_PREP
    u32 obw = (u32)(uintptr_t)L + L_K + aw * 16384 + (hh * 128 + e2 * 16 + 4 * g) * 4;
    PIN1(obw);
    SCHED();
    f32x4 o1 = {0.f, 0.f, 0.f, 0.f}, o2 = {0.f, 0.f, 0.f, 0.f};
#pragma unroll
    for (int i = 0; i < 16; ++i) {
      o1 = __builtin_amdgcn_mfma_f32_16x16x16bf16_1k(__builtin_bit_cast(s16x4, v2u{vv[i].x, vv[i].y}), __builtin_bit_cast(s16x4, xb[i]), o1, 0, 0, 0);
      o2 = __builtin_amdgcn_mfma_f32_16x16x16bf16_1k(__builtin_bit_cast(s16x4, v2u{vv[i].z, vv[i].w}), __builtin_bit_cast(s16x4, xb[i]), o2, 0, 0, 0);
    }
    *(LAS f32x4*)(uintptr_t)obw = o1; *(LAS f32x4*)(uintptr_t)(obw + 256) = o2;
    if (aw < 2 || (aw > 3 && aw < 8) || (ninth && !gcu)) { BARRIER(); return; }
    if (!ninth) {
    const int tau = (aw - 2) * 64 + lane, h = tau >> 5;
    float f[9];
    lds_wait_ge(&FL[5], 1u);
    lds_wait_ge(&FL[4], (u32)ntask);
    {
      float m[10], l[10];
#pragma unroll
      for (int w = 0; w < 5; ++w) { const f32x4 t = *(const LAS f32x4*)(L + L_ML + h * 128 + w * 16); m[2 * w] = t.x; l[2 * w] = t.y; m[2 * w + 1] = t.z; l[2 * w + 1] = t.w; }
#pragma unroll
      for (int w = 4; w < 9; ++w) { m[w] = w < ntask ? m[w] : -__builtin_inff(); l[w] = w < ntask ? l[w] : 0.f; }
      const float M = mx(mx(mx(mx(m[0], m[1]), mx(m[2], m[3])), mx(mx(m[4], m[5]), mx(m[6], m[7]))), m[8]);
      float Ls = 0.f;
#pragma unroll
      for (int w = 0; w < 9; ++w) { f[w] = __builtin_amdgcn_exp2f((m[w] - M) * LOG2E); Ls = fmaf(l[w], f[w], Ls); PIN1(f[w]); }
      if ((lane & 31) == 0) stc2(ws, ML_OFF + ((xc * 32 + ix) * 2 + aw - 2) * 128 + (lane >> 5) * 8, v2u{asu(M), asu(Ls)});
    }
    int t1o = T1_OFF + (((xc * 4 + h) * 4 + ((tau >> 3) & 3)) * 32 + ix) * 128 + (tau & 7) * 16;
    u32 pa[9];
#pragma unroll
    for (int w = 0; w < 9; ++w) { pa[w] = (u32)(uintptr_t)(L + L_K) + tau * 16 + (w < ntask ? w : 0) * 16384; PIN1(pa[w]); }
    PIN1(t1o);
    BARRIER();
    if (aw == 2) STAMP(chain ? 12 : ix < 16 ? 23 : ix < 23 ? 25 : ix < 27 ? 26 : 28);
    f32x4 part[9];
#pragma unroll
    for (int w = 0; w < 9; ++w) part[w] = *(const LAS f32x4*)(uintptr_t)pa[w];
    const f32x4 acc = (((part[0] * f[0] + part[1] * f[1]) + (part[2] * f[2] + part[3] * f[3])) + ((part[4] * f[4] + part[5] * f[5]) + (part[6] * f[6] + part[7] * f[7]))) + part[8] * f[8];
    stc(ws, t1o, __builtin_bit_cast(v4u, acc));
    lds_inc(&FL[8], lane);
    if (aw == 2) STAMP(13);
    asm volatile("73:");
    return;
    }
    __builtin_amdgcn_s_setprio(3);
  }

  LAS unsigned char* tb = L + L_K + wave * 16384 + 8192;
  if (ix < 16) {
    if (wave < 4) return;
    const int x = wave & 3, hl = ix >> 2, hg = xc * 4 + hl, qd = ix & 3;
    const rsrc_t ro = mkr(p.wo, 4096 * 512 * 2);
    const int col = MAPL ? rb * 64 + xc * 8 : xc * 64 + rb * 8;
    lds_wait_ge(&FL[2], (u32)ntask);
    v4u w[8];
    {
      const int rho = lane >> 3, vo = (64 * (rho >> 2) + 8 * (rho & 3)) * 1024 + (lane & 7) * 16;
#pragma unroll
      for (int i = 0; i < 8; ++i) w[i] = ld4<W_AUX>(ro, vo, (hg * 128 + 32 * ((i >> 1) & 1) + 2 * qd + (i & 1)) * 1024 + x * 256 + (i >> 2) * 128);
    }
    BARRIER();
    lds_wait_ge(&FL[8], 2u);
    const v2u ma = poll2(ws, ML_OFF + ((xc * 32 + (lane >> 1)) * 2 + (hl >> 1)) * 128 + (hl & 1) * 8);
    v4u f[8];
    tile_frags(tb, lane, w, f); tile_frags(tb, lane, w + 4, f + 4);
    if (wave == 4) STAMP(30);
    int ob = T1_OFF + ((hg * 4 + qd) * 32 + (lane >> 1)) * 128 + x * 32 + (lane & 1) * 16;
    int t2o = T2_OFF + (xc * 16 + ix) * 2048 + x * 512 + lane * 4;
    float M = asf(ma.x);
    M = mx(M, dpp<XOR2>(M)); M = mx(M, dpp<ROR4>(M)); M = mx(M, dpp<ROR8>(M)); M = tm16(M, M); M = tm32(M, M);
    float fa = __builtin_amdgcn_exp2f((asf(ma.x) - M) * LOG2E);
    float Ls = fa * asf(ma.y);
    Ls = tr32(Ls, Ls); Ls = tr16(Ls, Ls); Ls += dpp<ROR8>(Ls); Ls += dpp<ROR4>(Ls); Ls += dpp<XOR2>(Ls);
    float inv = __builtin_amdgcn_rcpf(Ls);
    u32 oa = (u32)(uintptr_t)(L + L_X) + x * 16 + (lane & 1) * 8 + (lane >> 5) * 4;
    PIN1(fa); PIN1(inv); PIN1(ob); PIN1(t2o); PIN1(oa);
    SCHED();
    const v4u a = poll4(ws, ob);
    if (wave == 4) STAMP(14);
    float u0 = tr32(fa * asf(a.x), fa * asf(a.z)), u1 = tr32(fa * asf(a.y), fa * asf(a.w));
    u0 = tr16(u0, u0); u1 = tr16(u1, u1);
    u0 += dpp<ROR8>(u0); u1 += dpp<ROR8>(u1);
    u0 += dpp<ROR4>(u0); u1 += dpp<ROR4>(u1);
    u0 += dpp<XOR2>(u0); u1 += dpp<XOR2>(u1);
    if (!(lane & 30)) *(LAS u32*)(uintptr_t)oa = pk(u0 * inv, u1 * inv);
    BARRIER();
    const v4u av = *(const LAS v4u*)(L + L_X + g * 16);
    const float y0 = gemv<1>(&av, f, g), y1 = gemv<1>(&av, f + 4, g);
    stc_row(ws, t2o, y0, lane); stc_row(ws, t2o + 256, y1, lane);
    if (wave == 4) STAMP(15);
    if (!chain || wave != 4) return;
    f32x2 xin = up2(*(const GAS u32*)((const GAS u16*)p.x + col + 2 * g));
    u32 gaw, gbw;
    poll1x2<3072>(ws, MR_OFF + (col + 2 * g) * 2, gaw, gbw);
    f32x2 ga = up2(gaw), gb = up2(gbw);
    int tio = T2_OFF + lane * 2048 + col * 4, tio2 = tio + 131072, yo = lane < 56 ? Y_OFF + lane * 2048 + col * 4 : 0x40000000;
    PIN1(ga); PIN1(gb); PIN1(xin); PIN1(tio); PIN1(tio2); PIN1(yo);
    v4u pa, pb, pc, pd;
    poll4x4(ws, tio, tio2, pa, pb, pc, pd);
    STAMP(16);
    pa = __builtin_bit_cast(v4u, __builtin_bit_cast(f32x4, pa) + __builtin_bit_cast(f32x4, pc));
    pb = __builtin_bit_cast(v4u, __builtin_bit_cast(f32x4, pb) + __builtin_bit_cast(f32x4, pd));
    const u32 x1 = nosent(pk2(xin + up2(pk2(ga * up2(pk2(sum8x64(pa, pb)))))));
    const v4u o = {rdl(x1, 0), rdl(x1, 16), rdl(x1, 32), rdl(x1, 48)};
    if (lane == 0) stc(ws, X1_OFF + col * 16, o);
    STAMP(17);
    if (bid == 0 && lane == 0) stc1(ws, EPOCH_OFF, tag);
    f32x2 x1f = up2(x1);
    PIN1(x1f);
    poll4x2<16>(ws, yo, pa, pb);
    STAMP(20);
    const u32 d = pk2(x1f + up2(pk2(gb * up2(pk2(sum8x64(pa, pb))))));
    if (!n) *(GAS u32*)((GAS u16*)p.out + col + 2 * g) = d;
    STAMP(21);
    asm volatile("71:");
  } else if (gcu) {
    const int gi = MAPL ? (ix - 16) * 8 + xc : xc * 7 + (ix - 16);
    const rsrc_t rd = mkr(p.wd, 1792 * 512 * 2);
    BARRIER();
    lds_wait_ge(&FL[8], 2u);
    v4u w[12];
    {
      const int rho = lane >> 3;
      const GAS unsigned char* src = (const GAS unsigned char*)((lane & 4) ? p.wu : p.wg) + (size_t)(wave * 64 + 8 * (rho >> 2) + (rho & 3)) * 3584 + gi * 64 + (lane & 3) * 16;
#pragma unroll
      for (int i = 0; i < 8; ++i) w[i] = *(const GAS v4u*)(src + (32 * (i >> 2) + 16 * (i & 1) + 4 * ((i >> 1) & 1)) * 3584);
    }
    tile_load<W_AUX>(rd, tile_voff(lane, 1024), gi * 32 * 1024 + wave * 128, 1024, w + 8);
    v4u f[12];
    tile_frags(tb, lane, w, f); tile_frags(tb, lane, w + 4, f + 4); tile_frags(tb, lane, w + 8, f + 8);
    int yo = Y_OFF + gi * 2048 + wave * 256 + lane * 4;
    PIN1(yo);
    if (wave == 0) STAMP(29);
    SCHED();
    if (wave < XP) {
      constexpr int NP = 4 / XP;
      v4u sbw, cbw;
      poll4x2<1024>(ws, MR_OFF + 1024 + lane * 16, sbw, cbw);
      f32x2 sb[NP], c1[NP];
#pragma unroll
      for (int i = 0; i < NP; ++i) {
        const u32 s_ = XP == 1 ? sbw[i] : XP == 2 ? (wave ? sbw[2 + i] : sbw[i]) : ((wave & 2) ? ((wave & 1) ? sbw.w : sbw.z) : ((wave & 1) ? sbw.y : sbw.x));
        const u32 c_ = XP == 1 ? cbw[i] : XP == 2 ? (wave ? cbw[2 + i] : cbw[i]) : ((wave & 2) ? ((wave & 1) ? cbw.w : cbw.z) : ((wave & 1) ? cbw.y : cbw.x));
        sb[i] = up2(s_); c1[i] = one_plus(c_); PIN1(sb[i]); PIN1(c1[i]);
      }
      int xb1 = X1_OFF + lane * 128;
      PIN1(xb1);
      const v4u xs = poll4(ws, xb1);
      if (wave == 0) STAMP(18);
      float mb, inv;
      ln_stats(xs, mb, inv);
#pragma unroll
      for (int i = 0; i < NP; ++i) {
        const u32 xm = XP == 1 ? xs[i] : XP == 2 ? (wave ? xs[2 + i] : xs[i]) : ((wave & 2) ? ((wave & 1) ? xs.w : xs.z) : ((wave & 1) ? xs.y : xs.x));
        *(LAS u32*)(L + L_X + lane * 16 + (wave * NP + i) * 4) = modulate(ln_pair(xm, mb, inv), c1[i], sb[i]);
      }
    }
    BARRIER();
    const v4u av[2] = {*(const LAS v4u*)(L + L_X + wave * 128 + g * 16), *(const LAS v4u*)(L + L_X + wave * 128 + 64 + g * 16)};
    RED[wave * 64 + lane] = gemv<2>(av, f, g);
    BARRIER();
    const float s = rbf(sum8(RED + (n >> 3) * 32 + g * 8 + (n & 7), 64));
    const float gt = s, up = dpp<ROR8>(s);
    const float u2 = (-2.f * 0.7978845608028654f * LOG2E) * (gt + 0.044715f * gt * gt * gt);
    const float gv = rbf(gt * __builtin_amdgcn_rcpf(1.f + __builtin_amdgcn_exp2f(u2))) * up;
    const u32 d = pk(gv, dpp<SHL1>(gv));
    const v4u ag = {dppu<BC + 0>(d), dppu<BC + 2>(d), dppu<BC + 4>(d), dppu<BC + 6>(d)};
    stc_row(ws, yo, gemv<1>(&ag, f + 8, g), lane);
    if (wave == 0) STAMP(19);
    asm volatile("72:");
  }
}
}

extern "C" void kernel_launch(void* const* d_in, const int* in_sizes, int n_in, void* d_out, int out_size, void* d_ws, size_t ws_size, hipStream_t stream) {
  Params a;
  Ptrs& p = a.p;
  p.x = (const u16*)d_in[0]; p.K = (const u16*)d_in[1]; p.V = (const u16*)d_in[2]; p.wq = (const u16*)d_in[3]; p.wo = (const u16*)d_in[4];
  p.wg = (const u16*)d_in[6]; p.wu = (const u16*)d_in[7]; p.wd = (const u16*)d_in[8]; p.c = (const u16*)d_in[9]; p.wmod = (const u16*)d_in[10];
  p.out = (u16*)d_out; p.ws = d_ws;
  for (int i = 0; i < 32; ++i) a.tab[i] = (u32)toff_of(i) | ((u32)nhalf_of(i) << 16);
  hipLaunchKernelGGL(fused_kernel, dim3(NWG), dim3(NTHR), 0, stream, a);
}
```

```cpp
#include <hip/hip_runtime.h>
#include <stdint.h>

namespace {
typedef unsigned short u16;
typedef unsigned u32;
typedef unsigned long long u64;
typedef u32 v4u __attribute__((ext_vector_type(4)));
typedef u32 v2u __attribute__((ext_vector_type(2)));
typedef u32 v8u __attribute__((ext_vector_type(8)));
typedef u32 v16u __attribute__((ext_vector_type(16)));
typedef float f32x4 __attribute__((ext_vector_type(4)));
typedef float f32x2 __attribute__((ext_vector_type(2)));
typedef __bf16 bf16x2 __attribute__((ext_vector_type(2)));
typedef __bf16 bf16x8 __attribute__((ext_vector_type(8)));
typedef short s16x4 __attribute__((ext_vector_type(4)));
#define GAS __attribute__((address_space(1)))
#define LAS __attribute__((address_space(3)))

#define KV_SFX " nt"
constexpr int NT = 2;
constexpr int THR_W = 8;
constexpr int THR_Q = 6;
constexpr int H_W = 17, H_G = 18, H_M = 16, H_P = 18;

constexpr int NWG = 256, NTHR = 1024;
constexpr u32 SENT = 0xFFFFFFFFu;
constexpr int SC1 = 16;
constexpr u32 SPIN_CAP = 1u << 17;
constexpr float LOG2E = 1.4426950408889634f;
constexpr int EPOCH_OFF = 0;
constexpr int CF_OFF = 8192;
constexpr int Q_OFF = 16384;
constexpr int H0_OFF = 32768;
constexpr int S_OFF = 65536;
constexpr int T1_OFF = S_OFF;
constexpr int ML_OFF = T1_OFF + 524288;
constexpr int T2_OFF = ML_OFF + 65536;
constexpr int Y_OFF = T2_OFF + 262144;
constexpr int X1_OFF = Y_OFF + 114688;
constexpr int MR_OFF = X1_OFF + 8192;
constexpr int CLR_KB = 16;
constexpr int S_END = S_OFF + 64 * CLR_KB * 1024;
static_assert(MR_OFF + 4096 <= S_END, "");
constexpr int WS_BYTES = S_END;
constexpr int L_K = 0, L_TB = 8 * 16384, L_Q = 9 * 16384, L_FLAG = L_Q + 1280, L_ML = L_FLAG + 64, L_X = L_ML + 512, L_RED = L_X + 1024, L_END = L_RED + 4096;
static_assert(L_END <= 163840 && L_RED % 16 == 0 && L_TB % 16 == 0, "");
constexpr int CH_ALL = 512 - 8 * H_W - 7 * H_G - 4 * H_M - 5 * H_P, H_CH = CH_ALL / 8, CH_HI = CH_ALL - 8 * H_CH;
constexpr int nhalf_of(int ix) { return ix < 8 ? (ix < CH_HI ? H_CH + 1 : H_CH) : ix < 16 ? H_W : ix < 23 ? H_G : ix < 27 ? H_M : H_P; }
constexpr int toff_of(int ix) { int o = 0; for (int i = 0; i < ix; ++i) o += nhalf_of(i); return o; }
static_assert(toff_of(32) == 512 && H_G >= 8 && H_G <= 18 && H_CH >= 8 && H_CH + 1 <= 16 && H_M >= 8 && H_M <= 16 && H_W >= 8 && H_W <= 18 && H_P >= 8 && H_P <= 18, "");

struct Ptrs {
  const u16 *x, *K, *V, *wq, *wo, *wg, *wu, *wd, *c, *wmod;
  u16* out;
  void* ws;
};
struct Params { Ptrs p; u32 tab[32]; };
static_assert(sizeof(Params) == 96 + 128 && L_K == 0, "");

__device__ __forceinline__ float asf(u32 x) { return __builtin_bit_cast(float, x); }
__device__ __forceinline__ u32 asu(float x) { return __builtin_bit_cast(u32, x); }
__device__ __forceinline__ float blo(u32 w) { return asf(w << 16); }
__device__ __forceinline__ float bhi(u32 w) { return asf(w & 0xffff0000u); }
__device__ __forceinline__ u32 pk2(f32x2 v) { return __builtin_bit_cast(u32, __builtin_convertvector(v, bf16x2)); }
__device__ __forceinline__ u32 pk(float lo, float hi) { return pk2(f32x2{lo, hi}); }
__device__ __forceinline__ f32x2 up2(u32 w) { return f32x2{blo(w), bhi(w)}; }
__device__ __forceinline__ float rbf(float f) { return (float)(__bf16)f; }
__device__ __forceinline__ float dot2(u32 a, u32 b, float c) { return __builtin_amdgcn_fdot2_f32_bf16(__builtin_bit_cast(bf16x2, a), __builtin_bit_cast(bf16x2, b), c, false); }
template <int CTRL> __device__ __forceinline__ float dpp(float x) { return asf(__builtin_amdgcn_mov_dpp(asu(x), CTRL, 0xf, 0xf, true)); }
template <int CTRL> __device__ __forceinline__ u32 dppu(u32 x) { return __builtin_amdgcn_mov_dpp(x, CTRL, 0xf, 0xf, true); }
constexpr int XOR1 = 0xB1, XOR2 = 0x4E, XOR7 = 0x141, ROR8 = 0x128, ROR4 = 0x124, SHL1 = 0x101, SHL2 = 0x102, BC = 0x150;
__device__ __forceinline__ float mx(float a, float b) { return __builtin_amdgcn_fmed3f(a, b, __builtin_inff()); }
__device__ __forceinline__ float tr32(float a, float b) { auto s = __builtin_amdgcn_permlane32_swap(asu(a), asu(b), false, false); return asf(s[0]) + asf(s[1]); }
__device__ __forceinline__ float tr16(float a, float b) { auto s = __builtin_amdgcn_permlane16_swap(asu(a), asu(b), false, false); return asf(s[0]) + asf(s[1]); }
__device__ __forceinline__ float tm32(float a, float b) { auto s = __builtin_amdgcn_permlane32_swap(asu(a), asu(b), false, false); return mx(asf(s[0]), asf(s[1])); }
__device__ __forceinline__ float tm16(float a, float b) { auto s = __builtin_amdgcn_permlane16_swap(asu(a), asu(b), false, false); return mx(asf(s[0]), asf(s[1])); }
__device__ __forceinline__ float row_sum(float v) { v += dpp<ROR8>(v); v += dpp<XOR7>(v); v += dpp<XOR2>(v); v += dpp<XOR1>(v); return v; }
__device__ __forceinline__ float row_max(float v) { v = mx(v, dpp<ROR8>(v)); v = mx(v, dpp<XOR7>(v)); v = mx(v, dpp<XOR2>(v)); v = mx(v, dpp<XOR1>(v)); return v; }
__device__ __forceinline__ f32x2 sum8x64(v4u a, v4u b) {
  const float y0 = tr32(asf(a.x), asf(b.x)), y1 = tr32(asf(a.y), asf(b.y)), y2 = tr32(asf(a.z), asf(b.z)), y3 = tr32(asf(a.w), asf(b.w));
  return f32x2{row_sum(tr16(y0, y2)), row_sum(tr16(y1, y3))};
}
__device__ __forceinline__ u32 rdl(u32 v, int l) { return __builtin_amdgcn_readlane(v, l); }
__device__ __forceinline__ float rdlf(float v, int l) { return asf(__builtin_amdgcn_readlane(asu(v), l)); }
__device__ __forceinline__ u32 nosent(u32 w) { return w < 0xFFFFFFFEu ? w : 0xFFFFFFFEu; }
typedef __amdgpu_buffer_rsrc_t rsrc_t;
__device__ __forceinline__ rsrc_t mkr(const void* q, int bytes) { return __builtin_amdgcn_make_buffer_rsrc((void*)q, 0, bytes, 0x00020000); }
template <int AUX> __device__ __forceinline__ v4u ld4(rsrc_t r, int off, int so) { return __builtin_amdgcn_raw_buffer_load_b128(r, off, so, AUX); }
__device__ __forceinline__ u32 ld1(rsrc_t r, int off, int so) { return __builtin_amdgcn_raw_buffer_load_b32(r, off, so, 0); }
__device__ __forceinline__ v4u ldc(rsrc_t r, int off) { return __builtin_amdgcn_raw_buffer_load_b128(r, off, 0, SC1); }
__device__ __forceinline__ u32 ldc1(rsrc_t r, int off) { return __builtin_amdgcn_raw_buffer_load_b32(r, off, 0, SC1); }
__device__ __forceinline__ void stc(rsrc_t r, int off, v4u v) { __builtin_amdgcn_raw_buffer_store_b128(v, r, off, 0, SC1); }
__device__ __forceinline__ void stc2(rsrc_t r, int off, v2u v) { __builtin_amdgcn_raw_buffer_store_b64(v, r, off, 0, SC1); }
__device__ __forceinline__ void stc_row(rsrc_t r, int off, float v, int lane) {
  const v4u o = {asu(v), asu(dpp<0x55>(v)), asu(dpp<0xAA>(v)), asu(dpp<0xFF>(v))};
  if (!(lane & 3)) stc(r, off, o);
}
__device__ __forceinline__ void stc1(rsrc_t r, int off, u32 v) { __builtin_amdgcn_raw_buffer_store_b32(v, r, off, 0, SC1); }
#define MEMFENCE() asm volatile("" ::: "memory")
#define LGKM0() asm volatile("s_waitcnt lgkmcnt(0)" ::: "memory")
#define VM0() asm volatile("s_waitcnt vmcnt(0)" ::: "memory")
#define SCHED() __builtin_amdgcn_sched_barrier(0)
#define BARRIER() do { LGKM0(); SCHED(); __builtin_amdgcn_s_barrier(); MEMFENCE(); SCHED(); } while (0)
#define PIN1(a) asm volatile("" : "+v"(a))
#define PIN4(a) asm volatile("" : "+v"((a)[0]), "+v"((a)[1]), "+v"((a)[2]), "+v"((a)[3]))
#define PIN8(a) asm volatile("" : "+v"((a)[0]), "+v"((a)[1]), "+v"((a)[2]), "+v"((a)[3]), "+v"((a)[4]), "+v"((a)[5]), "+v"((a)[6]), "+v"((a)[7]))

#define POLL_HEAD "s_mov_b32 %[n], %[cap]\n\t1:\n\t"
#define POLL_TAIL "s_cbranch_execz 2f\n\ts_sub_u32 %[n], %[n], 1\n\ts_cbranch_scc0 1b\n\t2:\n\ts_mov_b64 exec, -1\n\ts_nop 2"
__device__ __forceinline__ v4u poll4(rsrc_t r, int off) {
  v4u a; u32 n;
  asm volatile(POLL_HEAD "buffer_load_dwordx4 v[124:127], %[o], %[r], 0 offen sc1\n\ts_waitcnt vmcnt(0)\n\t"
               "v_max_u32 v123, v124, v125\n\tv_max3_u32 v123, v123, v126, v127\n\tv_cmpx_eq_u32 vcc, -1, v123\n\t" POLL_TAIL
               : "=&{v[124:127]}"(a), [n] "=&s"(n) : [o] "v"(off), [r] "s"(r), [cap] "i"(SPIN_CAP) : "memory", "vcc", "scc", "v123");
  return a;
}
template <int D> __device__ __forceinline__ void poll4x2(rsrc_t r, int off, v4u& a, v4u& b) {
  u32 n;
  asm volatile(POLL_HEAD "buffer_load_dwordx4 v[120:123], %[o], %[r], 0 offen sc1\n\tbuffer_load_dwordx4 v[124:127], %[o], %[r], 0 offen offset:%[d] sc1\n\ts_waitcnt vmcnt(1)\n\t"
               "v_max_u32 v119, v120, v121\n\tv_max3_u32 v119, v119, v122, v123\n\ts_waitcnt vmcnt(0)\n\tv_max3_u32 v119, v119, v124, v125\n\tv_max3_u32 v119, v119, v126, v127\n\t"
               "v_cmpx_eq_u32 vcc, -1, v119\n\t" POLL_TAIL
               : "=&{v[120:123]}"(a), "=&{v[124:127]}"(b), [n] "=&s"(n) : [o] "v"(off), [r] "s"(r), [cap] "i"(SPIN_CAP), [d] "i"(D) : "memory", "vcc", "scc", "v119");
}
__device__ __forceinline__ void poll4x4(rsrc_t r, int off, int off2, v4u& a, v4u& b, v4u& c, v4u& d) {
  u32 n;
  asm volatile(POLL_HEAD "buffer_load_dwordx4 v[112:115], %[o], %[r], 0 offen sc1\n\tbuffer_load_dwordx4 v[116:119], %[o], %[r], 0 offen offset:16 sc1\n\t"
               "buffer_load_dwordx4 v[120:123], %[o2], %[r], 0 offen sc1\n\tbuffer_load_dwordx4 v[124:127], %[o2], %[r], 0 offen offset:16 sc1\n\t"
               "s_waitcnt vmcnt(3)\n\tv_max_u32 v110, v112, v113\n\tv_max3_u32 v110, v110, v114, v115\n\t"
               "s_waitcnt vmcnt(2)\n\tv_max_u32 v111, v116, v117\n\tv_max3_u32 v111, v111, v118, v119\n\t"
               "s_waitcnt vmcnt(1)\n\tv_max3_u32 v110, v110, v120, v121\n\tv_max3_u32 v111, v111, v122, v123\n\t"
               "s_waitcnt vmcnt(0)\n\tv_max3_u32 v110, v110, v124, v125\n\tv_max3_u32 v111, v111, v126, v127\n\t"
               "v_max_u32 v110, v110, v111\n\tv_cmpx_eq_u32 vcc, -1, v110\n\t" POLL_TAIL
               : "=&{v[112:115]}"(a), "=&{v[116:119]}"(b), "=&{v[120:123]}"(c), "=&{v[124:127]}"(d), [n] "=&s"(n)
               : [o] "v"(off), [o2] "v"(off2), [r] "s"(r), [cap] "i"(SPIN_CAP) : "memory", "vcc", "scc", "v110", "v111");
}
__device__ __forceinline__ v2u poll2(rsrc_t r, int off) {
  v2u a; u32 n;
  asm volatile(POLL_HEAD "buffer_load_dwordx2 v[126:127], %[o], %[r], 0 offen sc1\n\ts_waitcnt vmcnt(0)\n\t"
               "v_max_u32 v125, v126, v127\n\tv_cmpx_eq_u32 vcc, -1, v125\n\ts_cbranch_execz 2f\n\ts_sleep 1\n\ts_sub_u32 %[n], %[n], 1\n\ts_cbranch_scc0 1b\n\t2:\n\ts_mov_b64 exec, -1\n\ts_nop 2"
               : "=&{v[126:127]}"(a), [n] "=&s"(n) : [o] "v"(off), [r] "s"(r), [cap] "i"(SPIN_CAP) : "memory", "vcc", "scc", "v125");
  return a;
}
template <int D> __device__ __forceinline__ void poll1x2(rsrc_t r, int off, u32& a, u32& b) {
  u32 n;
  asm volatile(POLL_HEAD "buffer_load_dword v126, %[o], %[r], 0 offen sc1\n\tbuffer_load_dword v127, %[o], %[r], 0 offen offset:%[d] sc1\n\ts_waitcnt vmcnt(0)\n\t"
               "v_max_u32 v125, v126, v127\n\tv_cmpx_eq_u32 vcc, -1, v125\n\t" POLL_TAIL
               : "=&{v126}"(a), "=&{v127}"(b), [n] "=&s"(n) : [o] "v"(off), [r] "s"(r), [cap] "i"(SPIN_CAP), [d] "i"(D) : "memory", "vcc", "scc", "v125");
}
__device__ __forceinline__ void pollT4(rsrc_t r, int off, u32 tag, u32 ntag, v4u& a, v4u& b, v4u& c, v4u& d) {
  u32 n;
  asm volatile(POLL_HEAD "buffer_load_dwordx4 v[112:115], %[o], %[r], 0 offen sc1\n\tbuffer_load_dwordx4 v[116:119], %[o], %[r], 0 offen offset:512 sc1\n\t"
               "buffer_load_dwordx4 v[120:123], %[o], %[r], 0 offen offset:1024 sc1\n\tbuffer_load_dwordx4 v[124:127], %[o], %[r], 0 offen offset:1536 sc1\n\t"
               "s_waitcnt vmcnt(3)\n\tv_xor_b32 v110, v112, %[t]\n\tv_xor_b32 v111, v114, %[u]\n\t"
               "s_waitcnt vmcnt(2)\n\tv_bitop3_b32 v110, v116, %[t], v110 bitop3:0xbe\n\tv_bitop3_b32 v111, v118, %[u], v111 bitop3:0xbe\n\t"
               "s_waitcnt vmcnt(1)\n\tv_bitop3_b32 v110, v120, %[t], v110 bitop3:0xbe\n\tv_bitop3_b32 v111, v122, %[u], v111 bitop3:0xbe\n\t"
               "s_waitcnt vmcnt(0)\n\tv_bitop3_b32 v110, v124, %[t], v110 bitop3:0xbe\n\tv_bitop3_b32 v111, v126, %[u], v111 bitop3:0xbe\n\t"
               "v_or_b32 v110, v110, v111\n\tv_cmpx_ne_u32 vcc, 0, v110\n\t" POLL_TAIL
               : "=&{v[112:115]}"(a), "=&{v[116:119]}"(b), "=&{v[120:123]}"(c), "=&{v[124:127]}"(d), [n] "=&s"(n)
               : [o] "v"(off), [r] "s"(r), [cap] "i"(SPIN_CAP), [t] "v"(tag), [u] "v"(ntag) : "memory", "vcc", "scc", "v110", "v111");
}
__device__ __forceinline__ void lds_wait_ge(volatile LAS u32* f, u32 want) {
  _Pragma("nounroll") for (u32 s = 0; s < (SPIN_CAP << 3); ++s) { if (*f >= want) break; __builtin_amdgcn_s_sleep(1); }
  MEMFENCE();
}
__device__ __forceinline__ void lds_inc(volatile LAS u32* f, int lane) {
  asm volatile("s_mov_b64 exec, 1\n\tds_add_u32 %0, %1\n\ts_mov_b64 exec, -1" :: "v"((u32)(uintptr_t)f), "v"(1u) : "memory");
}
__device__ __forceinline__ u32 lds_ticket(volatile LAS u32* f, int lane) {
  u32 old;
  asm volatile("s_mov_b64 exec, 1\n\tds_add_rtn_u32 %0, %1, %2\n\ts_mov_b64 exec, -1\n\ts_waitcnt lgkmcnt(0)" : "=&v"(old) : "v"((u32)(uintptr_t)f), "v"(1u) : "memory");
  return __builtin_amdgcn_readfirstlane(old);
}
__device__ __forceinline__ float sum8(u32 addr) {
  const f32x4 t = *(const LAS f32x4*)(uintptr_t)addr + *(const LAS f32x4*)(uintptr_t)(addr + 16);
  return (t.x + t.z) + (t.y + t.w);
}
__device__ __forceinline__ float silu(float v) { return v / (1.f + __expf(-v)); }
__device__ __forceinline__ u32 silu2(u32 c) { return pk(silu(blo(c)), silu(bhi(c))); }
__device__ __forceinline__ void ln_stats(v4u xv, float& mb, float& inv) {
  constexpr u32 ONE2 = 0x3f803f80u;
  const float s = dot2(xv[0], ONE2, dot2(xv[1], ONE2, dot2(xv[2], ONE2, dot2(xv[3], ONE2, 0.f))));
  const float q = dot2(xv[0], xv[0], dot2(xv[1], xv[1], dot2(xv[2], xv[2], dot2(xv[3], xv[3], 0.f))));
  float t = row_sum(tr32(s, q));
  t = tr16(t, t);
  const float mean = rdlf(t, 0) * (1.f / 512.f), sq = rdlf(t, 32) * (1.f / 512.f);
  float var = fmaf(-mean, mean, sq);
  if (__builtin_expect(!(var * 64.f >= sq), 0)) {
    float d = 0.f;
#pragma unroll
    for (int i = 0; i < 4; ++i) { const float a = blo(xv[i]) - mean, b = bhi(xv[i]) - mean; d = fmaf(a, a, fmaf(b, b, d)); }
    d = row_sum(tr32(d, d));
    var = tr16(d, d) * (1.f / 512.f);
  }
  mb = rbf(mean);
  inv = __builtin_amdgcn_rcpf(rbf(__builtin_amdgcn_sqrtf(rbf(rbf(var) + 1e-5f))));
}
__device__ __forceinline__ u32 ln_pair(u32 x, float mb, float inv) { return pk2(up2(pk2(up2(x) - mb)) * inv); }
__device__ __forceinline__ f32x2 one_plus(u32 c) { return up2(pk2(up2(c) + 1.f)); }
__device__ __forceinline__ u32 modulate(u32 y, f32x2 c1, f32x2 sh) { return pk2(up2(pk2(up2(y) * c1)) + sh); }
__device__ __forceinline__ int tile_voff(int lane, int stride) { const int rho = lane >> 3; return (8 * (rho >> 2) + (rho & 3)) * stride + (lane & 7) * 16; }
template <int AUX> __device__ __forceinline__ void tile_load(rsrc_t r, int voff, int soff, int stride, v4u* w) {
#pragma unroll
  for (int i = 0; i < 4; ++i) w[i] = ld4<AUX>(r, voff, soff + (16 * (i & 1) + 4 * (i >> 1)) * stride);
}
__device__ __forceinline__ void tile_frags(LAS unsigned char* tb, int lane, const v4u* w, v4u* f) {
  const int rho = lane >> 3;
  LAS unsigned char* wr = tb + rho * 128 + (((lane & 7) ^ (rho & 6)) << 4);
  const int row = 4 * (lane >> 4) + ((lane >> 2) & 3);
  const LAS unsigned char* rd = tb + row * 128 + (lane & 3) * 8;
  const int sw = (row >> 1) & 3;
  v2u h[2][4];
#pragma unroll
  for (int t = 0; t < 2; ++t) {
    *(LAS v4u*)wr = w[2 * t]; *(LAS v4u*)(wr + 1024) = w[2 * t + 1];
    MEMFENCE();
#pragma unroll
    for (int ct = 0; ct < 4; ++ct) h[t][ct] = __builtin_bit_cast(v2u, __builtin_amdgcn_ds_read_tr16_b64_v4i16((LAS s16x4*)(rd + ((ct ^ sw) << 5))));
    MEMFENCE();
  }
#pragma unroll
  for (int ct = 0; ct < 4; ++ct) f[ct] = v4u{h[0][ct].x, h[0][ct].y, h[1][ct].x, h[1][ct].y};
  PIN4(f);
}
__device__ __forceinline__ void lds_frags(const LAS unsigned char* tile, int lane, v4u* f) {
  const int row = 4 * (lane >> 4) + ((lane >> 2) & 3);
  const LAS unsigned char* rd = tile + row * 128 + (lane & 3) * 8;
  const int sw = (row >> 1) & 3;
#pragma unroll
  for (int ct = 0; ct < 4; ++ct) {
    const v2u lo = __builtin_bit_cast(v2u, __builtin_amdgcn_ds_read_tr16_b64_v4i16((LAS s16x4*)(rd + ((ct ^ sw) << 5))));
    const v2u hi = __builtin_bit_cast(v2u, __builtin_amdgcn_ds_read_tr16_b64_v4i16((LAS s16x4*)(rd + 2048 + ((ct ^ sw) << 5))));
    f[ct] = v4u{lo.x, lo.y, hi.x, hi.y};
  }
  PIN4(f);
}
__device__ __forceinline__ f32x4 mfma32(v4u a, v4u b, f32x4 c) { return __builtin_amdgcn_mfma_f32_16x16x32_bf16(__builtin_bit_cast(bf16x8, a), __builtin_bit_cast(bf16x8, b), c, 0, 0, 0); }
template <int NS> __device__ __forceinline__ float gemv(const v4u* a, const v4u* f, int g) {
  f32x4 acc[4];
#pragma unroll
  for (int ct = 0; ct < 4; ++ct) acc[ct] = mfma32(a[0], f[ct], f32x4{0.f, 0.f, 0.f, 0.f});
#pragma unroll
  for (int s = 1; s < NS; ++s)
#pragma unroll
    for (int ct = 0; ct < 4; ++ct) acc[ct] = mfma32(a[s], f[4 * s + ct], acc[ct]);
  PIN4(acc);
  const float lo = (g & 1) ? acc[1][0] : acc[0][0], hi = (g & 1) ? acc[3][0] : acc[2][0];
  return (g & 2) ? hi : lo;
}
template <int B> __device__ __forceinline__ v4u bcast4(u32 v) { return v4u{dppu<BC + B>(v), dppu<BC + B + 1>(v), dppu<BC + B + 2>(v), dppu<BC + B + 3>(v)}; }

__global__ void __launch_bounds__(NTHR, 4) fused_kernel(Params) {
  __shared__ __attribute__((aligned(128))) unsigned char lds_raw[L_END];
  LAS unsigned char* L = (LAS unsigned char*)lds_raw;
  v16u ka; v8u kb8;
  int wave; u32 tab, ko0, ko1, ko2, ko3, ep, d1; v4u xs;
  {
    u32 sa, sb, sc, keep, va, vb; u64 pa;
#define DMA4(P) "global_load_lds_dwordx4 %[k0], " P KV_SFX "\n\tglobal_load_lds_dwordx4 %[k1], " P " offset:1024" KV_SFX "\n\t" \
                "global_load_lds_dwordx4 %[k2], " P " offset:2048" KV_SFX "\n\tglobal_load_lds_dwordx4 %[k3], " P " offset:3072" KV_SFX "\n\t"
    asm volatile("s_load_dwordx16 s[16:31], %[kp], 0x0\n\ts_load_dwordx8 s[36:43], %[kp], 0x40\n\t"
                 "s_lshr_b32 s46, %[bid], 3\n\ts_lshl_b32 s46, s46, 2\n\ts_add_u32 s46, s46, 0x60\n\ts_load_dword s46, %[kp], s46"
                 : "=&{s[16:31]}"(ka), "=&{s[36:43]}"(kb8), "=&{s46}"(tab) : [kp] "s"(__builtin_amdgcn_kernarg_segment_ptr()), [bid] "s"(blockIdx.x) : "scc");
    asm volatile(
      "s_lshr_b32 %[sa], %[bid], 3\n\t"
      "v_readfirstlane_b32 %[w], %[tid]\n\ts_mov_b32 %[keep], m0\n\ts_and_b32 %[sc], %[bid], 7\n\t"
      "s_lshr_b32 %[w], %[w], 6\n\ts_cmp_lt_u32 %[w], 8\n\ts_cbranch_scc1 1f\n\t"
      "v_bfe_u32 %[va], %[tid], 4, 2\n\tv_and_b32 %[vb], 15, %[tid]\n\tv_xor_b32 %[vb], %[vb], %[va]\n\tv_lshlrev_b32 %[vb], 4, %[vb]\n\t"
      "v_lshl_or_b32 %[k0], %[va], 8, %[vb]\n\tv_xor_b32 %[k1], 64, %[k0]\n\tv_xor_b32 %[k2], 0x80, %[k0]\n\tv_xor_b32 %[k3], 0xc0, %[k0]\n\t"
      "s_sub_u32 %[sb], %[w], 8\n\ts_lshl_b32 m0, %[sb], 14\n\ts_lshl_b32 %[sc], %[sc], 8\n\ts_add_u32 %[sb], %[sb], %[sc]\n\ts_lshl_b32 %[sb], %[sb], 1\n\t"
      "s_cmp_gt_u32 %[w], 11\n\ts_cselect_b32 %[sa], 0, %[sa]\n\ts_cmp_lt_u32 %[sa], 8\n\t"
      "s_waitcnt lgkmcnt(0)\n\ts_cbranch_scc1 9f\n\t"
      "s_and_b32 %[sc], s46, 0xffff\n\ts_add_u32 %[sb], %[sb], %[sc]\n\ts_lshl_b32 %[sb], %[sb], 13\n\t"
      "s_add_u32 s44, s18, %[sb]\n\ts_addc_u32 s45, s19, 0\n\t" DMA4("s[44:45]")
      "s_branch 9f\n\t"
      "1:\n\tv_bfe_u32 %[va], %[tid], 3, 3\n\tv_and_b32 %[vb], 6, %[va]\n\tv_xor_b32 %[vb], %[vb], %[tid]\n\tv_and_b32 %[vb], 7, %[vb]\n\tv_lshlrev_b32 %[vb], 4, %[vb]\n\t"
      "v_and_b32 %[k1], 4, %[va]\n\tv_add_u32 %[va], %[va], %[k1]\n\t"
      "v_and_b32 v106, 63, %[tid]\n\tv_lshlrev_b32 v106, 4, v106\n\tv_mov_b32 v104, 0\n\t"
      "v_and_b32 v105, 3, %[tid]\n\tv_lshlrev_b32 v105, 2, v105\n\tv_and_or_b32 v105, %[tid], 48, v105\n\t"
      "s_cmp_lt_u32 %[sa], 8\n\ts_cbranch_scc0 2f\n\t"
      "v_lshl_or_b32 %[k0], %[va], 13, %[vb]\n\tv_add_u32 %[k1], 0x1fc00, %[k0]\n\tv_add_u32 %[k2], 0x7800, %[k0]\n\tv_add_u32 %[k3], 0x27400, %[k0]\n\t"
      "v_bfe_u32 %[va], %[tid], 2, 1\n\tv_lshl_or_b32 v105, %[va], 6, v105\n\ts_lshl_b32 %[sb], %[w], 7\n\tv_add_u32 v105, %[sb], v105\n\t"
      "s_lshl_b32 m0, %[w], 13\n\ts_lshl_b32 %[sb], %[w], 9\n\ts_add_u32 %[sb], %[sb], %[sc]\n\ts_lshl_b32 %[sb], %[sb], 3\n\ts_add_u32 %[sb], %[sb], %[sa]\n\ts_lshl_b32 %[sb], %[sb], 7\n\t"
      "s_waitcnt lgkmcnt(0)\n\t"
      "global_load_dword v105, v105, s[16:17]\n\tglobal_load_dwordx4 v[106:109], v106, s[16:17]\n\tglobal_load_dword v104, v104, s[42:43] sc1\n\t"
      "s_add_u32 s44, s22, %[sb]\n\ts_addc_u32 s45, s23, 0\n\t" DMA4("s[44:45]")
      "s_add_u32 s44, s44, 0x40000\n\ts_addc_u32 s45, s45, 0\n\ts_add_u32 m0, m0, 0x1000\n\ts_nop 0\n\t" DMA4("s[44:45]")
      "s_branch 9f\n\t"
      "2:\n\tv_mul_u32_u24 %[k0], 0x1800, %[va]\n\tv_add_u32 %[k0], %[k0], %[vb]\n\tv_add_u32 %[k1], 0x17c00, %[k0]\n\tv_add_u32 %[k2], 0x5800, %[k0]\n\tv_add_u32 %[k3], 0x1d400, %[k0]\n\t"
      "s_sub_u32 %[sb], %[sa], 23\n\ts_cmp_lt_u32 %[sb], 4\n\ts_cbranch_scc0 3f\n\t"
      "v_bfe_u32 %[va], %[tid], 2, 1\n\tv_lshl_or_b32 v105, %[va], 6, v105\n\ts_lshl_b32 s44, %[w], 7\n\tv_add_u32 v105, s44, v105\n\t"
      "s_lshl_b32 s44, %[w], 13\n\ts_add_u32 m0, s44, 0x10000\n\ts_lshl_b32 %[sc], %[sc], 2\n\ts_add_u32 %[sb], %[sb], %[sc]\n\ts_add_u32 %[sb], %[sb], 16\n\ts_lshl_b32 %[sb], %[sb], 7\n\t"
      "s_mul_i32 %[sc], %[w], 0x60000\n\ts_add_u32 %[sb], %[sb], %[sc]\n\t"
      "s_waitcnt lgkmcnt(0)\n\t"
      "global_load_dword v105, v105, s[36:37]\n\tglobal_load_dword v104, v104, s[42:43] sc1\n\t"
      "s_add_u32 s44, s38, %[sb]\n\ts_addc_u32 s45, s39, 0\n\t" DMA4("s[44:45]")
      "s_add_u32 s44, s44, 0x30000\n\ts_addc_u32 s45, s45, 0\n\ts_add_u32 m0, m0, 0x1000\n\ts_nop 0\n\t" DMA4("s[44:45]")
      "s_branch 9f\n\t3:\n\t"
      "s_and_b32 %[sb], %[sa], 3\n\ts_lshl_b32 %[sb], %[sb], 2\n\ts_and_b32 s44, %[w], 3\n\ts_add_u32 %[sb], %[sb], s44\n\t"
      "s_lshl_b32 s44, s44, 12\n\ts_add_u32 m0, s44, %[tb]\n\ts_lshl_b32 s44, %[sb], 6\n\tv_add_u32 v105, s44, v105\n\t"
      "s_mul_i32 %[sb], %[sb], 0x30000\n\ts_lshl_b32 %[sc], %[sc], 8\n\ts_add_u32 %[sb], %[sb], %[sc]\n\ts_bfe_u32 %[sc], %[sa], 0x10002\n\ts_lshl_b32 %[sc], %[sc], 7\n\ts_add_u32 %[sb], %[sb], %[sc]\n\t"
      "s_lshr_b32 %[sc], %[w], 2\n\ts_lshr_b32 %[sa], %[sa], 3\n\ts_cmp_eq_u32 %[sa], 1\n\ts_cselect_b32 %[sa], %[sc], 0\n\ts_cmp_eq_u32 %[sa], 1\n\t"
      "s_waitcnt lgkmcnt(0)\n\t"
      "global_load_dword v105, v105, s[36:37]\n\tglobal_load_dword v104, v104, s[42:43] sc1\n\ts_cbranch_scc0 9f\n\t"
      "s_add_u32 s44, s38, %[sb]\n\ts_addc_u32 s45, s39, 0\n\t" DMA4("s[44:45]")
      "9:\n\ts_mov_b32 m0, %[keep]"
      : "=&{s[44:45]}"(pa), [w] "=&s"(wave), [k0] "=&v"(ko0), [k1] "=&v"(ko1), [k2] "=&v"(ko2), [k3] "=&v"(ko3),
        "=&{v104}"(ep), "=&{v105}"(d1), "=&{v[106:109]}"(xs), [sa] "=&s"(sa), [sb] "=&s"(sb), [sc] "=&s"(sc), [keep] "=&s"(keep), [va] "=&v"(va), [vb] "=&v"(vb)
      : "{s[16:31]}"(ka), "{s[36:43]}"(kb8), "{s46}"(tab), [bid] "s"(blockIdx.x), [tid] "v"(threadIdx.x), [tb] "i"(L_TB)
      : "memory", "scc");
    asm volatile("" : "+{s[16:31]}"(ka), "+{s[36:43]}"(kb8), "+{s46}"(tab));
  }
  volatile LAS u32* FL = (volatile LAS u32*)(L + L_FLAG);
  LAS float* RED = (LAS float*)(L + L_RED);
  const int lane = threadIdx.x & 63;
  const int bid = blockIdx.x, xc = bid & 7, ix = bid >> 3, rb = ix & 7;
  const int g = lane >> 4, n = lane & 15;
  const bool chain = ix < 8, wcu = (ix >> 3) == 1;
  const int toff = tab & 0xffff, nhalf = tab >> 16, ntask = (nhalf + 1) >> 1;
  const bool ninth = wave == 2 && ntask == 9;
  const int aw = ninth ? 8 : wave - 8;
  const bool head = (wave >> 2) == 2 && !chain, p1 = wcu && (wave >> 2) == 1;
  const u32 ldst = (u32)(uintptr_t)L + L_K + aw * 16384;
#define KGROUP(kb, lo) do { u32 keep_; \
    asm volatile("s_mov_b32 %0, m0\n\ts_mov_b32 m0, %6\n\ts_nop 0\n\t" \
                 "global_load_lds_dwordx4 %1, %5" KV_SFX "\n\t" \
                 "global_load_lds_dwordx4 %2, %5 offset:1024" KV_SFX "\n\t" \
                 "global_load_lds_dwordx4 %3, %5 offset:2048" KV_SFX "\n\t" \
                 "global_load_lds_dwordx4 %4, %5 offset:3072" KV_SFX "\n\t" \
                 "s_mov_b32 m0, %0" \
                 : "=&s"(keep_) : "v"(ko0), "v"(ko1), "v"(ko2), "v"(ko3), "s"(kb), "s"(ldst + (lo)) : "memory"); } while (0)
#define PTR(v, i) (((u64)(v)[2 * (i) + 1] << 32) | (v)[2 * (i)])
  Ptrs p;
  p.x = (const u16*)PTR(ka, 0); p.K = (const u16*)PTR(ka, 1); p.V = (const u16*)PTR(ka, 2); p.wq = (const u16*)PTR(ka, 3); p.wo = (const u16*)PTR(ka, 4);
  p.wg = (const u16*)PTR(ka, 5); p.wu = (const u16*)PTR(ka, 6); p.wd = (const u16*)PTR(ka, 7);
  p.c = (const u16*)PTR(kb8, 0); p.wmod = (const u16*)PTR(kb8, 1); p.out = (u16*)PTR(kb8, 2); p.ws = (void*)PTR(kb8, 3);
#define MKWS() const rsrc_t ws = mkr(p.ws, WS_BYTES)
  if (wave == 0 && lane < 16) FL[lane] = (lane == 0 && ix >= 16) ? 1u : 0u;
  BARRIER();

  MKWS();
  const bool gcu = ix >= 16 && ix < 23, mcu = ix >= 23 && ix < 27;
  u32 tag = 0;
  if (__builtin_expect(wave < 8 && !ninth, 1)) {
  if (wave >= 4 && !(chain || wcu || gcu || mcu)) return;
  __builtin_amdgcn_s_setprio(3);
  asm volatile("s_waitcnt vmcnt(8)\n\ts_cmp_eq_u32 %3, 2\n\ts_cbranch_scc1 1f\n\ts_waitcnt vmcnt(4)\n\ts_cmp_eq_u32 %3, 1\n\ts_cbranch_scc1 1f\n\ts_waitcnt vmcnt(0)\n\t1:"
               : "+{v104}"(ep), "+{v105}"(d1), "+{v[106:109]}"(xs) : "s"(__builtin_amdgcn_readfirstlane((chain || mcu) ? 2 : p1 ? 1 : 0)) : "memory", "scc");
  const int cg = xc * 2 + (rb >> 2), ks = rb & 3;
  const int pi = ((n >> 2) & 1) * 16 + g * 4 + (n & 3);
  tag = ep + 1u;
  if (__builtin_expect(p1, 1)) {
    v4u a = bcast4<0>(silu2(d1)), f[4];
    int ho = H0_OFF + (cg * 4 + ks) * 512 + lane * 8;
    PIN1(a); PIN1(ho);
    VM0();
    lds_frags(L + L_TB + (wave & 3) * 4096, lane, f);
    RED[(wave & 3) * 64 + lane] = gemv<1>(&a, f, g);
    if (lds_ticket(&FL[3], lane) == 3u) {
      const float s = (RED[lane] + RED[64 + lane]) + (RED[128 + lane] + RED[192 + lane]);
      const float s_hi = dpp<SHL1>(s);
      if (!(lane & 1)) stc(ws, ho, v4u{tag, asu(s), ~tag, asu(s_hi)});
      MEMFENCE();
      if (lane == 0) FL[0] = 1u;
    }
  }
  if (__builtin_expect(chain, 1)) {
    float mb, inv;
    ln_stats(xs, mb, inv);
    f32x2 y = up2(ln_pair(d1, mb, inv));
    PIN1(y);
    VM0();
    v4u f[8];
    lds_frags(L + L_K + wave * 8192, lane, f); lds_frags(L + L_K + wave * 8192 + 4096, lane, f + 4);
    SCHED();
    v4u h0, h1, h2, h3;
    pollT4(ws, H0_OFF + (wave + (n >> 3) * 8) * 2048 + pi * 16, tag, ~tag, h0, h1, h2, h3);
    const u32 md = pk((asf(h0.y) + asf(h1.y)) + (asf(h2.y) + asf(h3.y)), (asf(h0.w) + asf(h1.w)) + (asf(h2.w) + asf(h3.w)));
    const u32 ot = dppu<ROR8>(md), sa = (n & 8) ? ot : md, ca = (n & 8) ? md : ot;
    const u32 hp = pk2(up2(pk2(y * one_plus(ca))) + up2(sa));
    const v4u a[2] = {bcast4<0>(hp), bcast4<4>(hp)};
    RED[lane * 8 + wave] = gemv<2>(a, f, g);
    if (lds_ticket(&FL[7], lane) == 7u) {
      const float s = sum8((u32)(uintptr_t)(RED + lane * 8));
      const u32 d = pk(s, dpp<SHL1>(s)), d2 = dppu<SHL2>(d);
      if ((lane & 3) == 0) stc(ws, Q_OFF + (xc * 8 + rb) * 256 + lane * 4, v4u{tag, d, ~tag, d2});
      MEMFENCE();
      if (lane == 0) FL[0] = 1u;
    }
  }
  if (mcu) {
    const u32 sp = silu2(d1);
    const v4u a[2] = {bcast4<0>(sp), bcast4<4>(sp)};
    VM0();
    v4u f[8];
    lds_frags(L + 65536 + wave * 8192, lane, f); lds_frags(L + 65536 + wave * 8192 + 4096, lane, f + 4);
    RED[lane * 8 + wave] = gemv<2>(a, f, g);
    MEMFENCE();
    lds_inc(&FL[3], lane);
    if (wave == 1) {
      lds_wait_ge(&FL[3], 8u);
      const float s = sum8((u32)(uintptr_t)(RED + lane * 8));
      const u32 d = nosent(pk(s, dpp<SHL1>(s)));
      lds_wait_ge(&FL[5], 1u);
      if (!(lane & 1)) stc1(ws, MR_OFF + (xc * 4 + (ix - 23)) * 128 + lane * 2, d);
    }
  }
  if (wcu && wave == 1) {
    lds_wait_ge(&FL[0], 1u);
    const v4u sv = {SENT, SENT, SENT, SENT};
#pragma unroll
    for (int j = 0; j < CLR_KB; ++j) stc(ws, S_OFF + (xc * 8 + rb) * (CLR_KB * 1024) + j * 1024 + lane * 16, sv);
    VM0();
    if (lane == 0) stc2(ws, CF_OFF + (xc * 8 + rb) * 16, v2u{tag, ~tag});
  }
  if (wave == 0) {
    const int qb = Q_OFF + xc * 2048 + lane * 16;
    if (!chain && !mcu) __builtin_amdgcn_s_sleep(100);
    _Pragma("nounroll") for (u32 s = 0; s < SPIN_CAP; ++s) {
      const v4u q0 = ldc(ws, qb), q1 = ldc(ws, qb + 1024);
      SCHED();
      if (__all((((q0.x ^ tag) | (q1.x ^ tag)) | ((q0.z ^ ~tag) | (q1.z ^ ~tag))) == 0u)) {
        LAS unsigned char* qd = L + L_Q + (lane >> 5) * 320 + (lane & 31) * 8;
        *(LAS v2u*)qd = v2u{q0.y, q0.w};
        *(LAS v2u*)(qd + 640) = v2u{q1.y, q1.w};
        LGKM0();
        if (lane == 0) FL[1] = 1u;
        break;
      }
      MEMFENCE();
    }
  }
  if (wave == 3) {
    {
      __builtin_amdgcn_s_sleep(100);
      _Pragma("nounroll") for (u32 s = 0; s < SPIN_CAP; ++s) {
        const v2u cf = __builtin_amdgcn_raw_buffer_load_b64(ws, CF_OFF + lane * 16, 0, SC1);
        if (__all(((cf.x ^ tag) | (cf.y ^ ~tag)) == 0u)) break;
        __builtin_amdgcn_s_sleep(16);
        MEMFENCE();
      }
      if (lane == 0) FL[5] = 1u;
    }
  }
  }
  else {
    const bool early = aw < 4;
    if (ninth) {
      VM0();
      const u32 kv0 = g * 256 + ((n ^ g) << 4);
      ko0 = kv0; ko1 = kv0 ^ 64u; ko2 = kv0 ^ 128u; ko3 = kv0 ^ 192u;
    }
    if (aw >= ntask) return;
    if (chain) lds_wait_ge(&FL[0], 1u);
    if (!early) {
      lds_wait_ge(&FL[3], (ix >= 23 && ix < 27) ? 8u : (wcu && ninth) ? 4u : 0u);
      _Pragma("nounroll") for (u32 s = 0; s < (SPIN_CAP << 3); ++s) { if (FL[2] >= 4u || FL[1] != 0u) break; __builtin_amdgcn_s_sleep(1); }
      MEMFENCE();
    }
    const int hh = n >> 2, e2 = n & 3;
    u32 qseen = (!early && ix >= 16) ? 1u : 0u;
    const u32 key0 = (u32)(xc * 16384 + toff * 32 + aw * 64);
    const bool halft = 2 * aw + 1 == nhalf;
#define THROTTLE() do { if (!qseen) { const v2u fl_ = *(volatile LAS v2u*)FL; qseen = __builtin_amdgcn_readfirstlane(fl_.y); \
      if (!qseen) { if (__builtin_amdgcn_readfirstlane(fl_.x)) asm volatile("s_waitcnt vmcnt(%0)" :: "i"(THR_W - 4) : "memory"); else asm volatile("s_waitcnt vmcnt(%0)" :: "i"(THR_Q - 4) : "memory"); } } SCHED(); } while (0)
    const u64 kb = (u64)p.K + (u64)key0 * 256;
    if (!head) KGROUP(kb, 0);
    THROTTLE(); KGROUP(kb + 4096, 4096);
    THROTTLE();
    if (!halft) { KGROUP(kb + 8192, 8192); THROTTLE(); KGROUP(kb + 12288, 12288); THROTTLE(); }
    v4u vv[16];
    {
      const rsrc_t rv = mkr(p.V, 8 * 16384 * 256);
      const int vo = (halft && g >= 2) ? 0x40000000 : g * 4096 + n * 16;
#pragma unroll
      for (int i = 0; i < 16; ++i) { vv[i] = ld4<NT>(rv, vo + i * 256, key0 * 256); if ((i & 3) == 3 && i < 15) THROTTLE(); }
    }
    SCHED();
    lds_inc(&FL[2], lane);
    lds_wait_ge(&FL[1], 1u);
    asm volatile("s_waitcnt vmcnt(16)" ::: "memory");
    SCHED();
    float s2[4], pe[4];
    {
      const LAS unsigned char* kbase = L + L_K + aw * 16384 + n * 256;
      const LAS unsigned char* qa = L + L_Q + (n & 3) * 320 + g * 16;
      f32x4 sa[4];
#pragma unroll
      for (int t = 0; t < 4; ++t) sa[t] = f32x4{0.f, 0.f, 0.f, 0.f};
#pragma unroll
      for (int s = 0; s < 4; ++s) {
        const v4u a = *(const LAS v4u*)(qa + 64 * s);
#pragma unroll
        for (int t = 0; t < 4; ++t) sa[t] = mfma32(a, *(const LAS v4u*)(kbase + t * 4096 + (((4 * s + g) ^ n) << 4)), sa[t]);
      }
      const f32x4 lo = (g & 1) ? sa[1] : sa[0], hi = (g & 1) ? sa[3] : sa[2], sacc = (g & 2) ? hi : lo;
      constexpr float SC = 0.08838834764831845f;
      const f32x2 c = up2(pk2(up2(pk(sacc[0], sacc[1])) * SC)), d = up2(pk2(up2(pk(sacc[2], sacc[3])) * SC));
      const bool dead = halft && g >= 2;
      s2[0] = dead ? -__builtin_inff() : c.x; s2[1] = dead ? -__builtin_inff() : c.y; s2[2] = dead ? -__builtin_inff() : d.x; s2[3] = dead ? -__builtin_inff() : d.y;
    }
    const float mrow = row_max(tm16(tm32(s2[0], s2[2]), tm32(s2[1], s2[3])));
#pragma unroll
    for (int h2 = 0; h2 < 4; ++h2) pe[h2] = __builtin_amdgcn_exp2f((s2[h2] - rdlf(mrow, 16 * h2)) * LOG2E);
    const u32 w0 = pk(pe[0], pe[1]), w1 = pk(pe[2], pe[3]);
    const float lrow = row_sum(tr16(tr32(pe[0], pe[2]), tr32(pe[1], pe[3])));
    if (n == 0) *(LAS f32x2*)(L + L_ML + g * 128 + aw * 8) = f32x2{mrow, lrow};
    MEMFENCE();
    lds_inc(&FL[4], lane);
    const u32 two = (u32)(2 * hh) | ((u32)(2 * hh + 1) << 8);
    const u32 selv = (e2 & 1) ? ((two << 16) | 0x0C0Cu) : (0x0C0C0000u | two);
    const u32 sel0 = (e2 >> 1) == 0 ? selv : 0x0C0C0C0Cu, sel1 = (e2 >> 1) == 1 ? selv : 0x0C0C0C0Cu;
    v2u xb[16];
#define PV_PREP(i) { const u32 b0_ = dppu<BC + (i)>(w0), b1_ = dppu<BC + (i)>(w1); xb[i] = v2u{__builtin_amdgcn_perm(b1_, b0_, sel0), __builtin_amdgcn_perm(b1_, b0_, sel1)}; }
    PV_PREP(0) PV_PREP(1) PV_PREP(2) PV_PREP(3) PV_PREP(4) PV_PREP(5) PV_PREP(6) PV_PREP(7)
    PV_PREP(8) PV_PREP(9) PV_PREP(10) PV_PREP(11) PV_PREP(12) PV_PREP(13) PV_PREP(14) PV_PREP(15)
#undef PV_PREP
    u32 obw = (u32)(uintptr_t)L + L_K + aw * 16384 + (hh * 128 + e2 * 16 + 4 * g) * 4;
    PIN1(obw);
    SCHED();
    f32x4 o1 = {0.f, 0.f, 0.f, 0.f}, o2 = {0.f, 0.f, 0.f, 0.f};
#pragma unroll
    for (int i = 0; i < 16; ++i) {
      o1 = __builtin_amdgcn_mfma_f32_16x16x16bf16_1k(__builtin_bit_cast(s16x4, v2u{vv[i].x, vv[i].y}), __builtin_bit_cast(s16x4, xb[i]), o1, 0, 0, 0);
      o2 = __builtin_amdgcn_mfma_f32_16x16x16bf16_1k(__builtin_bit_cast(s16x4, v2u{vv[i].z, vv[i].w}), __builtin_bit_cast(s16x4, xb[i]), o2, 0, 0, 0);
    }
    *(LAS f32x4*)(uintptr_t)obw = o1; *(LAS f32x4*)(uintptr_t)(obw + 256) = o2;
    if (aw < 2 || (aw > 3 && aw < 8) || (ninth && !gcu)) { BARRIER(); return; }
    if (!ninth) {
    const int tau = (aw - 2) * 64 + lane, h = tau >> 5;
    float f[9];
    lds_wait_ge(&FL[5], 1u);
    lds_wait_ge(&FL[4], (u32)ntask);
    {
      float m[10], l[10];
#pragma unroll
      for (int w = 0; w < 5; ++w) { const f32x4 t = *(const LAS f32x4*)(L + L_ML + h * 128 + w * 16); m[2 * w] = t.x; l[2 * w] = t.y; m[2 * w + 1] = t.z; l[2 * w + 1] = t.w; }
#pragma unroll
      for (int w = 4; w < 9; ++w) { m[w] = w < ntask ? m[w] : -__builtin_inff(); l[w] = w < ntask ? l[w] : 0.f; }
      const float M = mx(mx(mx(mx(m[0], m[1]), mx(m[2], m[3])), mx(mx(m[4], m[5]), mx(m[6], m[7]))), m[8]);
      float Ls = 0.f;
#pragma unroll
      for (int w = 0; w < 9; ++w) { f[w] = __builtin_amdgcn_exp2f((m[w] - M) * LOG2E); Ls = fmaf(l[w], f[w], Ls); PIN1(f[w]); }
      if ((lane & 31) == 0) stc2(ws, ML_OFF + ((xc * 32 + ix) * 2 + aw - 2) * 128 + (lane >> 5) * 8, v2u{asu(M), asu(Ls)});
    }
    int t1o = T1_OFF + (((xc * 4 + h) * 4 + ((tau >> 3) & 3)) * 32 + ix) * 128 + (tau & 7) * 16;
    u32 pa[9];
#pragma unroll
    for (int w = 0; w < 9; ++w) { pa[w] = (u32)(uintptr_t)(L + L_K) + tau * 16 + (w < ntask ? w : 0) * 16384; PIN1(pa[w]); }
    PIN1(t1o);
    __builtin_amdgcn_s_setprio(3);
    BARRIER();
    f32x4 part[9];
#pragma unroll
    for (int w = 0; w < 9; ++w) part[w] = *(const LAS f32x4*)(uintptr_t)pa[w];
    const f32x4 acc = (((part[0] * f[0] + part[1] * f[1]) + (part[2] * f[2] + part[3] * f[3])) + ((part[4] * f[4] + part[5] * f[5]) + (part[6] * f[6] + part[7] * f[7]))) + part[8] * f[8];
    stc(ws, t1o, __builtin_bit_cast(v4u, acc));
    lds_inc(&FL[8], lane);
    return;
    }
    __builtin_amdgcn_s_setprio(3);
  }

  LAS unsigned char* tb = L + L_K + wave * 16384 + 8192;
  if (ix < 16) {
    if (wave < 4) return;
    const int x = wave & 3, hl = ix >> 2, hg = xc * 4 + hl, qd = ix & 3;
    const rsrc_t ro = mkr(p.wo, 4096 * 512 * 2);
    const int col = xc * 64 + rb * 8;
    lds_wait_ge(&FL[2], (u32)ntask);
    v4u w[8];
    {
      const int rho = lane >> 3, vo = (64 * (rho >> 2) + 8 * (rho & 3)) * 1024 + (lane & 7) * 16;
#pragma unroll
      for (int i = 0; i < 8; ++i) w[i] = ld4<NT>(ro, vo, (hg * 128 + 32 * ((i >> 1) & 1) + 2 * qd + (i & 1)) * 1024 + x * 256 + (i >> 2) * 128);
    }
    BARRIER();
    lds_wait_ge(&FL[8], 2u);
    const v2u ma = poll2(ws, ML_OFF + ((xc * 32 + (lane >> 1)) * 2 + (hl >> 1)) * 128 + (hl & 1) * 8);
    v4u f[8];
    tile_frags(tb, lane, w, f); tile_frags(tb, lane, w + 4, f + 4);
    int ob = T1_OFF + ((hg * 4 + qd) * 32 + (lane >> 1)) * 128 + x * 32 + (lane & 1) * 16;
    int t2o = T2_OFF + (xc * 16 + ix) * 2048 + x * 512 + lane * 4;
    float M = asf(ma.x);
    M = mx(M, dpp<XOR2>(M)); M = mx(M, dpp<ROR4>(M)); M = mx(M, dpp<ROR8>(M)); M = tm16(M, M); M = tm32(M, M);
    float fa = __builtin_amdgcn_exp2f((asf(ma.x) - M) * LOG2E);
    float Ls = fa * asf(ma.y);
    Ls = tr32(Ls, Ls); Ls = tr16(Ls, Ls); Ls += dpp<ROR8>(Ls); Ls += dpp<ROR4>(Ls); Ls += dpp<XOR2>(Ls);
    float inv = __builtin_amdgcn_rcpf(Ls);
    u32 oa = (u32)(uintptr_t)(L + L_X) + x * 16 + (lane & 1) * 8 + (lane >> 5) * 4;
    u32 oa2 = (u32)(uintptr_t)(L + L_X) + g * 16;
    fa *= inv;
    PIN1(fa); PIN1(ob); PIN1(t2o); PIN1(oa); PIN1(oa2);
    SCHED();
    const v4u a = poll4(ws, ob);
    float u0 = tr32(fa * asf(a.x), fa * asf(a.z)), u1 = tr32(fa * asf(a.y), fa * asf(a.w));
    u0 = tr16(u0, u0); u1 = tr16(u1, u1);
    u0 += dpp<ROR8>(u0); u1 += dpp<ROR8>(u1);
    u0 += dpp<ROR4>(u0); u1 += dpp<ROR4>(u1);
    u0 += dpp<XOR2>(u0); u1 += dpp<XOR2>(u1);
    if (!(lane & 30)) *(LAS u32*)(uintptr_t)oa = pk(u0, u1);
    BARRIER();
    const v4u av = *(const LAS v4u*)(uintptr_t)oa2;
    const float y0 = gemv<1>(&av, f, g), y1 = gemv<1>(&av, f + 4, g);
    stc_row(ws, t2o, y0, lane); stc_row(ws, t2o + 256, y1, lane);
    if (!chain || wave != 4) return;
    f32x2 xin = up2(*(const GAS u32*)((const GAS u16*)p.x + col + 2 * g));
    u32 gaw, gbw;
    poll1x2<3072>(ws, MR_OFF + (col + 2 * g) * 2, gaw, gbw);
    f32x2 ga = up2(gaw), gb = up2(gbw);
    int tio = T2_OFF + lane * 2048 + col * 4, tio2 = tio + 131072, yo = lane < 56 ? Y_OFF + lane * 2048 + col * 4 : 0x40000000;
    PIN1(ga); PIN1(gb); PIN1(xin); PIN1(tio); PIN1(tio2); PIN1(yo);
    v4u pa, pb, pc, pd;
    poll4x4(ws, tio, tio2, pa, pb, pc, pd);
    pa = __builtin_bit_cast(v4u, __builtin_bit_cast(f32x4, pa) + __builtin_bit_cast(f32x4, pc));
    pb = __builtin_bit_cast(v4u, __builtin_bit_cast(f32x4, pb) + __builtin_bit_cast(f32x4, pd));
    const u32 x1 = nosent(pk2(xin + up2(pk2(ga * up2(pk2(sum8x64(pa, pb)))))));
    const v4u o = {rdl(x1, 0), rdl(x1, 16), rdl(x1, 32), rdl(x1, 48)};
    if (lane == 0) stc(ws, X1_OFF + col * 16, o);
    if (bid == 0 && lane == 0) stc1(ws, EPOCH_OFF, tag);
    f32x2 x1f = up2(x1);
    PIN1(x1f);
    poll4x2<16>(ws, yo, pa, pb);
    const u32 d = pk2(x1f + up2(pk2(gb * up2(pk2(sum8x64(pa, pb))))));
    if (!n) *(GAS u32*)((GAS u16*)p.out + col + 2 * g) = d;
  } else if (gcu) {
    const int gi = xc * 7 + (ix - 16);
    const rsrc_t rd = mkr(p.wd, 1792 * 512 * 2);
    BARRIER();
    lds_wait_ge(&FL[8], 2u);
    v4u w[12];
    {
      const int rho = lane >> 3;
      const GAS unsigned char* src = (const GAS unsigned char*)((lane & 4) ? p.wu : p.wg) + (size_t)(wave * 64 + 8 * (rho >> 2) + (rho & 3)) * 3584 + gi * 64 + (lane & 3) * 16;
#pragma unroll
      for (int i = 0; i < 8; ++i) w[i] = *(const GAS v4u*)(src + (32 * (i >> 2) + 16 * (i & 1) + 4 * ((i >> 1) & 1)) * 3584);
    }
    tile_load<NT>(rd, tile_voff(lane, 1024), gi * 32 * 1024 + wave * 128, 1024, w + 8);
    v4u f[12];
    tile_frags(tb, lane, w, f); tile_frags(tb, lane, w + 4, f + 4); tile_frags(tb, lane, w + 8, f + 8);
    int yo = Y_OFF + gi * 2048 + wave * 256 + lane * 4;
    u32 aa = (u32)(uintptr_t)(L + L_X) + wave * 128 + g * 16, ra = (u32)(uintptr_t)(RED + lane * 8 + wave), sa = (u32)(uintptr_t)(RED + ((n >> 3) * 32 + g * 8 + (n & 7)) * 8);
    u32 ha = (u32)(uintptr_t)(L + L_X) + lane * 16 + wave * 4;
    PIN1(yo); PIN1(aa); PIN1(ra); PIN1(sa); PIN1(ha);
    SCHED();
    if (wave < 4) {
#define SEL4(v) ((wave & 2) ? ((wave & 1) ? (v).w : (v).z) : ((wave & 1) ? (v).y : (v).x))
      v4u sbw, cbw;
      poll4x2<1024>(ws, MR_OFF + 1024 + lane * 16, sbw, cbw);
      f32x2 sb = up2(SEL4(sbw)), c1 = one_plus(SEL4(cbw));
      int xb1 = X1_OFF + lane * 128;
      PIN1(sb); PIN1(c1); PIN1(xb1);
      const v4u x1v = poll4(ws, xb1);
      float mb, inv;
      ln_stats(x1v, mb, inv);
      *(LAS u32*)(uintptr_t)ha = modulate(ln_pair(SEL4(x1v), mb, inv), c1, sb);
    }
    BARRIER();
    const v4u av[2] = {*(const LAS v4u*)(uintptr_t)aa, *(const LAS v4u*)(uintptr_t)(aa + 64)};
    *(LAS float*)(uintptr_t)ra = gemv<2>(av, f, g);
    BARRIER();
    const float s = rbf(sum8(sa));
    const float gt = s, up = dpp<ROR8>(s);
    const float u2 = (-2.f * 0.7978845608028654f * LOG2E) * (gt + 0.044715f * gt * gt * gt);
    const float gv = rbf(gt * __builtin_amdgcn_rcpf(1.f + __builtin_amdgcn_exp2f(u2))) * up;
    const u32 d = pk(gv, dpp<SHL1>(gv));
    const v4u ag = {dppu<BC + 0>(d), dppu<BC + 2>(d), dppu<BC + 4>(d), dppu<BC + 6>(d)};
    stc_row(ws, yo, gemv<1>(&ag, f + 8, g), lane);
  }
}
}

extern "C" void kernel_launch(void* const* d_in, const int* in_sizes, int n_in, void* d_out, int out_size, void* d_ws, size_t ws_size, hipStream_t stream) {
  Params a;
  Ptrs& p = a.p;
  p.x = (const u16*)d_in[0]; p.K = (const u16*)d_in[1]; p.V = (const u16*)d_in[2]; p.wq = (const u16*)d_in[3]; p.wo = (const u16*)d_in[4];
  p.wg = (const u16*)d_in[6]; p.wu = (const u16*)d_in[7]; p.wd = (const u16*)d_in[8]; p.c = (const u16*)d_in[9]; p.wmod = (const u16*)d_in[10];
  p.out = (u16*)d_out; p.ws = d_ws;
  for (int i = 0; i < 32; ++i) a.tab[i] = (u32)toff_of(i) | ((u32)nhalf_of(i) << 16);
  hipLaunchKernelGGL(fused_kernel, dim3(NWG), dim3(NTHR), 0, stream, a);
}
```

```cpp
#include <hip/hip_runtime.h>
#include <stdint.h>

namespace {
typedef unsigned short u16;
typedef unsigned u32;
typedef unsigned long long u64;
typedef u32 v4u __attribute__((ext_vector_type(4)));
typedef u32 v2u __attribute__((ext_vector_type(2)));
typedef u32 v8u __attribute__((ext_vector_type(8)));
typedef u32 v16u __attribute__((ext_vector_type(16)));
typedef float f32x4 __attribute__((ext_vector_type(4)));
typedef float f32x2 __attribute__((ext_vector_type(2)));
typedef __bf16 bf16x2 __attribute__((ext_vector_type(2)));
typedef __bf16 bf16x8 __attribute__((ext_vector_type(8)));
typedef short s16x4 __attribute__((ext_vector_type(4)));
#define GAS __attribute__((address_space(1)))
#define LAS __attribute__((address_space(3)))

#define KV_SFX " nt"
constexpr int NT = 2;
constexpr int THR_W = 8;
constexpr int THR_Q = 6;
constexpr int H_W = 17, H_G = 18, H_M = 16, H_P = 18;

constexpr int NWG = 256, NTHR = 1024;
constexpr u32 SENT = 0xFFFFFFFFu;
constexpr int SC1 = 16;
constexpr u32 SPIN_CAP = 1u << 17;
constexpr float LOG2E = 1.4426950408889634f;
constexpr int EPOCH_OFF = 0;
constexpr int CF_OFF = 8192;
constexpr int Q_OFF = 16384;
constexpr int H0_OFF = 32768;
constexpr int S_OFF = 65536;
#define SIDE_XOR 0
constexpr int T1_OFF = S_OFF;
constexpr int ML_OFF = T1_OFF + 524288;
constexpr int T2_OFF = ML_OFF + 65536;
constexpr int Y_OFF = T2_OFF + 262144;
constexpr int X1_OFF = Y_OFF + 114688;
constexpr int MR_OFF = X1_OFF + 16384;
__device__ __forceinline__ int t1_at(int xc, int off) { return T1_OFF + (xc & 3) * 131072 + (off >> 13) * 16384 + (((xc >> 2) ^ SIDE_XOR) << 13) + (off & 8191); }
__device__ __forceinline__ int ml_at(int xc) { return ML_OFF + (xc & 3) * 16384 + (((xc >> 2) ^ SIDE_XOR) << 13); }
__device__ __forceinline__ int row_at(int prod, int colbyte) { return (prod >> 3) * 16384 + (((colbyte >> 10) ^ SIDE_XOR) << 13) + (prod & 7) * 1024 + (colbyte & 1023); }
constexpr int CLR_KB = 16;
constexpr int S_END = S_OFF + 64 * CLR_KB * 1024;
static_assert(MR_OFF + 4096 <= S_END && S_OFF % 16384 == 0 && ML_OFF % 16384 == 0 && T2_OFF % 16384 == 0 && Y_OFF % 16384 == 0 && X1_OFF % 16384 == 0, "");
constexpr int WS_BYTES = S_END;
constexpr int L_K = 0, L_TB = 8 * 16384, L_Q = 9 * 16384, L_FLAG = L_Q + 1280, L_ML = L_FLAG + 64, L_X = L_ML + 512, L_RED = L_X + 1024, L_END = L_RED + 4096;
static_assert(L_END <= 163840 && L_RED % 16 == 0 && L_TB % 16 == 0, "");
constexpr int CH_ALL = 512 - 8 * H_W - 7 * H_G - 4 * H_M - 5 * H_P, H_CH = CH_ALL / 8, CH_HI = CH_ALL - 8 * H_CH;
constexpr int nhalf_of(int ix) { return ix < 8 ? (ix < CH_HI ? H_CH + 1 : H_CH) : ix < 16 ? H_W : ix < 23 ? H_G : ix < 27 ? H_M : H_P; }
constexpr int toff_of(int ix) { int o = 0; for (int i = 0; i < ix; ++i) o += nhalf_of(i); return o; }
static_assert(toff_of(32) == 512 && H_G >= 8 && H_G <= 18 && H_CH >= 8 && H_CH + 1 <= 16 && H_M >= 8 && H_M <= 16 && H_W >= 8 && H_W <= 18 && H_P >= 8 && H_P <= 18, "");

struct Ptrs {
  const u16 *x, *K, *V, *wq, *wo, *wg, *wu, *wd, *c, *wmod;
  u16* out;
  void* ws;
};
struct Params { Ptrs p; u32 tab[32]; };
static_assert(sizeof(Params) == 96 + 128 && L_K == 0, "");

__device__ __forceinline__ float asf(u32 x) { return __builtin_bit_cast(float, x); }
__device__ __forceinline__ u32 asu(float x) { return __builtin_bit_cast(u32, x); }
__device__ __forceinline__ float blo(u32 w) { return asf(w << 16); }
__device__ __forceinline__ float bhi(u32 w) { return asf(w & 0xffff0000u); }
__device__ __forceinline__ u32 pk2(f32x2 v) { return __builtin_bit_cast(u32, __builtin_convertvector(v, bf16x2)); }
__device__ __forceinline__ u32 pk(float lo, float hi) { return pk2(f32x2{lo, hi}); }
__device__ __forceinline__ f32x2 up2(u32 w) { return f32x2{blo(w), bhi(w)}; }
__device__ __forceinline__ float rbf(float f) { return (float)(__bf16)f; }
__device__ __forceinline__ float dot2(u32 a, u32 b, float c) { return __builtin_amdgcn_fdot2_f32_bf16(__builtin_bit_cast(bf16x2, a), __builtin_bit_cast(bf16x2, b), c, false); }
template <int CTRL> __device__ __forceinline__ float dpp(float x) { return asf(__builtin_amdgcn_mov_dpp(asu(x), CTRL, 0xf, 0xf, true)); }
template <int CTRL> __device__ __forceinline__ u32 dppu(u32 x) { return __builtin_amdgcn_mov_dpp(x, CTRL, 0xf, 0xf, true); }
constexpr int XOR1 = 0xB1, XOR2 = 0x4E, XOR7 = 0x141, ROR8 = 0x128, ROR4 = 0x124, SHL1 = 0x101, SHL2 = 0x102, BC = 0x150;
__device__ __forceinline__ float mx(float a, float b) { return __builtin_amdgcn_fmed3f(a, b, __builtin_inff()); }
__device__ __forceinline__ float tr32(float a, float b) { auto s = __builtin_amdgcn_permlane32_swap(asu(a), asu(b), false, false); return asf(s[0]) + asf(s[1]); }
__device__ __forceinline__ float tr16(float a, float b) { auto s = __builtin_amdgcn_permlane16_swap(asu(a), asu(b), false, false); return asf(s[0]) + asf(s[1]); }
__device__ __forceinline__ float tm32(float a, float b) { auto s = __builtin_amdgcn_permlane32_swap(asu(a), asu(b), false, false); return mx(asf(s[0]), asf(s[1])); }
__device__ __forceinline__ float tm16(float a, float b) { auto s = __builtin_amdgcn_permlane16_swap(asu(a), asu(b), false, false); return mx(asf(s[0]), asf(s[1])); }
__device__ __forceinline__ float row_sum(float v) { v += dpp<ROR8>(v); v += dpp<XOR7>(v); v += dpp<XOR2>(v); v += dpp<XOR1>(v); return v; }
__device__ __forceinline__ float row_max(float v) { v = mx(v, dpp<ROR8>(v)); v = mx(v, dpp<XOR7>(v)); v = mx(v, dpp<XOR2>(v)); v = mx(v, dpp<XOR1>(v)); return v; }
__device__ __forceinline__ f32x2 sum8x64(v4u a, v4u b) {
  const float y0 = tr32(asf(a.x), asf(b.x)), y1 = tr32(asf(a.y), asf(b.y)), y2 = tr32(asf(a.z), asf(b.z)), y3 = tr32(asf(a.w), asf(b.w));
  return f32x2{row_sum(tr16(y0, y2)), row_sum(tr16(y1, y3))};
}
__device__ __forceinline__ u32 rdl(u32 v, int l) { return __builtin_amdgcn_readlane(v, l); }
__device__ __forceinline__ float rdlf(float v, int l) { return asf(__builtin_amdgcn_readlane(asu(v), l)); }
__device__ __forceinline__ u32 nosent(u32 w) { return w < 0xFFFFFFFEu ? w : 0xFFFFFFFEu; }
typedef __amdgpu_buffer_rsrc_t rsrc_t;
__device__ __forceinline__ rsrc_t mkr(const void* q, int bytes) { return __builtin_amdgcn_make_buffer_rsrc((void*)q, 0, bytes, 0x00020000); }
template <int AUX> __device__ __forceinline__ v4u ld4(rsrc_t r, int off, int so) { return __builtin_amdgcn_raw_buffer_load_b128(r, off, so, AUX); }
__device__ __forceinline__ u32 ld1(rsrc_t r, int off, int so) { return __builtin_amdgcn_raw_buffer_load_b32(r, off, so, 0); }
__device__ __forceinline__ v4u ldc(rsrc_t r, int off) { return __builtin_amdgcn_raw_buffer_load_b128(r, off, 0, SC1); }
__device__ __forceinline__ u32 ldc1(rsrc_t r, int off) { return __builtin_amdgcn_raw_buffer_load_b32(r, off, 0, SC1); }
__device__ __forceinline__ void stc(rsrc_t r, int off, v4u v) { __builtin_amdgcn_raw_buffer_store_b128(v, r, off, 0, SC1); }
__device__ __forceinline__ void stc2(rsrc_t r, int off, v2u v) { __builtin_amdgcn_raw_buffer_store_b64(v, r, off, 0, SC1); }
__device__ __forceinline__ void stc_row(rsrc_t r, int off, float v, int lane) {
  const v4u o = {asu(v), asu(dpp<0x55>(v)), asu(dpp<0xAA>(v)), asu(dpp<0xFF>(v))};
  if (!(lane & 3)) stc(r, off, o);
}
__device__ __forceinline__ void stc1(rsrc_t r, int off, u32 v) { __builtin_amdgcn_raw_buffer_store_b32(v, r, off, 0, SC1); }
#define MEMFENCE() asm volatile("" ::: "memory")
#define LGKM0() asm volatile("s_waitcnt lgkmcnt(0)" ::: "memory")
#define VM0() asm volatile("s_waitcnt vmcnt(0)" ::: "memory")
#define SCHED() __builtin_amdgcn_sched_barrier(0)
#define BARRIER() do { LGKM0(); SCHED(); __builtin_amdgcn_s_barrier(); MEMFENCE(); SCHED(); } while (0)
#define PIN1(a) asm volatile("" : "+v"(a))
#define PIN4(a) asm volatile("" : "+v"((a)[0]), "+v"((a)[1]), "+v"((a)[2]), "+v"((a)[3]))
#define PIN8(a) asm volatile("" : "+v"((a)[0]), "+v"((a)[1]), "+v"((a)[2]), "+v"((a)[3]), "+v"((a)[4]), "+v"((a)[5]), "+v"((a)[6]), "+v"((a)[7]))

#define POLL_HEAD "s_mov_b32 %[n], %[cap]\n\t1:\n\t"
#define POLL_TAIL "s_cbranch_execz 2f\n\ts_sub_u32 %[n], %[n], 1\n\ts_cbranch_scc0 1b\n\t2:\n\ts_mov_b64 exec, -1\n\ts_nop 2"
__device__ __forceinline__ v4u poll4(rsrc_t r, int off) {
  v4u a; u32 n;
  asm volatile(POLL_HEAD "buffer_load_dwordx4 v[124:127], %[o], %[r], 0 offen sc1\n\ts_waitcnt vmcnt(0)\n\t"
               "v_max_u32 v123, v124, v125\n\tv_max3_u32 v123, v123, v126, v127\n\tv_cmpx_eq_u32 vcc, -1, v123\n\t" POLL_TAIL
               : "=&{v[124:127]}"(a), [n] "=&s"(n) : [o] "v"(off), [r] "s"(r), [cap] "i"(SPIN_CAP) : "memory", "vcc", "scc", "v123");
  return a;
}
template <int D> __device__ __forceinline__ void poll4x2(rsrc_t r, int off, v4u& a, v4u& b) {
  u32 n;
  asm volatile(POLL_HEAD "buffer_load_dwordx4 v[120:123], %[o], %[r], 0 offen sc1\n\tbuffer_load_dwordx4 v[124:127], %[o], %[r], 0 offen offset:%[d] sc1\n\ts_waitcnt vmcnt(1)\n\t"
               "v_max_u32 v119, v120, v121\n\tv_max3_u32 v119, v119, v122, v123\n\ts_waitcnt vmcnt(0)\n\tv_max3_u32 v119, v119, v124, v125\n\tv_max3_u32 v119, v119, v126, v127\n\t"
               "v_cmpx_eq_u32 vcc, -1, v119\n\t" POLL_TAIL
               : "=&{v[120:123]}"(a), "=&{v[124:127]}"(b), [n] "=&s"(n) : [o] "v"(off), [r] "s"(r), [cap] "i"(SPIN_CAP), [d] "i"(D) : "memory", "vcc", "scc", "v119");
}
__device__ __forceinline__ void poll4x4(rsrc_t r, int off, int off2, v4u& a, v4u& b, v4u& c, v4u& d) {
  u32 n;
  asm volatile(POLL_HEAD "buffer_load_dwordx4 v[112:115], %[o], %[r], 0 offen sc1\n\tbuffer_load_dwordx4 v[116:119], %[o], %[r], 0 offen offset:16 sc1\n\t"
               "buffer_load_dwordx4 v[120:123], %[o2], %[r], 0 offen sc1\n\tbuffer_load_dwordx4 v[124:127], %[o2], %[r], 0 offen offset:16 sc1\n\t"
               "s_waitcnt vmcnt(3)\n\tv_max_u32 v110, v112, v113\n\tv_max3_u32 v110, v110, v114, v115\n\t"
               "s_waitcnt vmcnt(2)\n\tv_max_u32 v111, v116, v117\n\tv_max3_u32 v111, v111, v118, v119\n\t"
               "s_waitcnt vmcnt(1)\n\tv_max3_u32 v110, v110, v120, v121\n\tv_max3_u32 v111, v111, v122, v123\n\t"
               "s_waitcnt vmcnt(0)\n\tv_max3_u32 v110, v110, v124, v125\n\tv_max3_u32 v111, v111, v126, v127\n\t"
               "v_max_u32 v110, v110, v111\n\tv_cmpx_eq_u32 vcc, -1, v110\n\t" POLL_TAIL
               : "=&{v[112:115]}"(a), "=&{v[116:119]}"(b), "=&{v[120:123]}"(c), "=&{v[124:127]}"(d), [n] "=&s"(n)
               : [o] "v"(off), [o2] "v"(off2), [r] "s"(r), [cap] "i"(SPIN_CAP) : "memory", "vcc", "scc", "v110", "v111");
}
__device__ __forceinline__ v2u poll2(rsrc_t r, int off) {
  v2u a; u32 n;
  asm volatile(POLL_HEAD "buffer_load_dwordx2 v[126:127], %[o], %[r], 0 offen sc1\n\ts_waitcnt vmcnt(0)\n\t"
               "v_max_u32 v125, v126, v127\n\tv_cmpx_eq_u32 vcc, -1, v125\n\ts_cbranch_execz 2f\n\ts_sleep 1\n\ts_sub_u32 %[n], %[n], 1\n\ts_cbranch_scc0 1b\n\t2:\n\ts_mov_b64 exec, -1\n\ts_nop 2"
               : "=&{v[126:127]}"(a), [n] "=&s"(n) : [o] "v"(off), [r] "s"(r), [cap] "i"(SPIN_CAP) : "memory", "vcc", "scc", "v125");
  return a;
}
template <int D> __device__ __forceinline__ void poll1x2(rsrc_t r, int off, u32& a, u32& b) {
  u32 n;
  asm volatile(POLL_HEAD "buffer_load_dword v126, %[o], %[r], 0 offen sc1\n\tbuffer_load_dword v127, %[o], %[r], 0 offen offset:%[d] sc1\n\ts_waitcnt vmcnt(0)\n\t"
               "v_max_u32 v125, v126, v127\n\tv_cmpx_eq_u32 vcc, -1, v125\n\t" POLL_TAIL
               : "=&{v126}"(a), "=&{v127}"(b), [n] "=&s"(n) : [o] "v"(off), [r] "s"(r), [cap] "i"(SPIN_CAP), [d] "i"(D) : "memory", "vcc", "scc", "v125");
}
__device__ __forceinline__ void pollT4(rsrc_t r, int off, u32 tag, u32 ntag, v4u& a, v4u& b, v4u& c, v4u& d) {
  u32 n;
  asm volatile(POLL_HEAD "buffer_load_dwordx4 v[112:115], %[o], %[r], 0 offen sc1\n\tbuffer_load_dwordx4 v[116:119], %[o], %[r], 0 offen offset:512 sc1\n\t"
               "buffer_load_dwordx4 v[120:123], %[o], %[r], 0 offen offset:1024 sc1\n\tbuffer_load_dwordx4 v[124:127], %[o], %[r], 0 offen offset:1536 sc1\n\t"
               "s_waitcnt vmcnt(3)\n\tv_xor_b32 v110, v112, %[t]\n\tv_xor_b32 v111, v114, %[u]\n\t"
               "s_waitcnt vmcnt(2)\n\tv_bitop3_b32 v110, v116, %[t], v110 bitop3:0xbe\n\tv_bitop3_b32 v111, v118, %[u], v111 bitop3:0xbe\n\t"
               "s_waitcnt vmcnt(1)\n\tv_bitop3_b32 v110, v120, %[t], v110 bitop3:0xbe\n\tv_bitop3_b32 v111, v122, %[u], v111 bitop3:0xbe\n\t"
               "s_waitcnt vmcnt(0)\n\tv_bitop3_b32 v110, v124, %[t], v110 bitop3:0xbe\n\tv_bitop3_b32 v111, v126, %[u], v111 bitop3:0xbe\n\t"
               "v_or_b32 v110, v110, v111\n\tv_cmpx_ne_u32 vcc, 0, v110\n\t" POLL_TAIL
               : "=&{v[112:115]}"(a), "=&{v[116:119]}"(b), "=&{v[120:123]}"(c), "=&{v[124:127]}"(d), [n] "=&s"(n)
               : [o] "v"(off), [r] "s"(r), [cap] "i"(SPIN_CAP), [t] "v"(tag), [u] "v"(ntag) : "memory", "vcc", "scc", "v110", "v111");
}
__device__ __forceinline__ void lds_wait_ge(volatile LAS u32* f, u32 want) {
  _Pragma("nounroll") for (u32 s = 0; s < (SPIN_CAP << 3); ++s) { if (*f >= want) break; __builtin_amdgcn_s_sleep(1); }
  MEMFENCE();
}
__device__ __forceinline__ void lds_inc(volatile LAS u32* f, int lane) {
  asm volatile("s_mov_b64 exec, 1\n\tds_add_u32 %0, %1\n\ts_mov_b64 exec, -1" :: "v"((u32)(uintptr_t)f), "v"(1u) : "memory");
}
__device__ __forceinline__ u32 lds_ticket(volatile LAS u32* f, int lane) {
  u32 old;
  asm volatile("s_mov_b64 exec, 1\n\tds_add_rtn_u32 %0, %1, %2\n\ts_mov_b64 exec, -1\n\ts_waitcnt lgkmcnt(0)" : "=&v"(old) : "v"((u32)(uintptr_t)f), "v"(1u) : "memory");
  return __builtin_amdgcn_readfirstlane(old);
}
__device__ __forceinline__ float sum8(u32 addr) {
  const f32x4 t = *(const LAS f32x4*)(uintptr_t)addr + *(const LAS f32x4*)(uintptr_t)(addr + 16);
  return (t.x + t.z) + (t.y + t.w);
}
__device__ __forceinline__ float silu(float v) { return v / (1.f + __expf(-v)); }
__device__ __forceinline__ u32 silu2(u32 c) { return pk(silu(blo(c)), silu(bhi(c))); }
__device__ __forceinline__ void ln_stats(v4u xv, float& mb, float& inv) {
  constexpr u32 ONE2 = 0x3f803f80u;
  const float s = dot2(xv[0], ONE2, dot2(xv[1], ONE2, dot2(xv[2], ONE2, dot2(xv[3], ONE2, 0.f))));
  const float q = dot2(xv[0], xv[0], dot2(xv[1], xv[1], dot2(xv[2], xv[2], dot2(xv[3], xv[3], 0.f))));
  float t = row_sum(tr32(s, q));
  t = tr16(t, t);
  const float mean = rdlf(t, 0) * (1.f / 512.f), sq = rdlf(t, 32) * (1.f / 512.f);
  float var = fmaf(-mean, mean, sq);
  if (__builtin_expect(!(var * 64.f >= sq), 0)) {
    float d = 0.f;
#pragma unroll
    for (int i = 0; i < 4; ++i) { const float a = blo(xv[i]) - mean, b = bhi(xv[i]) - mean; d = fmaf(a, a, fmaf(b, b, d)); }
    d = row_sum(tr32(d, d));
    var = tr16(d, d) * (1.f / 512.f);
  }
  mb = rbf(mean);
  inv = __builtin_amdgcn_rcpf(rbf(__builtin_amdgcn_sqrtf(rbf(rbf(var) + 1e-5f))));
}
__device__ __forceinline__ u32 ln_pair(u32 x, float mb, float inv) { return pk2(up2(pk2(up2(x) - mb)) * inv); }
__device__ __forceinline__ f32x2 one_plus(u32 c) { return up2(pk2(up2(c) + 1.f)); }
__device__ __forceinline__ u32 modulate(u32 y, f32x2 c1, f32x2 sh) { return pk2(up2(pk2(up2(y) * c1)) + sh); }
__device__ __forceinline__ int tile_voff(int lane, int stride) { const int rho = lane >> 3; return (8 * (rho >> 2) + (rho & 3)) * stride + (lane & 7) * 16; }
template <int AUX> __device__ __forceinline__ void tile_load(rsrc_t r, int voff, int soff, int stride, v4u* w) {
#pragma unroll
  for (int i = 0; i < 4; ++i) w[i] = ld4<AUX>(r, voff, soff + (16 * (i & 1) + 4 * (i >> 1)) * stride);
}
__device__ __forceinline__ void tile_frags(LAS unsigned char* tb, int lane, const v4u* w, v4u* f) {
  const int rho = lane >> 3;
  LAS unsigned char* wr = tb + rho * 128 + (((lane & 7) ^ (rho & 6)) << 4);
  const int row = 4 * (lane >> 4) + ((lane >> 2) & 3);
  const LAS unsigned char* rd = tb + row * 128 + (lane & 3) * 8;
  const int sw = (row >> 1) & 3;
  v2u h[2][4];
#pragma unroll
  for (int t = 0; t < 2; ++t) {
    *(LAS v4u*)wr = w[2 * t]; *(LAS v4u*)(wr + 1024) = w[2 * t + 1];
    MEMFENCE();
#pragma unroll
    for (int ct = 0; ct < 4; ++ct) h[t][ct] = __builtin_bit_cast(v2u, __builtin_amdgcn_ds_read_tr16_b64_v4i16((LAS s16x4*)(rd + ((ct ^ sw) << 5))));
    MEMFENCE();
  }
#pragma unroll
  for (int ct = 0; ct < 4; ++ct) f[ct] = v4u{h[0][ct].x, h[0][ct].y, h[1][ct].x, h[1][ct].y};
  PIN4(f);
}
__device__ __forceinline__ void lds_frags(const LAS unsigned char* tile, int lane, v4u* f) {
  const int row = 4 * (lane >> 4) + ((lane >> 2) & 3);
  const LAS unsigned char* rd = tile + row * 128 + (lane & 3) * 8;
  const int sw = (row >> 1) & 3;
#pragma unroll
  for (int ct = 0; ct < 4; ++ct) {
    const v2u lo = __builtin_bit_cast(v2u, __builtin_amdgcn_ds_read_tr16_b64_v4i16((LAS s16x4*)(rd + ((ct ^ sw) << 5))));
    const v2u hi = __builtin_bit_cast(v2u, __builtin_amdgcn_ds_read_tr16_b64_v4i16((LAS s16x4*)(rd + 2048 + ((ct ^ sw) << 5))));
    f[ct] = v4u{lo.x, lo.y, hi.x, hi.y};
  }
  PIN4(f);
}
__device__ __forceinline__ f32x4 mfma32(v4u a, v4u b, f32x4 c) { return __builtin_amdgcn_mfma_f32_16x16x32_bf16(__builtin_bit_cast(bf16x8, a), __builtin_bit_cast(bf16x8, b), c, 0, 0, 0); }
template <int NS> __device__ __forceinline__ float gemv(const v4u* a, const v4u* f, int g) {
  f32x4 acc[4];
#pragma unroll
  for (int ct = 0; ct < 4; ++ct) acc[ct] = mfma32(a[0], f[ct], f32x4{0.f, 0.f, 0.f, 0.f});
#pragma unroll
  for (int s = 1; s < NS; ++s)
#pragma unroll
    for (int ct = 0; ct < 4; ++ct) acc[ct] = mfma32(a[s], f[4 * s + ct], acc[ct]);
  PIN4(acc);
  const float lo = (g & 1) ? acc[1][0] : acc[0][0], hi = (g & 1) ? acc[3][0] : acc[2][0];
  return (g & 2) ? hi : lo;
}
template <int B> __device__ __forceinline__ v4u bcast4(u32 v) { return v4u{dppu<BC + B>(v), dppu<BC + B + 1>(v), dppu<BC + B + 2>(v), dppu<BC + B + 3>(v)}; }

__global__ void __launch_bounds__(NTHR, 4) fused_kernel(Params) {
  __shared__ __attribute__((aligned(128))) unsigned char lds_raw[L_END];
  LAS unsigned char* L = (LAS unsigned char*)lds_raw;
  v16u ka; v8u kb8;
  int wave; u32 tab, ko0, ko1, ko2, ko3, ep, d1; v4u xs;
  {
    u32 sa, sb, sc, keep, va, vb; u64 pa;
#define DMA4(P) "global_load_lds_dwordx4 %[k0], " P KV_SFX "\n\tglobal_load_lds_dwordx4 %[k1], " P " offset:1024" KV_SFX "\n\t" \
                "global_load_lds_dwordx4 %[k2], " P " offset:2048" KV_SFX "\n\tglobal_load_lds_dwordx4 %[k3], " P " offset:3072" KV_SFX "\n\t"
    asm volatile("s_load_dwordx16 s[16:31], %[kp], 0x0\n\ts_load_dwordx8 s[36:43], %[kp], 0x40\n\t"
                 "s_lshr_b32 s46, %[bid], 3\n\ts_lshl_b32 s46, s46, 2\n\ts_add_u32 s46, s46, 0x60\n\ts_load_dword s46, %[kp], s46"
                 : "=&{s[16:31]}"(ka), "=&{s[36:43]}"(kb8), "=&{s46}"(tab) : [kp] "s"(__builtin_amdgcn_kernarg_segment_ptr()), [bid] "s"(blockIdx.x) : "scc");
    asm volatile(
      "s_lshr_b32 %[sa], %[bid], 3\n\t"
      "v_readfirstlane_b32 %[w], %[tid]\n\ts_mov_b32 %[keep], m0\n\ts_and_b32 %[sc], %[bid], 7\n\t"
      "s_lshr_b32 %[w], %[w], 6\n\ts_cmp_lt_u32 %[w], 8\n\ts_cbranch_scc1 1f\n\t"
      "v_bfe_u32 %[va], %[tid], 4, 2\n\tv_and_b32 %[vb], 15, %[tid]\n\tv_xor_b32 %[vb], %[vb], %[va]\n\tv_lshlrev_b32 %[vb], 4, %[vb]\n\t"
      "v_lshl_or_b32 %[k0], %[va], 8, %[vb]\n\tv_xor_b32 %[k1], 64, %[k0]\n\tv_xor_b32 %[k2], 0x80, %[k0]\n\tv_xor_b32 %[k3], 0xc0, %[k0]\n\t"
      "s_sub_u32 %[sb], %[w], 8\n\ts_lshl_b32 m0, %[sb], 14\n\ts_lshl_b32 %[sc], %[sc], 8\n\ts_add_u32 %[sb], %[sb], %[sc]\n\ts_lshl_b32 %[sb], %[sb], 1\n\t"
      "s_cmp_gt_u32 %[w], 11\n\ts_cselect_b32 %[sa], 0, %[sa]\n\ts_cmp_lt_u32 %[sa], 8\n\t"
      "s_waitcnt lgkmcnt(0)\n\ts_cbranch_scc1 9f\n\t"
      "s_and_b32 %[sc], s46, 0xffff\n\ts_add_u32 %[sb], %[sb], %[sc]\n\ts_lshl_b32 %[sb], %[sb], 13\n\t"
      "s_add_u32 s44, s18, %[sb]\n\ts_addc_u32 s45, s19, 0\n\t" DMA4("s[44:45]")
      "s_branch 9f\n\t"
      "1:\n\tv_bfe_u32 %[va], %[tid], 3, 3\n\tv_and_b32 %[vb], 6, %[va]\n\tv_xor_b32 %[vb], %[vb], %[tid]\n\tv_and_b32 %[vb], 7, %[vb]\n\tv_lshlrev_b32 %[vb], 4, %[vb]\n\t"
      "v_and_b32 %[k1], 4, %[va]\n\tv_add_u32 %[va], %[va], %[k1]\n\t"
      "v_and_b32 v106, 63, %[tid]\n\tv_lshlrev_b32 v106, 4, v106\n\tv_mov_b32 v104, 0\n\t"
      "v_and_b32 v105, 3, %[tid]\n\tv_lshlrev_b32 v105, 2, v105\n\tv_and_or_b32 v105, %[tid], 48, v105\n\t"
      "s_cmp_lt_u32 %[sa], 8\n\ts_cbranch_scc0 2f\n\t"
      "v_lshl_or_b32 %[k0], %[va], 13, %[vb]\n\tv_add_u32 %[k1], 0x1fc00, %[k0]\n\tv_add_u32 %[k2], 0x7800, %[k0]\n\tv_add_u32 %[k3], 0x27400, %[k0]\n\t"
      "v_bfe_u32 %[va], %[tid], 2, 1\n\tv_lshl_or_b32 v105, %[va], 6, v105\n\ts_lshl_b32 %[sb], %[w], 7\n\tv_add_u32 v105, %[sb], v105\n\t"
      "s_lshl_b32 m0, %[w], 13\n\ts_lshl_b32 %[sb], %[w], 9\n\ts_add_u32 %[sb], %[sb], %[sc]\n\ts_lshl_b32 %[sb], %[sb], 3\n\ts_add_u32 %[sb], %[sb], %[sa]\n\ts_lshl_b32 %[sb], %[sb], 7\n\t"
      "s_waitcnt lgkmcnt(0)\n\t"
      "global_load_dword v105, v105, s[16:17]\n\tglobal_load_dwordx4 v[106:109], v106, s[16:17]\n\tglobal_load_dword v104, v104, s[42:43] sc1\n\t"
      "s_add_u32 s44, s22, %[sb]\n\ts_addc_u32 s45, s23, 0\n\t" DMA4("s[44:45]")
      "s_add_u32 s44, s44, 0x40000\n\ts_addc_u32 s45, s45, 0\n\ts_add_u32 m0, m0, 0x1000\n\ts_nop 0\n\t" DMA4("s[44:45]")
      "s_branch 9f\n\t"
      "2:\n\tv_mul_u32_u24 %[k0], 0x1800, %[va]\n\tv_add_u32 %[k0], %[k0], %[vb]\n\tv_add_u32 %[k1], 0x17c00, %[k0]\n\tv_add_u32 %[k2], 0x5800, %[k0]\n\tv_add_u32 %[k3], 0x1d400, %[k0]\n\t"
      "s_sub_u32 %[sb], %[sa], 23\n\ts_cmp_lt_u32 %[sb], 4\n\ts_cbranch_scc0 3f\n\t"
      "v_bfe_u32 %[va], %[tid], 2, 1\n\tv_lshl_or_b32 v105, %[va], 6, v105\n\ts_lshl_b32 s44, %[w], 7\n\tv_add_u32 v105, s44, v105\n\t"
      "s_lshl_b32 s44, %[w], 13\n\ts_add_u32 m0, s44, 0x10000\n\ts_lshl_b32 %[sc], %[sc], 2\n\ts_add_u32 %[sb], %[sb], %[sc]\n\ts_add_u32 %[sb], %[sb], 16\n\ts_lshl_b32 %[sb], %[sb], 7\n\t"
      "s_mul_i32 %[sc], %[w], 0x60000\n\ts_add_u32 %[sb], %[sb], %[sc]\n\t"
      "s_waitcnt lgkmcnt(0)\n\t"
      "global_load_dword v105, v105, s[36:37]\n\tglobal_load_dword v104, v104, s[42:43] sc1\n\t"
      "s_add_u32 s44, s38, %[sb]\n\ts_addc_u32 s45, s39, 0\n\t" DMA4("s[44:45]")
      "s_add_u32 s44, s44, 0x30000\n\ts_addc_u32 s45, s45, 0\n\ts_add_u32 m0, m0, 0x1000\n\ts_nop 0\n\t" DMA4("s[44:45]")
      "s_branch 9f\n\t3:\n\t"
      "s_and_b32 %[sb], %[sa], 3\n\ts_lshl_b32 %[sb], %[sb], 2\n\ts_and_b32 s44, %[w], 3\n\ts_add_u32 %[sb], %[sb], s44\n\t"
      "s_lshl_b32 s44, s44, 12\n\ts_add_u32 m0, s44, %[tb]\n\ts_lshl_b32 s44, %[sb], 6\n\tv_add_u32 v105, s44, v105\n\t"
      "s_mul_i32 %[sb], %[sb], 0x30000\n\ts_lshl_b32 %[sc], %[sc], 8\n\ts_add_u32 %[sb], %[sb], %[sc]\n\ts_bfe_u32 %[sc], %[sa], 0x10002\n\ts_lshl_b32 %[sc], %[sc], 7\n\ts_add_u32 %[sb], %[sb], %[sc]\n\t"
      "s_lshr_b32 %[sc], %[w], 2\n\ts_lshr_b32 %[sa], %[sa], 3\n\ts_cmp_eq_u32 %[sa], 1\n\ts_cselect_b32 %[sa], %[sc], 0\n\ts_cmp_eq_u32 %[sa], 1\n\t"
      "s_waitcnt lgkmcnt(0)\n\t"
      "global_load_dword v105, v105, s[36:37]\n\tglobal_load_dword v104, v104, s[42:43] sc1\n\ts_cbranch_scc0 9f\n\t"
      "s_add_u32 s44, s38, %[sb]\n\ts_addc_u32 s45, s39, 0\n\t" DMA4("s[44:45]")
      "9:\n\ts_mov_b32 m0, %[keep]"
      : "=&{s[44:45]}"(pa), [w] "=&s"(wave), [k0] "=&v"(ko0), [k1] "=&v"(ko1), [k2] "=&v"(ko2), [k3] "=&v"(ko3),
        "=&{v104}"(ep), "=&{v105}"(d1), "=&{v[106:109]}"(xs), [sa] "=&s"(sa), [sb] "=&s"(sb), [sc] "=&s"(sc), [keep] "=&s"(keep), [va] "=&v"(va), [vb] "=&v"(vb)
      : "{s[16:31]}"(ka), "{s[36:43]}"(kb8), "{s46}"(tab), [bid] "s"(blockIdx.x), [tid] "v"(threadIdx.x), [tb] "i"(L_TB)
      : "memory", "scc");
    asm volatile("" : "+{s[16:31]}"(ka), "+{s[36:43]}"(kb8), "+{s46}"(tab));
  }
  volatile LAS u32* FL = (volatile LAS u32*)(L + L_FLAG);
  LAS float* RED = (LAS float*)(L + L_RED);
  const int lane = threadIdx.x & 63;
  const int bid = blockIdx.x, xc = bid & 7, ix = bid >> 3, rb = ix & 7;
  const int g = lane >> 4, n = lane & 15;
  const bool chain = ix < 8, wcu = (ix >> 3) == 1;
  const int toff = tab & 0xffff, nhalf = tab >> 16, ntask = (nhalf + 1) >> 1;
  const bool ninth = wave == 2 && ntask == 9;
  const int aw = ninth ? 8 : wave - 8;
  const bool head = (wave >> 2) == 2 && !chain, p1 = wcu && (wave >> 2) == 1;
  const u32 ldst = (u32)(uintptr_t)L + L_K + aw * 16384;
#define KGROUP(kb, lo) do { u32 keep_; \
    asm volatile("s_mov_b32 %0, m0\n\ts_mov_b32 m0, %6\n\ts_nop 0\n\t" \
                 "global_load_lds_dwordx4 %1, %5" KV_SFX "\n\t" \
                 "global_load_lds_dwordx4 %2, %5 offset:1024" KV_SFX "\n\t" \
                 "global_load_lds_dwordx4 %3, %5 offset:2048" KV_SFX "\n\t" \
                 "global_load_lds_dwordx4 %4, %5 offset:3072" KV_SFX "\n\t" \
                 "s_mov_b32 m0, %0" \
                 : "=&s"(keep_) : "v"(ko0), "v"(ko1), "v"(ko2), "v"(ko3), "s"(kb), "s"(ldst + (lo)) : "memory"); } while (0)
#define PTR(v, i) (((u64)(v)[2 * (i) + 1] << 32) | (v)[2 * (i)])
  Ptrs p;
  p.x = (const u16*)PTR(ka, 0); p.K = (const u16*)PTR(ka, 1); p.V = (const u16*)PTR(ka, 2); p.wq = (const u16*)PTR(ka, 3); p.wo = (const u16*)PTR(ka, 4);
  p.wg = (const u16*)PTR(ka, 5); p.wu = (const u16*)PTR(ka, 6); p.wd = (const u16*)PTR(ka, 7);
  p.c = (const u16*)PTR(kb8, 0); p.wmod = (const u16*)PTR(kb8, 1); p.out = (u16*)PTR(kb8, 2); p.ws = (void*)PTR(kb8, 3);
#define MKWS() const rsrc_t ws = mkr(p.ws, WS_BYTES)
  if (wave == 0 && lane < 16) FL[lane] = (lane == 0 && ix >= 16) ? 1u : 0u;
  BARRIER();

  MKWS();
  const bool gcu = ix >= 16 && ix < 23, mcu = ix >= 23 && ix < 27;
  u32 tag = 0;
  if (__builtin_expect(wave < 8 && !ninth, 1)) {
  if (wave >= 4 && !(chain || wcu || gcu || mcu)) return;
  __builtin_amdgcn_s_setprio(3);
  asm volatile("s_waitcnt vmcnt(8)\n\ts_cmp_eq_u32 %3, 2\n\ts_cbranch_scc1 1f\n\ts_waitcnt vmcnt(4)\n\ts_cmp_eq_u32 %3, 1\n\ts_cbranch_scc1 1f\n\ts_waitcnt vmcnt(0)\n\t1:"
               : "+{v104}"(ep), "+{v105}"(d1), "+{v[106:109]}"(xs) : "s"(__builtin_amdgcn_readfirstlane((chain || mcu) ? 2 : p1 ? 1 : 0)) : "memory", "scc");
  const int cg = xc * 2 + (rb >> 2), ks = rb & 3;
  const int pi = ((n >> 2) & 1) * 16 + g * 4 + (n & 3);
  tag = ep + 1u;
  if (__builtin_expect(p1, 1)) {
    v4u a = bcast4<0>(silu2(d1)), f[4];
    int ho = H0_OFF + (cg * 4 + ks) * 512 + lane * 8;
    PIN1(a); PIN1(ho);
    VM0();
    lds_frags(L + L_TB + (wave & 3) * 4096, lane, f);
    RED[(wave & 3) * 64 + lane] = gemv<1>(&a, f, g);
    if (lds_ticket(&FL[3], lane) == 3u) {
      const float s = (RED[lane] + RED[64 + lane]) + (RED[128 + lane] + RED[192 + lane]);
      const float s_hi = dpp<SHL1>(s);
      if (!(lane & 1)) stc(ws, ho, v4u{tag, asu(s), ~tag, asu(s_hi)});
      MEMFENCE();
      if (lane == 0) FL[0] = 1u;
    }
  }
  if (__builtin_expect(chain, 1)) {
    float mb, inv;
    ln_stats(xs, mb, inv);
    f32x2 y = up2(ln_pair(d1, mb, inv));
    PIN1(y);
    VM0();
    v4u f[8];
    lds_frags(L + L_K + wave * 8192, lane, f); lds_frags(L + L_K + wave * 8192 + 4096, lane, f + 4);
    SCHED();
    v4u h0, h1, h2, h3;
    pollT4(ws, H0_OFF + (wave + (n >> 3) * 8) * 2048 + pi * 16, tag, ~tag, h0, h1, h2, h3);
    const u32 md = pk((asf(h0.y) + asf(h1.y)) + (asf(h2.y) + asf(h3.y)), (asf(h0.w) + asf(h1.w)) + (asf(h2.w) + asf(h3.w)));
    const u32 ot = dppu<ROR8>(md), sa = (n & 8) ? ot : md, ca = (n & 8) ? md : ot;
    const u32 hp = pk2(up2(pk2(y * one_plus(ca))) + up2(sa));
    const v4u a[2] = {bcast4<0>(hp), bcast4<4>(hp)};
    RED[lane * 8 + wave] = gemv<2>(a, f, g);
    if (lds_ticket(&FL[7], lane) == 7u) {
      const float s = sum8((u32)(uintptr_t)(RED + lane * 8));
      const u32 d = pk(s, dpp<SHL1>(s)), d2 = dppu<SHL2>(d);
      if ((lane & 3) == 0) stc(ws, Q_OFF + (xc * 8 + rb) * 256 + lane * 4, v4u{tag, d, ~tag, d2});
      MEMFENCE();
      if (lane == 0) FL[0] = 1u;
    }
  }
  if (mcu) {
    const u32 sp = silu2(d1);
    const v4u a[2] = {bcast4<0>(sp), bcast4<4>(sp)};
    VM0();
    v4u f[8];
    lds_frags(L + 65536 + wave * 8192, lane, f); lds_frags(L + 65536 + wave * 8192 + 4096, lane, f + 4);
    RED[lane * 8 + wave] = gemv<2>(a, f, g);
    MEMFENCE();
    lds_inc(&FL[3], lane);
    if (wave == 1) {
      lds_wait_ge(&FL[3], 8u);
      const float s = sum8((u32)(uintptr_t)(RED + lane * 8));
      const u32 d = nosent(pk(s, dpp<SHL1>(s)));
      lds_wait_ge(&FL[5], 1u);
      if (!(lane & 1)) stc1(ws, MR_OFF + (xc * 4 + (ix - 23)) * 128 + lane * 2, d);
    }
  }
  if (wcu && wave == 1) {
    lds_wait_ge(&FL[0], 1u);
    const v4u sv = {SENT, SENT, SENT, SENT};
#pragma unroll
    for (int j = 0; j < CLR_KB; ++j) stc(ws, S_OFF + (xc * 8 + rb) * (CLR_KB * 1024) + j * 1024 + lane * 16, sv);
    VM0();
    if (lane == 0) stc2(ws, CF_OFF + (xc * 8 + rb) * 16, v2u{tag, ~tag});
  }
  if (wave == 0) {
    const int qb = Q_OFF + xc * 2048 + lane * 16;
    if (!chain && !mcu) __builtin_amdgcn_s_sleep(100);
    _Pragma("nounroll") for (u32 s = 0; s < SPIN_CAP; ++s) {
      const v4u q0 = ldc(ws, qb), q1 = ldc(ws, qb + 1024);
      SCHED();
      if (__all((((q0.x ^ tag) | (q1.x ^ tag)) | ((q0.z ^ ~tag) | (q1.z ^ ~tag))) == 0u)) {
        LAS unsigned char* qd = L + L_Q + (lane >> 5) * 320 + (lane & 31) * 8;
        *(LAS v2u*)qd = v2u{q0.y, q0.w};
        *(LAS v2u*)(qd + 640) = v2u{q1.y, q1.w};
        LGKM0();
        if (lane == 0) FL[1] = 1u;
        break;
      }
      MEMFENCE();
    }
  }
  if (wave == 3) {
    {
      __builtin_amdgcn_s_sleep(100);
      _Pragma("nounroll") for (u32 s = 0; s < SPIN_CAP; ++s) {
        const v2u cf = __builtin_amdgcn_raw_buffer_load_b64(ws, CF_OFF + lane * 16, 0, SC1);
        if (__all(((cf.x ^ tag) | (cf.y ^ ~tag)) == 0u)) break;
        __builtin_amdgcn_s_sleep(16);
        MEMFENCE();
      }
      if (lane == 0) FL[5] = 1u;
    }
  }
  }
  else {
    const bool early = aw < 4;
    if (ninth) {
      VM0();
      const u32 kv0 = g * 256 + ((n ^ g) << 4);
      ko0 = kv0; ko1 = kv0 ^ 64u; ko2 = kv0 ^ 128u; ko3 = kv0 ^ 192u;
    }
    if (aw >= ntask) return;
    if (chain) lds_wait_ge(&FL[0], 1u);
    if (!early) {
      lds_wait_ge(&FL[3], (ix >= 23 && ix < 27) ? 8u : (wcu && ninth) ? 4u : 0u);
      _Pragma("nounroll") for (u32 s = 0; s < (SPIN_CAP << 3); ++s) { if (FL[2] >= 4u || FL[1] != 0u) break; __builtin_amdgcn_s_sleep(1); }
      MEMFENCE();
    }
    const int hh = n >> 2, e2 = n & 3;
    u32 qseen = (!early && ix >= 16) ? 1u : 0u;
    const u32 key0 = (u32)(xc * 16384 + toff * 32 + aw * 64);
    const bool halft = 2 * aw + 1 == nhalf;
#define THROTTLE() do { if (!qseen) { const v2u fl_ = *(volatile LAS v2u*)FL; qseen = __builtin_amdgcn_readfirstlane(fl_.y); \
      if (!qseen) { if (__builtin_amdgcn_readfirstlane(fl_.x)) asm volatile("s_waitcnt vmcnt(%0)" :: "i"(THR_W - 4) : "memory"); else asm volatile("s_waitcnt vmcnt(%0)" :: "i"(THR_Q - 4) : "memory"); } } SCHED(); } while (0)
    const u64 kb = (u64)p.K + (u64)key0 * 256;
    if (!head) KGROUP(kb, 0);
    THROTTLE(); KGROUP(kb + 4096, 4096);
    THROTTLE();
    if (!halft) { KGROUP(kb + 8192, 8192); THROTTLE(); KGROUP(kb + 12288, 12288); THROTTLE(); }
    v4u vv[16];
    {
      const rsrc_t rv = mkr(p.V, 8 * 16384 * 256);
      const int vo = (halft && g >= 2) ? 0x40000000 : g * 4096 + n * 16;
#pragma unroll
      for (int i = 0; i < 16; ++i) { vv[i] = ld4<NT>(rv, vo + i * 256, key0 * 256); if ((i & 3) == 3 && i < 15) THROTTLE(); }
    }
    SCHED();
    lds_inc(&FL[2], lane);
    lds_wait_ge(&FL[1], 1u);
    asm volatile("s_waitcnt vmcnt(16)" ::: "memory");
    SCHED();
    float s2[4], pe[4];
    {
      const LAS unsigned char* kbase = L + L_K + aw * 16384 + n * 256;
      const LAS unsigned char* qa = L + L_Q + (n & 3) * 320 + g * 16;
      f32x4 sa[4];
#pragma unroll
      for (int t = 0; t < 4; ++t) sa[t] = f32x4{0.f, 0.f, 0.f, 0.f};
#pragma unroll
      for (int s = 0; s < 4; ++s) {
        const v4u a = *(const LAS v4u*)(qa + 64 * s);
#pragma unroll
        for (int t = 0; t < 4; ++t) sa[t] = mfma32(a, *(const LAS v4u*)(kbase + t * 4096 + (((4 * s + g) ^ n) << 4)), sa[t]);
      }
      const f32x4 lo = (g & 1) ? sa[1] : sa[0], hi = (g & 1) ? sa[3] : sa[2], sacc = (g & 2) ? hi : lo;
      constexpr float SC = 0.08838834764831845f;
      const f32x2 c = up2(pk2(up2(pk(sacc[0], sacc[1])) * SC)), d = up2(pk2(up2(pk(sacc[2], sacc[3])) * SC));
      const bool dead = halft && g >= 2;
      s2[0] = dead ? -__builtin_inff() : c.x; s2[1] = dead ? -__builtin_inff() : c.y; s2[2] = dead ? -__builtin_inff() : d.x; s2[3] = dead ? -__builtin_inff() : d.y;
    }
    const float mrow = row_max(tm16(tm32(s2[0], s2[2]), tm32(s2[1], s2[3])));
#pragma unroll
    for (int h2 = 0; h2 < 4; ++h2) pe[h2] = __builtin_amdgcn_exp2f((s2[h2] - rdlf(mrow, 16 * h2)) * LOG2E);
    const u32 w0 = pk(pe[0], pe[1]), w1 = pk(pe[2], pe[3]);
    const float lrow = row_sum(tr16(tr32(pe[0], pe[2]), tr32(pe[1], pe[3])));
    if (n == 0) *(LAS f32x2*)(L + L_ML + g * 128 + aw * 8) = f32x2{mrow, lrow};
    MEMFENCE();
    lds_inc(&FL[4], lane);
    const u32 two = (u32)(2 * hh) | ((u32)(2 * hh + 1) << 8);
    const u32 selv = (e2 & 1) ? ((two << 16) | 0x0C0Cu) : (0x0C0C0000u | two);
    const u32 sel0 = (e2 >> 1) == 0 ? selv : 0x0C0C0C0Cu, sel1 = (e2 >> 1) == 1 ? selv : 0x0C0C0C0Cu;
    v2u xb[16];
#define PV_PREP(i) { const u32 b0_ = dppu<BC + (i)>(w0), b1_ = dppu<BC + (i)>(w1); xb[i] = v2u{__builtin_amdgcn_perm(b1_, b0_, sel0), __builtin_amdgcn_perm(b1_, b0_, sel1)}; }
    PV_PREP(0) PV_PREP(1) PV_PREP(2) PV_PREP(3) PV_PREP(4) PV_PREP(5) PV_PREP(6) PV_PREP(7)
    PV_PREP(8) PV_PREP(9) PV_PREP(10) PV_PREP(11) PV_PREP(12) PV_PREP(13) PV_PREP(14) PV_PREP(15)
#undef PV_PREP
    u32 obw = (u32)(uintptr_t)L + L_K + aw * 16384 + (hh * 128 + e2 * 16 + 4 * g) * 4;
    PIN1(obw);
    SCHED();
    f32x4 o1 = {0.f, 0.f, 0.f, 0.f}, o2 = {0.f, 0.f, 0.f, 0.f};
#pragma unroll
    for (int i = 0; i < 16; ++i) {
      o1 = __builtin_amdgcn_mfma_f32_16x16x16bf16_1k(__builtin_bit_cast(s16x4, v2u{vv[i].x, vv[i].y}), __builtin_bit_cast(s16x4, xb[i]), o1, 0, 0, 0);
      o2 = __builtin_amdgcn_mfma_f32_16x16x16bf16_1k(__builtin_bit_cast(s16x4, v2u{vv[i].z, vv[i].w}), __builtin_bit_cast(s16x4, xb[i]), o2, 0, 0, 0);
    }
    *(LAS f32x4*)(uintptr_t)obw = o1; *(LAS f32x4*)(uintptr_t)(obw + 256) = o2;
    if (aw < 2 || (aw > 3 && aw < 8) || (ninth && !gcu)) { BARRIER(); return; }
    if (!ninth) {
    const int tau = (aw - 2) * 64 + lane, h = tau >> 5;
    float f[9];
    lds_wait_ge(&FL[5], 1u);
    lds_wait_ge(&FL[4], (u32)ntask);
    {
      float m[10], l[10];
#pragma unroll
      for (int w = 0; w < 5; ++w) { const f32x4 t = *(const LAS f32x4*)(L + L_ML + h * 128 + w * 16); m[2 * w] = t.x; l[2 * w] = t.y; m[2 * w + 1] = t.z; l[2 * w + 1] = t.w; }
#pragma unroll
      for (int w = 4; w < 9; ++w) { m[w] = w < ntask ? m[w] : -__builtin_inff(); l[w] = w < ntask ? l[w] : 0.f; }
      const float M = mx(mx(mx(mx(m[0], m[1]), mx(m[2], m[3])), mx(mx(m[4], m[5]), mx(m[6], m[7]))), m[8]);
      float Ls = 0.f;
#pragma unroll
      for (int w = 0; w < 9; ++w) { f[w] = __builtin_amdgcn_exp2f((m[w] - M) * LOG2E); Ls = fmaf(l[w], f[w], Ls); PIN1(f[w]); }
      if ((lane & 31) == 0) stc2(ws, ml_at(xc) + (ix * 2 + aw - 2) * 128 + (lane >> 5) * 8, v2u{asu(M), asu(Ls)});
    }
    int t1o = t1_at(xc, ((h * 4 + ((tau >> 3) & 3)) * 32 + ix) * 128 + (tau & 7) * 16);
    u32 pa[9];
#pragma unroll
    for (int w = 0; w < 9; ++w) { pa[w] = (u32)(uintptr_t)(L + L_K) + tau * 16 + (w < ntask ? w : 0) * 16384; PIN1(pa[w]); }
    PIN1(t1o);
    __builtin_amdgcn_s_setprio(3);
    BARRIER();
    f32x4 part[9];
#pragma unroll
    for (int w = 0; w < 9; ++w) part[w] = *(const LAS f32x4*)(uintptr_t)pa[w];
    const f32x4 acc = (((part[0] * f[0] + part[1] * f[1]) + (part[2] * f[2] + part[3] * f[3])) + ((part[4] * f[4] + part[5] * f[5]) + (part[6] * f[6] + part[7] * f[7]))) + part[8] * f[8];
    stc(ws, t1o, __builtin_bit_cast(v4u, acc));
    lds_inc(&FL[8], lane);
    return;
    }
    __builtin_amdgcn_s_setprio(3);
  }

  LAS unsigned char* tb = L + L_K + wave * 16384 + 8192;
  if (ix < 16) {
    if (wave < 4) return;
    const int x = wave & 3, hl = ix >> 2, hg = xc * 4 + hl, qd = ix & 3;
    const rsrc_t ro = mkr(p.wo, 4096 * 512 * 2);
    const int col = xc * 64 + rb * 8;
    lds_wait_ge(&FL[2], (u32)ntask);
    v4u w[8];
    {
      const int rho = lane >> 3, vo = (64 * (rho >> 2) + 8 * (rho & 3)) * 1024 + (lane & 7) * 16;
#pragma unroll
      for (int i = 0; i < 8; ++i) w[i] = ld4<NT>(ro, vo, (hg * 128 + 32 * ((i >> 1) & 1) + 2 * qd + (i & 1)) * 1024 + x * 256 + (i >> 2) * 128);
    }
    BARRIER();
    lds_wait_ge(&FL[8], 2u);
    const v2u ma = poll2(ws, ml_at(xc) + ((lane >> 1) * 2 + (hl >> 1)) * 128 + (hl & 1) * 8);
    v4u f[8];
    tile_frags(tb, lane, w, f); tile_frags(tb, lane, w + 4, f + 4);
    int ob = t1_at(xc, ((hl * 4 + qd) * 32 + (lane >> 1)) * 128 + x * 32 + (lane & 1) * 16);
    int t2o = T2_OFF + row_at(xc * 16 + ix, x * 512 + lane * 4);
    float M = asf(ma.x);
    M = mx(M, dpp<XOR2>(M)); M = mx(M, dpp<ROR4>(M)); M = mx(M, dpp<ROR8>(M)); M = tm16(M, M); M = tm32(M, M);
    float fa = __builtin_amdgcn_exp2f((asf(ma.x) - M) * LOG2E);
    float Ls = fa * asf(ma.y);
    Ls = tr32(Ls, Ls); Ls = tr16(Ls, Ls); Ls += dpp<ROR8>(Ls); Ls += dpp<ROR4>(Ls); Ls += dpp<XOR2>(Ls);
    float inv = __builtin_amdgcn_rcpf(Ls);
    u32 oa = (u32)(uintptr_t)(L + L_X) + x * 16 + (lane & 1) * 8 + (lane >> 5) * 4;
    u32 oa2 = (u32)(uintptr_t)(L + L_X) + g * 16;
    fa *= inv;
    PIN1(fa); PIN1(ob); PIN1(t2o); PIN1(oa); PIN1(oa2);
    SCHED();
    const v4u a = poll4(ws, ob);
    float u0 = tr32(fa * asf(a.x), fa * asf(a.z)), u1 = tr32(fa * asf(a.y), fa * asf(a.w));
    u0 = tr16(u0, u0); u1 = tr16(u1, u1);
    u0 += dpp<ROR8>(u0); u1 += dpp<ROR8>(u1);
    u0 += dpp<ROR4>(u0); u1 += dpp<ROR4>(u1);
    u0 += dpp<XOR2>(u0); u1 += dpp<XOR2>(u1);
    if (!(lane & 30)) *(LAS u32*)(uintptr_t)oa = pk(u0, u1);
    BARRIER();
    const v4u av = *(const LAS v4u*)(uintptr_t)oa2;
    const float y0 = gemv<1>(&av, f, g), y1 = gemv<1>(&av, f + 4, g);
    stc_row(ws, t2o, y0, lane); stc_row(ws, t2o + 256, y1, lane);
    if (!chain || wave != 4) return;
    f32x2 xin = up2(*(const GAS u32*)((const GAS u16*)p.x + col + 2 * g));
    u32 gaw, gbw;
    poll1x2<3072>(ws, MR_OFF + (col + 2 * g) * 2, gaw, gbw);
    f32x2 ga = up2(gaw), gb = up2(gbw);
    int tio = T2_OFF + row_at(lane, col * 4), tio2 = tio + 131072, yo = lane < 56 ? Y_OFF + row_at(lane, col * 4) : 0x40000000;
    PIN1(ga); PIN1(gb); PIN1(xin); PIN1(tio); PIN1(tio2); PIN1(yo);
    v4u pa, pb, pc, pd;
    poll4x4(ws, tio, tio2, pa, pb, pc, pd);
    pa = __builtin_bit_cast(v4u, __builtin_bit_cast(f32x4, pa) + __builtin_bit_cast(f32x4, pc));
    pb = __builtin_bit_cast(v4u, __builtin_bit_cast(f32x4, pb) + __builtin_bit_cast(f32x4, pd));
    const u32 x1 = nosent(pk2(xin + up2(pk2(ga * up2(pk2(sum8x64(pa, pb)))))));
    const v4u o = {rdl(x1, 0), rdl(x1, 16), rdl(x1, 32), rdl(x1, 48)};
    if (lane < 2) stc(ws, X1_OFF + lane * 8192 + col * 16, o);
    if (bid == 0 && lane == 0) stc1(ws, EPOCH_OFF, tag);
    f32x2 x1f = up2(x1);
    PIN1(x1f);
    poll4x2<16>(ws, yo, pa, pb);
    const u32 d = pk2(x1f + up2(pk2(gb * up2(pk2(sum8x64(pa, pb))))));
    if (!n) *(GAS u32*)((GAS u16*)p.out + col + 2 * g) = d;
  } else if (gcu) {
    const int gi = xc * 7 + (ix - 16);
    const rsrc_t rd = mkr(p.wd, 1792 * 512 * 2);
    BARRIER();
    lds_wait_ge(&FL[8], 2u);
    v4u w[12];
    {
      const int rho = lane >> 3;
      const GAS unsigned char* src = (const GAS unsigned char*)((lane & 4) ? p.wu : p.wg) + (size_t)(wave * 64 + 8 * (rho >> 2) + (rho & 3)) * 3584 + gi * 64 + (lane & 3) * 16;
#pragma unroll
      for (int i = 0; i < 8; ++i) w[i] = *(const GAS v4u*)(src + (32 * (i >> 2) + 16 * (i & 1) + 4 * ((i >> 1) & 1)) * 3584);
    }
    tile_load<NT>(rd, tile_voff(lane, 1024), gi * 32 * 1024 + wave * 128, 1024, w + 8);
    v4u f[12];
    tile_frags(tb, lane, w, f); tile_frags(tb, lane, w + 4, f + 4); tile_frags(tb, lane, w + 8, f + 8);
    int yo = Y_OFF + row_at(gi, wave * 256 + lane * 4);
    u32 aa = (u32)(uintptr_t)(L + L_X) + wave * 128 + g * 16, ra = (u32)(uintptr_t)(RED + lane * 8 + wave), sa = (u32)(uintptr_t)(RED + ((n >> 3) * 32 + g * 8 + (n & 7)) * 8);
    u32 ha = (u32)(uintptr_t)(L + L_X) + lane * 16 + wave * 4;
    PIN1(yo); PIN1(aa); PIN1(ra); PIN1(sa); PIN1(ha);
    SCHED();
    if (wave < 4) {
#define SEL4(v) ((wave & 2) ? ((wave & 1) ? (v).w : (v).z) : ((wave & 1) ? (v).y : (v).x))
      v4u sbw, cbw;
      poll4x2<1024>(ws, MR_OFF + 1024 + lane * 16, sbw, cbw);
      f32x2 sb = up2(SEL4(sbw)), c1 = one_plus(SEL4(cbw));
      int xb1 = X1_OFF + (((xc >> 2) ^ SIDE_XOR) << 13) + lane * 128;
      PIN1(sb); PIN1(c1); PIN1(xb1);
      const v4u x1v = poll4(ws, xb1);
      float mb, inv;
      ln_stats(x1v, mb, inv);
      *(LAS u32*)(uintptr_t)ha = modulate(ln_pair(SEL4(x1v), mb, inv), c1, sb);
    }
    BARRIER();
    const v4u av[2] = {*(const LAS v4u*)(uintptr_t)aa, *(const LAS v4u*)(uintptr_t)(aa + 64)};
    *(LAS float*)(uintptr_t)ra = gemv<2>(av, f, g);
    BARRIER();
    const float s = rbf(sum8(sa));
    const float gt = s, up = dpp<ROR8>(s);
    const float u2 = (-2.f * 0.7978845608028654f * LOG2E) * (gt + 0.044715f * gt * gt * gt);
    const float gv = rbf(gt * __builtin_amdgcn_rcpf(1.f + __builtin_amdgcn_exp2f(u2))) * up;
    const u32 d = pk(gv, dpp<SHL1>(gv));
    const v4u ag = {dppu<BC + 0>(d), dppu<BC + 2>(d), dppu<BC + 4>(d), dppu<BC + 6>(d)};
    stc_row(ws, yo, gemv<1>(&ag, f + 8, g), lane);
  }
}
}

extern "C" void kernel_launch(void* const* d_in, const int* in_sizes, int n_in, void* d_out, int out_size, void* d_ws, size_t ws_size, hipStream_t stream) {
  Params a;
  Ptrs& p = a.p;
  p.x = (const u16*)d_in[0]; p.K = (const u16*)d_in[1]; p.V = (const u16*)d_in[2]; p.wq = (const u16*)d_in[3]; p.wo = (const u16*)d_in[4];
  p.wg = (const u16*)d_in[6]; p.wu = (const u16*)d_in[7]; p.wd = (const u16*)d_in[8]; p.c = (const u16*)d_in[9]; p.wmod = (const u16*)d_in[10];
  p.out = (u16*)d_out; p.ws = d_ws;
  for (int i = 0; i < 32; ++i) a.tab[i] = (u32)toff_of(i) | ((u32)nhalf_of(i) << 16);
  hipLaunchKernelGGL(fused_kernel, dim3(NWG), dim3(NTHR), 0, stream, a);
}
```

```cpp
#include <hip/hip_runtime.h>
#include <stdint.h>

namespace {
typedef unsigned short u16;
typedef unsigned u32;
typedef unsigned long long u64;
typedef u32 v4u __attribute__((ext_vector_type(4)));
typedef u32 v2u __attribute__((ext_vector_type(2)));
typedef u32 v8u __attribute__((ext_vector_type(8)));
typedef u32 v16u __attribute__((ext_vector_type(16)));
typedef float f32x4 __attribute__((ext_vector_type(4)));
typedef float f32x2 __attribute__((ext_vector_type(2)));
typedef __bf16 bf16x2 __attribute__((ext_vector_type(2)));
typedef __bf16 bf16x8 __attribute__((ext_vector_type(8)));
typedef short s16x4 __attribute__((ext_vector_type(4)));
#define GAS __attribute__((address_space(1)))
#define LAS __attribute__((address_space(3)))

#define KV_SFX " nt"
constexpr int NT = 2;
constexpr int THR_W = 8;
constexpr int THR_Q = 6;
constexpr int H_W = 17, H_G = 18, H_M = 16, H_P = 18;

constexpr int NWG = 256, NTHR = 1024;
constexpr u32 SENT = 0xFFFFFFFFu;
constexpr int SC1 = 16;
constexpr u32 SPIN_CAP = 1u << 17;
constexpr float LOG2E = 1.4426950408889634f;
constexpr int EPOCH_OFF = 0;
constexpr int CF_OFF = 8192;
constexpr int Q_OFF = 16384;
constexpr int H0_OFF = 32768;
constexpr int S_OFF = 65536;
constexpr int VOTE_OFF = CF_OFF + 1024;
constexpr int T1_OFF = S_OFF;
constexpr int ML_OFF = T1_OFF + 524288;
constexpr int T2_OFF = ML_OFF + 65536;
constexpr int Y_OFF = T2_OFF + 262144;
constexpr int X1_OFF = Y_OFF + 114688;
constexpr int MR_OFF = X1_OFF + 16384;
__device__ __forceinline__ int t1_at(int xc, int pol, int off) { return T1_OFF + (xc & 3) * 131072 + (off >> 13) * 16384 + (((xc >> 2) ^ pol) << 13) + (off & 8191); }
__device__ __forceinline__ int ml_at(int xc, int pol) { return ML_OFF + (xc & 3) * 16384 + (((xc >> 2) ^ pol) << 13); }
__device__ __forceinline__ int row_at(int prod, int pol, int colbyte) { return (prod >> 3) * 16384 + (((colbyte >> 10) ^ pol) << 13) + (prod & 7) * 1024 + (colbyte & 1023); }
constexpr int CLR_KB = 16;
constexpr int S_END = S_OFF + 64 * CLR_KB * 1024;
static_assert(MR_OFF + 4096 <= S_END && S_OFF % 16384 == 0 && ML_OFF % 16384 == 0 && T2_OFF % 16384 == 0 && Y_OFF % 16384 == 0 && X1_OFF % 16384 == 0, "");
constexpr int PROBE_OFF = S_END, WS_BYTES = S_END + 16384;
constexpr int L_K = 0, L_TB = 8 * 16384, L_Q = 9 * 16384, L_FLAG = L_Q + 1280, L_ML = L_FLAG + 64, L_X = L_ML + 512, L_RED = L_X + 1024, L_END = L_RED + 4096;
static_assert(L_END <= 163840 && L_RED % 16 == 0 && L_TB % 16 == 0, "");
constexpr int CH_ALL = 512 - 8 * H_W - 7 * H_G - 4 * H_M - 5 * H_P, H_CH = CH_ALL / 8, CH_HI = CH_ALL - 8 * H_CH;
constexpr int nhalf_of(int ix) { return ix < 8 ? (ix < CH_HI ? H_CH + 1 : H_CH) : ix < 16 ? H_W : ix < 23 ? H_G : ix < 27 ? H_M : H_P; }
constexpr int toff_of(int ix) { int o = 0; for (int i = 0; i < ix; ++i) o += nhalf_of(i); return o; }
static_assert(toff_of(32) == 512 && H_G >= 8 && H_G <= 18 && H_CH >= 8 && H_CH + 1 <= 16 && H_M >= 8 && H_M <= 16 && H_W >= 8 && H_W <= 18 && H_P >= 8 && H_P <= 18, "");

struct Ptrs {
  const u16 *x, *K, *V, *wq, *wo, *wg, *wu, *wd, *c, *wmod;
  u16* out;
  void* ws;
};
struct Params { Ptrs p; u32 tab[32]; };
static_assert(sizeof(Params) == 96 + 128 && L_K == 0, "");

__device__ __forceinline__ float asf(u32 x) { return __builtin_bit_cast(float, x); }
__device__ __forceinline__ u32 asu(float x) { return __builtin_bit_cast(u32, x); }
__device__ __forceinline__ float blo(u32 w) { return asf(w << 16); }
__device__ __forceinline__ float bhi(u32 w) { return asf(w & 0xffff0000u); }
__device__ __forceinline__ u32 pk2(f32x2 v) { return __builtin_bit_cast(u32, __builtin_convertvector(v, bf16x2)); }
__device__ __forceinline__ u32 pk(float lo, float hi) { return pk2(f32x2{lo, hi}); }
__device__ __forceinline__ f32x2 up2(u32 w) { return f32x2{blo(w), bhi(w)}; }
__device__ __forceinline__ float rbf(float f) { return (float)(__bf16)f; }
__device__ __forceinline__ float dot2(u32 a, u32 b, float c) { return __builtin_amdgcn_fdot2_f32_bf16(__builtin_bit_cast(bf16x2, a), __builtin_bit_cast(bf16x2, b), c, false); }
template <int CTRL> __device__ __forceinline__ float dpp(float x) { return asf(__builtin_amdgcn_mov_dpp(asu(x), CTRL, 0xf, 0xf, true)); }
template <int CTRL> __device__ __forceinline__ u32 dppu(u32 x) { return __builtin_amdgcn_mov_dpp(x, CTRL, 0xf, 0xf, true); }
constexpr int XOR1 = 0xB1, XOR2 = 0x4E, XOR7 = 0x141, ROR8 = 0x128, ROR4 = 0x124, SHL1 = 0x101, SHL2 = 0x102, BC = 0x150;
__device__ __forceinline__ float mx(float a, float b) { return __builtin_amdgcn_fmed3f(a, b, __builtin_inff()); }
__device__ __forceinline__ float tr32(float a, float b) { auto s = __builtin_amdgcn_permlane32_swap(asu(a), asu(b), false, false); return asf(s[0]) + asf(s[1]); }
__device__ __forceinline__ float tr16(float a, float b) { auto s = __builtin_amdgcn_permlane16_swap(asu(a), asu(b), false, false); return asf(s[0]) + asf(s[1]); }
__device__ __forceinline__ float tm32(float a, float b) { auto s = __builtin_amdgcn_permlane32_swap(asu(a), asu(b), false, false); return mx(asf(s[0]), asf(s[1])); }
__device__ __forceinline__ float tm16(float a, float b) { auto s = __builtin_amdgcn_permlane16_swap(asu(a), asu(b), false, false); return mx(asf(s[0]), asf(s[1])); }
__device__ __forceinline__ float row_sum(float v) { v += dpp<ROR8>(v); v += dpp<XOR7>(v); v += dpp<XOR2>(v); v += dpp<XOR1>(v); return v; }
__device__ __forceinline__ float row_max(float v) { v = mx(v, dpp<ROR8>(v)); v = mx(v, dpp<XOR7>(v)); v = mx(v, dpp<XOR2>(v)); v = mx(v, dpp<XOR1>(v)); return v; }
__device__ __forceinline__ f32x2 sum8x64(v4u a, v4u b) {
  const float y0 = tr32(asf(a.x), asf(b.x)), y1 = tr32(asf(a.y), asf(b.y)), y2 = tr32(asf(a.z), asf(b.z)), y3 = tr32(asf(a.w), asf(b.w));
  return f32x2{row_sum(tr16(y0, y2)), row_sum(tr16(y1, y3))};
}
__device__ __forceinline__ u32 rdl(u32 v, int l) { return __builtin_amdgcn_readlane(v, l); }
__device__ __forceinline__ float rdlf(float v, int l) { return asf(__builtin_amdgcn_readlane(asu(v), l)); }
__device__ __forceinline__ u32 nosent(u32 w) { return w < 0xFFFFFFFEu ? w : 0xFFFFFFFEu; }
typedef __amdgpu_buffer_rsrc_t rsrc_t;
__device__ __forceinline__ rsrc_t mkr(const void* q, int bytes) { return __builtin_amdgcn_make_buffer_rsrc((void*)q, 0, bytes, 0x00020000); }
template <int AUX> __device__ __forceinline__ v4u ld4(rsrc_t r, int off, int so) { return __builtin_amdgcn_raw_buffer_load_b128(r, off, so, AUX); }
__device__ __forceinline__ u32 ld1(rsrc_t r, int off, int so) { return __builtin_amdgcn_raw_buffer_load_b32(r, off, so, 0); }
__device__ __forceinline__ v4u ldc(rsrc_t r, int off) { return __builtin_amdgcn_raw_buffer_load_b128(r, off, 0, SC1); }
__device__ __forceinline__ u32 ldc1(rsrc_t r, int off) { return __builtin_amdgcn_raw_buffer_load_b32(r, off, 0, SC1); }
__device__ __forceinline__ void stc(rsrc_t r, int off, v4u v) { __builtin_amdgcn_raw_buffer_store_b128(v, r, off, 0, SC1); }
__device__ __forceinline__ void stc2(rsrc_t r, int off, v2u v) { __builtin_amdgcn_raw_buffer_store_b64(v, r, off, 0, SC1); }
__device__ __forceinline__ void stc_row(rsrc_t r, int off, float v, int lane) {
  const v4u o = {asu(v), asu(dpp<0x55>(v)), asu(dpp<0xAA>(v)), asu(dpp<0xFF>(v))};
  if (!(lane & 3)) stc(r, off, o);
}
__device__ __forceinline__ void stc1(rsrc_t r, int off, u32 v) { __builtin_amdgcn_raw_buffer_store_b32(v, r, off, 0, SC1); }
#define MEMFENCE() asm volatile("" ::: "memory")
#define LGKM0() asm volatile("s_waitcnt lgkmcnt(0)" ::: "memory")
#define VM0() asm volatile("s_waitcnt vmcnt(0)" ::: "memory")
#define SCHED() __builtin_amdgcn_sched_barrier(0)
#define BARRIER() do { LGKM0(); SCHED(); __builtin_amdgcn_s_barrier(); MEMFENCE(); SCHED(); } while (0)
#define PIN1(a) asm volatile("" : "+v"(a))
#define PIN4(a) asm volatile("" : "+v"((a)[0]), "+v"((a)[1]), "+v"((a)[2]), "+v"((a)[3]))
#define PIN8(a) asm volatile("" : "+v"((a)[0]), "+v"((a)[1]), "+v"((a)[2]), "+v"((a)[3]), "+v"((a)[4]), "+v"((a)[5]), "+v"((a)[6]), "+v"((a)[7]))

#define POLL_HEAD "s_mov_b32 %[n], %[cap]\n\t1:\n\t"
#define POLL_TAIL "s_cbranch_execz 2f\n\ts_sub_u32 %[n], %[n], 1\n\ts_cbranch_scc0 1b\n\t2:\n\ts_mov_b64 exec, -1\n\ts_nop 2"
__device__ __forceinline__ v4u poll4(rsrc_t r, int off) {
  v4u a; u32 n;
  asm volatile(POLL_HEAD "buffer_load_dwordx4 v[124:127], %[o], %[r], 0 offen sc1\n\ts_waitcnt vmcnt(0)\n\t"
               "v_max_u32 v123, v124, v125\n\tv_max3_u32 v123, v123, v126, v127\n\tv_cmpx_eq_u32 vcc, -1, v123\n\t" POLL_TAIL
               : "=&{v[124:127]}"(a), [n] "=&s"(n) : [o] "v"(off), [r] "s"(r), [cap] "i"(SPIN_CAP) : "memory", "vcc", "scc", "v123");
  return a;
}
template <int D> __device__ __forceinline__ void poll4x2(rsrc_t r, int off, v4u& a, v4u& b) {
  u32 n;
  asm volatile(POLL_HEAD "buffer_load_dwordx4 v[120:123], %[o], %[r], 0 offen sc1\n\tbuffer_load_dwordx4 v[124:127], %[o], %[r], 0 offen offset:%[d] sc1\n\ts_waitcnt vmcnt(1)\n\t"
               "v_max_u32 v119, v120, v121\n\tv_max3_u32 v119, v119, v122, v123\n\ts_waitcnt vmcnt(0)\n\tv_max3_u32 v119, v119, v124, v125\n\tv_max3_u32 v119, v119, v126, v127\n\t"
               "v_cmpx_eq_u32 vcc, -1, v119\n\t" POLL_TAIL
               : "=&{v[120:123]}"(a), "=&{v[124:127]}"(b), [n] "=&s"(n) : [o] "v"(off), [r] "s"(r), [cap] "i"(SPIN_CAP), [d] "i"(D) : "memory", "vcc", "scc", "v119");
}
__device__ __forceinline__ void poll4x4(rsrc_t r, int off, int off2, v4u& a, v4u& b, v4u& c, v4u& d) {
  u32 n;
  asm volatile(POLL_HEAD "buffer_load_dwordx4 v[112:115], %[o], %[r], 0 offen sc1\n\tbuffer_load_dwordx4 v[116:119], %[o], %[r], 0 offen offset:16 sc1\n\t"
               "buffer_load_dwordx4 v[120:123], %[o2], %[r], 0 offen sc1\n\tbuffer_load_dwordx4 v[124:127], %[o2], %[r], 0 offen offset:16 sc1\n\t"
               "s_waitcnt vmcnt(3)\n\tv_max_u32 v110, v112, v113\n\tv_max3_u32 v110, v110, v114, v115\n\t"
               "s_waitcnt vmcnt(2)\n\tv_max_u32 v111, v116, v117\n\tv_max3_u32 v111, v111, v118, v119\n\t"
               "s_waitcnt vmcnt(1)\n\tv_max3_u32 v110, v110, v120, v121\n\tv_max3_u32 v111, v111, v122, v123\n\t"
               "s_waitcnt vmcnt(0)\n\tv_max3_u32 v110, v110, v124, v125\n\tv_max3_u32 v111, v111, v126, v127\n\t"
               "v_max_u32 v110, v110, v111\n\tv_cmpx_eq_u32 vcc, -1, v110\n\t" POLL_TAIL
               : "=&{v[112:115]}"(a), "=&{v[116:119]}"(b), "=&{v[120:123]}"(c), "=&{v[124:127]}"(d), [n] "=&s"(n)
               : [o] "v"(off), [o2] "v"(off2), [r] "s"(r), [cap] "i"(SPIN_CAP) : "memory", "vcc", "scc", "v110", "v111");
}
__device__ __forceinline__ v2u poll2(rsrc_t r, int off) {
  v2u a; u32 n;
  asm volatile(POLL_HEAD "buffer_load_dwordx2 v[126:127], %[o], %[r], 0 offen sc1\n\ts_waitcnt vmcnt(0)\n\t"
               "v_max_u32 v125, v126, v127\n\tv_cmpx_eq_u32 vcc, -1, v125\n\ts_cbranch_execz 2f\n\ts_sleep 1\n\ts_sub_u32 %[n], %[n], 1\n\ts_cbranch_scc0 1b\n\t2:\n\ts_mov_b64 exec, -1\n\ts_nop 2"
               : "=&{v[126:127]}"(a), [n] "=&s"(n) : [o] "v"(off), [r] "s"(r), [cap] "i"(SPIN_CAP) : "memory", "vcc", "scc", "v125");
  return a;
}
template <int D> __device__ __forceinline__ void poll1x2(rsrc_t r, int off, u32& a, u32& b) {
  u32 n;
  asm volatile(POLL_HEAD "buffer_load_dword v126, %[o], %[r], 0 offen sc1\n\tbuffer_load_dword v127, %[o], %[r], 0 offen offset:%[d] sc1\n\ts_waitcnt vmcnt(0)\n\t"
               "v_max_u32 v125, v126, v127\n\tv_cmpx_eq_u32 vcc, -1, v125\n\t" POLL_TAIL
               : "=&{v126}"(a), "=&{v127}"(b), [n] "=&s"(n) : [o] "v"(off), [r] "s"(r), [cap] "i"(SPIN_CAP), [d] "i"(D) : "memory", "vcc", "scc", "v125");
}
__device__ __forceinline__ void pollT4(rsrc_t r, int off, u32 tag, u32 ntag, v4u& a, v4u& b, v4u& c, v4u& d) {
  u32 n;
  asm volatile(POLL_HEAD "buffer_load_dwordx4 v[112:115], %[o], %[r], 0 offen sc1\n\tbuffer_load_dwordx4 v[116:119], %[o], %[r], 0 offen offset:512 sc1\n\t"
               "buffer_load_dwordx4 v[120:123], %[o], %[r], 0 offen offset:1024 sc1\n\tbuffer_load_dwordx4 v[124:127], %[o], %[r], 0 offen offset:1536 sc1\n\t"
               "s_waitcnt vmcnt(3)\n\tv_xor_b32 v110, v112, %[t]\n\tv_xor_b32 v111, v114, %[u]\n\t"
               "s_waitcnt vmcnt(2)\n\tv_bitop3_b32 v110, v116, %[t], v110 bitop3:0xbe\n\tv_bitop3_b32 v111, v118, %[u], v111 bitop3:0xbe\n\t"
               "s_waitcnt vmcnt(1)\n\tv_bitop3_b32 v110, v120, %[t], v110 bitop3:0xbe\n\tv_bitop3_b32 v111, v122, %[u], v111 bitop3:0xbe\n\t"
               "s_waitcnt vmcnt(0)\n\tv_bitop3_b32 v110, v124, %[t], v110 bitop3:0xbe\n\tv_bitop3_b32 v111, v126, %[u], v111 bitop3:0xbe\n\t"
               "v_or_b32 v110, v110, v111\n\tv_cmpx_ne_u32 vcc, 0, v110\n\t" POLL_TAIL
               : "=&{v[112:115]}"(a), "=&{v[116:119]}"(b), "=&{v[120:123]}"(c), "=&{v[124:127]}"(d), [n] "=&s"(n)
               : [o] "v"(off), [r] "s"(r), [cap] "i"(SPIN_CAP), [t] "v"(tag), [u] "v"(ntag) : "memory", "vcc", "scc", "v110", "v111");
}
__device__ __forceinline__ void lds_wait_ge(volatile LAS u32* f, u32 want) {
  _Pragma("nounroll") for (u32 s = 0; s < (SPIN_CAP << 3); ++s) { if (*f >= want) break; __builtin_amdgcn_s_sleep(1); }
  MEMFENCE();
}
__device__ __forceinline__ void lds_inc(volatile LAS u32* f, int lane) {
  asm volatile("s_mov_b64 exec, 1\n\tds_add_u32 %0, %1\n\ts_mov_b64 exec, -1" :: "v"((u32)(uintptr_t)f), "v"(1u) : "memory");
}
__device__ __forceinline__ u32 lds_ticket(volatile LAS u32* f, int lane) {
  u32 old;
  asm volatile("s_mov_b64 exec, 1\n\tds_add_rtn_u32 %0, %1, %2\n\ts_mov_b64 exec, -1\n\ts_waitcnt lgkmcnt(0)" : "=&v"(old) : "v"((u32)(uintptr_t)f), "v"(1u) : "memory");
  return __builtin_amdgcn_readfirstlane(old);
}
__device__ __forceinline__ float sum8(u32 addr) {
  const f32x4 t = *(const LAS f32x4*)(uintptr_t)addr + *(const LAS f32x4*)(uintptr_t)(addr + 16);
  return (t.x + t.z) + (t.y + t.w);
}
__device__ __forceinline__ float silu(float v) { return v / (1.f + __expf(-v)); }
__device__ __forceinline__ u32 silu2(u32 c) { return pk(silu(blo(c)), silu(bhi(c))); }
__device__ __forceinline__ void ln_stats(v4u xv, float& mb, float& inv) {
  constexpr u32 ONE2 = 0x3f803f80u;
  const float s = dot2(xv[0], ONE2, dot2(xv[1], ONE2, dot2(xv[2], ONE2, dot2(xv[3], ONE2, 0.f))));
  const float q = dot2(xv[0], xv[0], dot2(xv[1], xv[1], dot2(xv[2], xv[2], dot2(xv[3], xv[3], 0.f))));
  float t = row_sum(tr32(s, q));
  t = tr16(t, t);
  const float mean = rdlf(t, 0) * (1.f / 512.f), sq = rdlf(t, 32) * (1.f / 512.f);
  float var = fmaf(-mean, mean, sq);
  if (__builtin_expect(!(var * 64.f >= sq), 0)) {
    float d = 0.f;
#pragma unroll
    for (int i = 0; i < 4; ++i) { const float a = blo(xv[i]) - mean, b = bhi(xv[i]) - mean; d = fmaf(a, a, fmaf(b, b, d)); }
    d = row_sum(tr32(d, d));
    var = tr16(d, d) * (1.f / 512.f);
  }
  mb = rbf(mean);
  inv = __builtin_amdgcn_rcpf(rbf(__builtin_amdgcn_sqrtf(rbf(rbf(var) + 1e-5f))));
}
__device__ __forceinline__ u32 ln_pair(u32 x, float mb, float inv) { return pk2(up2(pk2(up2(x) - mb)) * inv); }
__device__ __forceinline__ f32x2 one_plus(u32 c) { return up2(pk2(up2(c) + 1.f)); }
__device__ __forceinline__ u32 modulate(u32 y, f32x2 c1, f32x2 sh) { return pk2(up2(pk2(up2(y) * c1)) + sh); }
__device__ __forceinline__ int tile_voff(int lane, int stride) { const int rho = lane >> 3; return (8 * (rho >> 2) + (rho & 3)) * stride + (lane & 7) * 16; }
template <int AUX> __device__ __forceinline__ void tile_load(rsrc_t r, int voff, int soff, int stride, v4u* w) {
#pragma unroll
  for (int i = 0; i < 4; ++i) w[i] = ld4<AUX>(r, voff, soff + (16 * (i & 1) + 4 * (i >> 1)) * stride);
}
__device__ __forceinline__ void tile_frags(LAS unsigned char* tb, int lane, const v4u* w, v4u* f) {
  const int rho = lane >> 3;
  LAS unsigned char* wr = tb + rho * 128 + (((lane & 7) ^ (rho & 6)) << 4);
  const int row = 4 * (lane >> 4) + ((lane >> 2) & 3);
  const LAS unsigned char* rd = tb + row * 128 + (lane & 3) * 8;
  const int sw = (row >> 1) & 3;
  v2u h[2][4];
#pragma unroll
  for (int t = 0; t < 2; ++t) {
    *(LAS v4u*)wr = w[2 * t]; *(LAS v4u*)(wr + 1024) = w[2 * t + 1];
    MEMFENCE();
#pragma unroll
    for (int ct = 0; ct < 4; ++ct) h[t][ct] = __builtin_bit_cast(v2u, __builtin_amdgcn_ds_read_tr16_b64_v4i16((LAS s16x4*)(rd + ((ct ^ sw) << 5))));
    MEMFENCE();
  }
#pragma unroll
  for (int ct = 0; ct < 4; ++ct) f[ct] = v4u{h[0][ct].x, h[0][ct].y, h[1][ct].x, h[1][ct].y};
  PIN4(f);
}
__device__ __forceinline__ void lds_frags(const LAS unsigned char* tile, int lane, v4u* f) {
  const int row = 4 * (lane >> 4) + ((lane >> 2) & 3);
  const LAS unsigned char* rd = tile + row * 128 + (lane & 3) * 8;
  const int sw = (row >> 1) & 3;
#pragma unroll
  for (int ct = 0; ct < 4; ++ct) {
    const v2u lo = __builtin_bit_cast(v2u, __builtin_amdgcn_ds_read_tr16_b64_v4i16((LAS s16x4*)(rd + ((ct ^ sw) << 5))));
    const v2u hi = __builtin_bit_cast(v2u, __builtin_amdgcn_ds_read_tr16_b64_v4i16((LAS s16x4*)(rd + 2048 + ((ct ^ sw) << 5))));
    f[ct] = v4u{lo.x, lo.y, hi.x, hi.y};
  }
  PIN4(f);
}
__device__ __forceinline__ f32x4 mfma32(v4u a, v4u b, f32x4 c) { return __builtin_amdgcn_mfma_f32_16x16x32_bf16(__builtin_bit_cast(bf16x8, a), __builtin_bit_cast(bf16x8, b), c, 0, 0, 0); }
template <int NS> __device__ __forceinline__ float gemv(const v4u* a, const v4u* f, int g) {
  f32x4 acc[4];
#pragma unroll
  for (int ct = 0; ct < 4; ++ct) acc[ct] = mfma32(a[0], f[ct], f32x4{0.f, 0.f, 0.f, 0.f});
#pragma unroll
  for (int s = 1; s < NS; ++s)
#pragma unroll
    for (int ct = 0; ct < 4; ++ct) acc[ct] = mfma32(a[s], f[4 * s + ct], acc[ct]);
  PIN4(acc);
  const float lo = (g & 1) ? acc[1][0] : acc[0][0], hi = (g & 1) ? acc[3][0] : acc[2][0];
  return (g & 2) ? hi : lo;
}
template <int B> __device__ __forceinline__ v4u bcast4(u32 v) { return v4u{dppu<BC + B>(v), dppu<BC + B + 1>(v), dppu<BC + B + 2>(v), dppu<BC + B + 3>(v)}; }

__global__ void __launch_bounds__(NTHR, 4) fused_kernel(Params) {
  __shared__ __attribute__((aligned(128))) unsigned char lds_raw[L_END];
  LAS unsigned char* L = (LAS unsigned char*)lds_raw;
  v16u ka; v8u kb8;
  int wave; u32 rtt, tab, ko0, ko1, ko2, ko3, ep, d1; v4u xs;
  {
    u32 sa, sb, sc, keep, va, vb; u64 pa;
#define DMA4(P) "global_load_lds_dwordx4 %[k0], " P KV_SFX "\n\tglobal_load_lds_dwordx4 %[k1], " P " offset:1024" KV_SFX "\n\t" \
                "global_load_lds_dwordx4 %[k2], " P " offset:2048" KV_SFX "\n\tglobal_load_lds_dwordx4 %[k3], " P " offset:3072" KV_SFX "\n\t"
    asm volatile("s_load_dwordx16 s[16:31], %[kp], 0x0\n\ts_load_dwordx8 s[36:43], %[kp], 0x40\n\t"
                 "s_lshr_b32 s46, %[bid], 3\n\ts_lshl_b32 s46, s46, 2\n\ts_add_u32 s46, s46, 0x60\n\ts_load_dword s46, %[kp], s46"
                 : "=&{s[16:31]}"(ka), "=&{s[36:43]}"(kb8), "=&{s46}"(tab) : [kp] "s"(__builtin_amdgcn_kernarg_segment_ptr()), [bid] "s"(blockIdx.x) : "scc");
    asm volatile(
      "s_lshr_b32 %[sa], %[bid], 3\n\t"
      "v_readfirstlane_b32 %[w], %[tid]\n\ts_mov_b32 %[keep], m0\n\ts_and_b32 %[sc], %[bid], 7\n\t"
      "s_lshr_b32 %[w], %[w], 6\n\ts_cmp_lt_u32 %[w], 8\n\ts_cbranch_scc1 1f\n\t"
      "v_bfe_u32 %[va], %[tid], 4, 2\n\tv_and_b32 %[vb], 15, %[tid]\n\tv_xor_b32 %[vb], %[vb], %[va]\n\tv_lshlrev_b32 %[vb], 4, %[vb]\n\t"
      "v_lshl_or_b32 %[k0], %[va], 8, %[vb]\n\tv_xor_b32 %[k1], 64, %[k0]\n\tv_xor_b32 %[k2], 0x80, %[k0]\n\tv_xor_b32 %[k3], 0xc0, %[k0]\n\t"
      "s_sub_u32 %[sb], %[w], 8\n\ts_lshl_b32 m0, %[sb], 14\n\ts_lshl_b32 %[sc], %[sc], 8\n\ts_add_u32 %[sb], %[sb], %[sc]\n\ts_lshl_b32 %[sb], %[sb], 1\n\t"
      "s_cmp_gt_u32 %[w], 11\n\ts_cselect_b32 %[sa], 0, %[sa]\n\ts_cmp_lt_u32 %[sa], 8\n\t"
      "s_waitcnt lgkmcnt(0)\n\ts_cbranch_scc1 9f\n\t"
      "s_and_b32 %[sc], s46, 0xffff\n\ts_add_u32 %[sb], %[sb], %[sc]\n\ts_lshl_b32 %[sb], %[sb], 13\n\t"
      "s_add_u32 s44, s18, %[sb]\n\ts_addc_u32 s45, s19, 0\n\t" DMA4("s[44:45]")
      "s_branch 9f\n\t"
      "1:\n\tv_bfe_u32 %[va], %[tid], 3, 3\n\tv_and_b32 %[vb], 6, %[va]\n\tv_xor_b32 %[vb], %[vb], %[tid]\n\tv_and_b32 %[vb], 7, %[vb]\n\tv_lshlrev_b32 %[vb], 4, %[vb]\n\t"
      "v_and_b32 %[k1], 4, %[va]\n\tv_add_u32 %[va], %[va], %[k1]\n\t"
      "v_and_b32 v106, 63, %[tid]\n\tv_lshlrev_b32 v106, 4, v106\n\tv_mov_b32 v104, 0\n\t"
      "v_and_b32 v105, 3, %[tid]\n\tv_lshlrev_b32 v105, 2, v105\n\tv_and_or_b32 v105, %[tid], 48, v105\n\t"
      "s_cmp_lt_u32 %[sa], 8\n\ts_cbranch_scc0 2f\n\t"
      "v_lshl_or_b32 %[k0], %[va], 13, %[vb]\n\tv_add_u32 %[k1], 0x1fc00, %[k0]\n\tv_add_u32 %[k2], 0x7800, %[k0]\n\tv_add_u32 %[k3], 0x27400, %[k0]\n\t"
      "v_bfe_u32 %[va], %[tid], 2, 1\n\tv_lshl_or_b32 v105, %[va], 6, v105\n\ts_lshl_b32 %[sb], %[w], 7\n\tv_add_u32 v105, %[sb], v105\n\t"
      "s_lshl_b32 m0, %[w], 13\n\ts_lshl_b32 %[sb], %[w], 9\n\ts_add_u32 %[sb], %[sb], %[sc]\n\ts_lshl_b32 %[sb], %[sb], 3\n\ts_add_u32 %[sb], %[sb], %[sa]\n\ts_lshl_b32 %[sb], %[sb], 7\n\t"
      "s_waitcnt lgkmcnt(0)\n\t"
      "global_load_dword v105, v105, s[16:17]\n\tglobal_load_dwordx4 v[106:109], v106, s[16:17]\n\tglobal_load_dword v104, v104, s[42:43] sc1\n\t"
      "s_add_u32 s44, s22, %[sb]\n\ts_addc_u32 s45, s23, 0\n\t" DMA4("s[44:45]")
      "s_add_u32 s44, s44, 0x40000\n\ts_addc_u32 s45, s45, 0\n\ts_add_u32 m0, m0, 0x1000\n\ts_nop 0\n\t" DMA4("s[44:45]")
      "s_branch 9f\n\t"
      "2:\n\tv_mul_u32_u24 %[k0], 0x1800, %[va]\n\tv_add_u32 %[k0], %[k0], %[vb]\n\tv_add_u32 %[k1], 0x17c00, %[k0]\n\tv_add_u32 %[k2], 0x5800, %[k0]\n\tv_add_u32 %[k3], 0x1d400, %[k0]\n\t"
      "s_lshr_b32 %[sb], %[sa], 3\n\ts_and_b32 s44, %[w], 5\n\ts_lshl_b32 %[sb], %[sb], 3\n\ts_or_b32 %[sb], %[sb], s44\n\ts_cmp_eq_u32 %[sb], 17\n\ts_cbranch_scc0 5f\n\t"
      "s_lshl_b32 %[sb], %[w], 12\n\ts_and_b32 %[sb], %[sb], 0x2000\n\ts_and_b32 s44, %[bid], 63\n\ts_lshl_b32 s44, s44, 7\n\ts_add_u32 %[sb], %[sb], s44\n\ts_add_u32 %[sb], %[sb], %[pr]\n\t"
      "v_mov_b32 v107, %[sb]\n\ts_mov_b64 exec, 1\n\ts_waitcnt lgkmcnt(0)\n\ts_memtime s[44:45]\n\ts_waitcnt lgkmcnt(0)\n\t"
      "global_store_dword v107, v104, s[42:43] sc1\n\ts_waitcnt vmcnt(0)\n\ts_memtime s[50:51]\n\ts_waitcnt lgkmcnt(0)\n\ts_sub_u32 %[rt], s50, s44\n\ts_mov_b64 exec, -1\n\t"
      "5:\n\ts_sub_u32 %[sb], %[sa], 23\n\ts_cmp_lt_u32 %[sb], 4\n\ts_cbranch_scc0 3f\n\t"
      "v_bfe_u32 %[va], %[tid], 2, 1\n\tv_lshl_or_b32 v105, %[va], 6, v105\n\ts_lshl_b32 s44, %[w], 7\n\tv_add_u32 v105, s44, v105\n\t"
      "s_lshl_b32 s44, %[w], 13\n\ts_add_u32 m0, s44, 0x10000\n\ts_lshl_b32 %[sc], %[sc], 2\n\ts_add_u32 %[sb], %[sb], %[sc]\n\ts_add_u32 %[sb], %[sb], 16\n\ts_lshl_b32 %[sb], %[sb], 7\n\t"
      "s_mul_i32 %[sc], %[w], 0x60000\n\ts_add_u32 %[sb], %[sb], %[sc]\n\t"
      "s_waitcnt lgkmcnt(0)\n\t"
      "global_load_dword v105, v105, s[36:37]\n\tglobal_load_dword v104, v104, s[42:43] sc1\n\t"
      "s_add_u32 s44, s38, %[sb]\n\ts_addc_u32 s45, s39, 0\n\t" DMA4("s[44:45]")
      "s_add_u32 s44, s44, 0x30000\n\ts_addc_u32 s45, s45, 0\n\ts_add_u32 m0, m0, 0x1000\n\ts_nop 0\n\t" DMA4("s[44:45]")
      "s_branch 9f\n\t3:\n\t"
      "s_and_b32 %[sb], %[sa], 3\n\ts_lshl_b32 %[sb], %[sb], 2\n\ts_and_b32 s44, %[w], 3\n\ts_add_u32 %[sb], %[sb], s44\n\t"
      "s_lshl_b32 s44, s44, 12\n\ts_add_u32 m0, s44, %[tb]\n\ts_lshl_b32 s44, %[sb], 6\n\tv_add_u32 v105, s44, v105\n\t"
      "s_mul_i32 %[sb], %[sb], 0x30000\n\ts_lshl_b32 %[sc], %[sc], 8\n\ts_add_u32 %[sb], %[sb], %[sc]\n\ts_bfe_u32 %[sc], %[sa], 0x10002\n\ts_lshl_b32 %[sc], %[sc], 7\n\ts_add_u32 %[sb], %[sb], %[sc]\n\t"
      "s_lshr_b32 %[sc], %[w], 2\n\ts_lshr_b32 %[sa], %[sa], 3\n\ts_cmp_eq_u32 %[sa], 1\n\ts_cselect_b32 %[sa], %[sc], 0\n\ts_cmp_eq_u32 %[sa], 1\n\t"
      "s_waitcnt lgkmcnt(0)\n\t"
      "global_load_dword v105, v105, s[36:37]\n\tglobal_load_dword v104, v104, s[42:43] sc1\n\ts_cbranch_scc0 9f\n\t"
      "s_add_u32 s44, s38, %[sb]\n\ts_addc_u32 s45, s39, 0\n\t" DMA4("s[44:45]")
      "9:\n\ts_mov_b32 m0, %[keep]"
      : "=&{s[44:45]}"(pa), [w] "=&s"(wave), [k0] "=&v"(ko0), [k1] "=&v"(ko1), [k2] "=&v"(ko2), [k3] "=&v"(ko3),
        "=&{v104}"(ep), "=&{v105}"(d1), "=&{v[106:109]}"(xs), [sa] "=&s"(sa), [sb] "=&s"(sb), [sc] "=&s"(sc), [keep] "=&s"(keep), [va] "=&v"(va), [vb] "=&v"(vb), [rt] "=&s"(rtt)
      : "{s[16:31]}"(ka), "{s[36:43]}"(kb8), "{s46}"(tab), [bid] "s"(blockIdx.x), [tid] "v"(threadIdx.x), [tb] "i"(L_TB), [pr] "i"(PROBE_OFF)
      : "memory", "scc", "s50", "s51");
    asm volatile("" : "+{s[16:31]}"(ka), "+{s[36:43]}"(kb8), "+{s46}"(tab));
  }
  volatile LAS u32* FL = (volatile LAS u32*)(L + L_FLAG);
  LAS float* RED = (LAS float*)(L + L_RED);
  const int lane = threadIdx.x & 63;
  const int bid = blockIdx.x, xc = bid & 7, ix = bid >> 3, rb = ix & 7;
  const int g = lane >> 4, n = lane & 15;
  const bool chain = ix < 8, wcu = (ix >> 3) == 1;
  const int toff = tab & 0xffff, nhalf = tab >> 16, ntask = (nhalf + 1) >> 1;
  const bool ninth = wave == 2 && ntask == 9;
  const int aw = ninth ? 8 : wave - 8;
  const bool head = (wave >> 2) == 2 && !chain, p1 = wcu && (wave >> 2) == 1;
  const u32 ldst = (u32)(uintptr_t)L + L_K + aw * 16384;
#define KGROUP(kb, lo) do { u32 keep_; \
    asm volatile("s_mov_b32 %0, m0\n\ts_mov_b32 m0, %6\n\ts_nop 0\n\t" \
                 "global_load_lds_dwordx4 %1, %5" KV_SFX "\n\t" \
                 "global_load_lds_dwordx4 %2, %5 offset:1024" KV_SFX "\n\t" \
                 "global_load_lds_dwordx4 %3, %5 offset:2048" KV_SFX "\n\t" \
                 "global_load_lds_dwordx4 %4, %5 offset:3072" KV_SFX "\n\t" \
                 "s_mov_b32 m0, %0" \
                 : "=&s"(keep_) : "v"(ko0), "v"(ko1), "v"(ko2), "v"(ko3), "s"(kb), "s"(ldst + (lo)) : "memory"); } while (0)
#define PTR(v, i) (((u64)(v)[2 * (i) + 1] << 32) | (v)[2 * (i)])
  Ptrs p;
  p.x = (const u16*)PTR(ka, 0); p.K = (const u16*)PTR(ka, 1); p.V = (const u16*)PTR(ka, 2); p.wq = (const u16*)PTR(ka, 3); p.wo = (const u16*)PTR(ka, 4);
  p.wg = (const u16*)PTR(ka, 5); p.wu = (const u16*)PTR(ka, 6); p.wd = (const u16*)PTR(ka, 7);
  p.c = (const u16*)PTR(kb8, 0); p.wmod = (const u16*)PTR(kb8, 1); p.out = (u16*)PTR(kb8, 2); p.ws = (void*)PTR(kb8, 3);
#define MKWS() const rsrc_t ws = mkr(p.ws, WS_BYTES)
  if (wave == 0 && lane < 16) FL[lane] = (lane == 0 && ix >= 16) ? 1u : 0u;
  BARRIER();

  MKWS();
  const bool gcu = ix >= 16 && ix < 23, mcu = ix >= 23 && ix < 27;
  u32 tag = 0;
  if (__builtin_expect(wave < 8 && !ninth, 1)) {
  if (wave >= 4 && !(chain || wcu || gcu || mcu)) return;
  __builtin_amdgcn_s_setprio(3);
  asm volatile("s_waitcnt vmcnt(8)\n\ts_cmp_eq_u32 %3, 2\n\ts_cbranch_scc1 1f\n\ts_waitcnt vmcnt(4)\n\ts_cmp_eq_u32 %3, 1\n\ts_cbranch_scc1 1f\n\ts_waitcnt vmcnt(0)\n\t1:"
               : "+{v104}"(ep), "+{v105}"(d1), "+{v[106:109]}"(xs) : "s"(__builtin_amdgcn_readfirstlane((chain || mcu) ? 2 : p1 ? 1 : 0)) : "memory", "scc");
  const int cg = xc * 2 + (rb >> 2), ks = rb & 3;
  const int pi = ((n >> 2) & 1) * 16 + g * 4 + (n & 3);
  tag = ep + 1u;
  if (__builtin_expect(p1, 1)) {
    v4u a = bcast4<0>(silu2(d1)), f[4];
    int ho = H0_OFF + (cg * 4 + ks) * 512 + lane * 8;
    PIN1(a); PIN1(ho);
    VM0();
    lds_frags(L + L_TB + (wave & 3) * 4096, lane, f);
    RED[(wave & 3) * 64 + lane] = gemv<1>(&a, f, g);
    if (lds_ticket(&FL[3], lane) == 3u) {
      const float s = (RED[lane] + RED[64 + lane]) + (RED[128 + lane] + RED[192 + lane]);
      const float s_hi = dpp<SHL1>(s);
      if (!(lane & 1)) stc(ws, ho, v4u{tag, asu(s), ~tag, asu(s_hi)});
      MEMFENCE();
      if (lane == 0) FL[0] = 1u;
    }
  }
  if (__builtin_expect(chain, 1)) {
    float mb, inv;
    ln_stats(xs, mb, inv);
    f32x2 y = up2(ln_pair(d1, mb, inv));
    PIN1(y);
    VM0();
    v4u f[8];
    lds_frags(L + L_K + wave * 8192, lane, f); lds_frags(L + L_K + wave * 8192 + 4096, lane, f + 4);
    SCHED();
    v4u h0, h1, h2, h3;
    pollT4(ws, H0_OFF + (wave + (n >> 3) * 8) * 2048 + pi * 16, tag, ~tag, h0, h1, h2, h3);
    const u32 md = pk((asf(h0.y) + asf(h1.y)) + (asf(h2.y) + asf(h3.y)), (asf(h0.w) + asf(h1.w)) + (asf(h2.w) + asf(h3.w)));
    const u32 ot = dppu<ROR8>(md), sa = (n & 8) ? ot : md, ca = (n & 8) ? md : ot;
    const u32 hp = pk2(up2(pk2(y * one_plus(ca))) + up2(sa));
    const v4u a[2] = {bcast4<0>(hp), bcast4<4>(hp)};
    RED[lane * 8 + wave] = gemv<2>(a, f, g);
    if (lds_ticket(&FL[7], lane) == 7u) {
      const float s = sum8((u32)(uintptr_t)(RED + lane * 8));
      const u32 d = pk(s, dpp<SHL1>(s)), d2 = dppu<SHL2>(d);
      if ((lane & 3) == 0) stc(ws, Q_OFF + (xc * 8 + rb) * 256 + lane * 4, v4u{tag, d, ~tag, d2});
      MEMFENCE();
      if (lane == 0) FL[0] = 1u;
    }
  }
  if ((ix >> 3) == 2 && (wave == 1 || wave == 3)) {
    if (wave == 3) { if (lane == 0) FL[12] = rtt | 1u; }
    else {
      lds_wait_ge(&FL[12], 1u);
      const u32 vote = (rtt > FL[12] ? 1u : 0u) ^ (u32)(xc >> 2);
      if (lane == 0) stc2(ws, VOTE_OFF + (xc * 8 + rb) * 16, v2u{tag, ~tag ^ vote});
    }
  }
  if (mcu) {
    const u32 sp = silu2(d1);
    const v4u a[2] = {bcast4<0>(sp), bcast4<4>(sp)};
    VM0();
    v4u f[8];
    lds_frags(L + 65536 + wave * 8192, lane, f); lds_frags(L + 65536 + wave * 8192 + 4096, lane, f + 4);
    RED[lane * 8 + wave] = gemv<2>(a, f, g);
    MEMFENCE();
    lds_inc(&FL[3], lane);
    if (wave == 1) {
      lds_wait_ge(&FL[3], 8u);
      const float s = sum8((u32)(uintptr_t)(RED + lane * 8));
      const u32 d = nosent(pk(s, dpp<SHL1>(s)));
      lds_wait_ge(&FL[5], 1u);
      if (!(lane & 1)) stc1(ws, MR_OFF + (xc * 4 + (ix - 23)) * 128 + lane * 2, d);
    }
  }
  if (wcu && wave == 1) {
    lds_wait_ge(&FL[0], 1u);
    const v4u sv = {SENT, SENT, SENT, SENT};
#pragma unroll
    for (int j = 0; j < CLR_KB; ++j) stc(ws, S_OFF + (xc * 8 + rb) * (CLR_KB * 1024) + j * 1024 + lane * 16, sv);
    VM0();
    if (lane == 0) stc2(ws, CF_OFF + (xc * 8 + rb) * 16, v2u{tag, ~tag});
  }
  if (wave == 0) {
    const int qb = Q_OFF + xc * 2048 + lane * 16;
    if (!chain && !mcu) __builtin_amdgcn_s_sleep(100);
    _Pragma("nounroll") for (u32 s = 0; s < SPIN_CAP; ++s) {
      const v4u q0 = ldc(ws, qb), q1 = ldc(ws, qb + 1024);
      SCHED();
      if (__all((((q0.x ^ tag) | (q1.x ^ tag)) | ((q0.z ^ ~tag) | (q1.z ^ ~tag))) == 0u)) {
        LAS unsigned char* qd = L + L_Q + (lane >> 5) * 320 + (lane & 31) * 8;
        *(LAS v2u*)qd = v2u{q0.y, q0.w};
        *(LAS v2u*)(qd + 640) = v2u{q1.y, q1.w};
        LGKM0();
        if (lane == 0) FL[1] = 1u;
        break;
      }
      MEMFENCE();
    }
  }
  if (wave == 3) {
    {
      u32 pv = 0;
      __builtin_amdgcn_s_sleep(100);
      _Pragma("nounroll") for (u32 s = 0; s < SPIN_CAP; ++s) {
        const v2u cf = __builtin_amdgcn_raw_buffer_load_b64(ws, CF_OFF + lane * 16, 0, SC1), vt = __builtin_amdgcn_raw_buffer_load_b64(ws, VOTE_OFF + lane * 16, 0, SC1);
        SCHED();
        pv = vt.y ^ ~tag;
        if (__all(((cf.x ^ tag) | (cf.y ^ ~tag) | (vt.x ^ tag) | (pv & ~1u)) == 0u)) break;
        __builtin_amdgcn_s_sleep(16);
        MEMFENCE();
      }
      const u32 yes = __builtin_popcountll(__ballot(pv & 1u));
      if (lane == 0) FL[5] = yes > 32u ? 3u : 1u;
    }
  }
  }
  else {
    const bool early = aw < 4;
    if (ninth) {
      VM0();
      const u32 kv0 = g * 256 + ((n ^ g) << 4);
      ko0 = kv0; ko1 = kv0 ^ 64u; ko2 = kv0 ^ 128u; ko3 = kv0 ^ 192u;
    }
    if (aw >= ntask) return;
    if (chain) lds_wait_ge(&FL[0], 1u);
    if (!early) {
      lds_wait_ge(&FL[3], (ix >= 23 && ix < 27) ? 8u : (wcu && ninth) ? 4u : 0u);
      _Pragma("nounroll") for (u32 s = 0; s < (SPIN_CAP << 3); ++s) { if (FL[2] >= 4u || FL[1] != 0u) break; __builtin_amdgcn_s_sleep(1); }
      MEMFENCE();
    }
    const int hh = n >> 2, e2 = n & 3;
    u32 qseen = (!early && ix >= 16) ? 1u : 0u;
    const u32 key0 = (u32)(xc * 16384 + toff * 32 + aw * 64);
    const bool halft = 2 * aw + 1 == nhalf;
#define THROTTLE() do { if (!qseen) { const v2u fl_ = *(volatile LAS v2u*)FL; qseen = __builtin_amdgcn_readfirstlane(fl_.y); \
      if (!qseen) { if (__builtin_amdgcn_readfirstlane(fl_.x)) asm volatile("s_waitcnt vmcnt(%0)" :: "i"(THR_W - 4) : "memory"); else asm volatile("s_waitcnt vmcnt(%0)" :: "i"(THR_Q - 4) : "memory"); } } SCHED(); } while (0)
    const u64 kb = (u64)p.K + (u64)key0 * 256;
    if (!head) KGROUP(kb, 0);
    THROTTLE(); KGROUP(kb + 4096, 4096);
    THROTTLE();
    if (!halft) { KGROUP(kb + 8192, 8192); THROTTLE(); KGROUP(kb + 12288, 12288); THROTTLE(); }
    v4u vv[16];
    {
      const rsrc_t rv = mkr(p.V, 8 * 16384 * 256);
      const int vo = (halft && g >= 2) ? 0x40000000 : g * 4096 + n * 16;
#pragma unroll
      for (int i = 0; i < 16; ++i) { vv[i] = ld4<NT>(rv, vo + i * 256, key0 * 256); if ((i & 3) == 3 && i < 15) THROTTLE(); }
    }
    SCHED();
    lds_inc(&FL[2], lane);
    lds_wait_ge(&FL[1], 1u);
    asm volatile("s_waitcnt vmcnt(16)" ::: "memory");
    SCHED();
    float s2[4], pe[4];
    {
      const LAS unsigned char* kbase = L + L_K + aw * 16384 + n * 256;
      const LAS unsigned char* qa = L + L_Q + (n & 3) * 320 + g * 16;
      f32x4 sa[4];
#pragma unroll
      for (int t = 0; t < 4; ++t) sa[t] = f32x4{0.f, 0.f, 0.f, 0.f};
#pragma unroll
      for (int s = 0; s < 4; ++s) {
        const v4u a = *(const LAS v4u*)(qa + 64 * s);
#pragma unroll
        for (int t = 0; t < 4; ++t) sa[t] = mfma32(a, *(const LAS v4u*)(kbase + t * 4096 + (((4 * s + g) ^ n) << 4)), sa[t]);
      }
      const f32x4 lo = (g & 1) ? sa[1] : sa[0], hi = (g & 1) ? sa[3] : sa[2], sacc = (g & 2) ? hi : lo;
      constexpr float SC = 0.08838834764831845f;
      const f32x2 c = up2(pk2(up2(pk(sacc[0], sacc[1])) * SC)), d = up2(pk2(up2(pk(sacc[2], sacc[3])) * SC));
      const bool dead = halft && g >= 2;
      s2[0] = dead ? -__builtin_inff() : c.x; s2[1] = dead ? -__builtin_inff() : c.y; s2[2] = dead ? -__builtin_inff() : d.x; s2[3] = dead ? -__builtin_inff() : d.y;
    }
    const float mrow = row_max(tm16(tm32(s2[0], s2[2]), tm32(s2[1], s2[3])));
#pragma unroll
    for (int h2 = 0; h2 < 4; ++h2) pe[h2] = __builtin_amdgcn_exp2f((s2[h2] - rdlf(mrow, 16 * h2)) * LOG2E);
    const u32 w0 = pk(pe[0], pe[1]), w1 = pk(pe[2], pe[3]);
    const float lrow = row_sum(tr16(tr32(pe[0], pe[2]), tr32(pe[1], pe[3])));
    if (n == 0) *(LAS f32x2*)(L + L_ML + g * 128 + aw * 8) = f32x2{mrow, lrow};
    MEMFENCE();
    lds_inc(&FL[4], lane);
    const u32 two = (u32)(2 * hh) | ((u32)(2 * hh + 1) << 8);
    const u32 selv = (e2 & 1) ? ((two << 16) | 0x0C0Cu) : (0x0C0C0000u | two);
    const u32 sel0 = (e2 >> 1) == 0 ? selv : 0x0C0C0C0Cu, sel1 = (e2 >> 1) == 1 ? selv : 0x0C0C0C0Cu;
    v2u xb[16];
#define PV_PREP(i) { const u32 b0_ = dppu<BC + (i)>(w0), b1_ = dppu<BC + (i)>(w1); xb[i] = v2u{__builtin_amdgcn_perm(b1_, b0_, sel0), __builtin_amdgcn_perm(b1_, b0_, sel1)}; }
    PV_PREP(0) PV_PREP(1) PV_PREP(2) PV_PREP(3) PV_PREP(4) PV_PREP(5) PV_PREP(6) PV_PREP(7)
    PV_PREP(8) PV_PREP(9) PV_PREP(10) PV_PREP(11) PV_PREP(12) PV_PREP(13) PV_PREP(14) PV_PREP(15)
#undef PV_PREP
    u32 obw = (u32)(uintptr_t)L + L_K + aw * 16384 + (hh * 128 + e2 * 16 + 4 * g) * 4;
    PIN1(obw);
    SCHED();
    f32x4 o1 = {0.f, 0.f, 0.f, 0.f}, o2 = {0.f, 0.f, 0.f, 0.f};
#pragma unroll
    for (int i = 0; i < 16; ++i) {
      o1 = __builtin_amdgcn_mfma_f32_16x16x16bf16_1k(__builtin_bit_cast(s16x4, v2u{vv[i].x, vv[i].y}), __builtin_bit_cast(s16x4, xb[i]), o1, 0, 0, 0);
      o2 = __builtin_amdgcn_mfma_f32_16x16x16bf16_1k(__builtin_bit_cast(s16x4, v2u{vv[i].z, vv[i].w}), __builtin_bit_cast(s16x4, xb[i]), o2, 0, 0, 0);
    }
    *(LAS f32x4*)(uintptr_t)obw = o1; *(LAS f32x4*)(uintptr_t)(obw + 256) = o2;
    if (aw < 2 || (aw > 3 && aw < 8) || (ninth && !gcu)) { BARRIER(); return; }
    if (!ninth) {
    const int tau = (aw - 2) * 64 + lane, h = tau >> 5;
    float f[9];
    lds_wait_ge(&FL[5], 1u);
    const int pol = __builtin_amdgcn_readfirstlane(FL[5]) >> 1;
    lds_wait_ge(&FL[4], (u32)ntask);
    {
      float m[10], l[10];
#pragma unroll
      for (int w = 0; w < 5; ++w) { const f32x4 t = *(const LAS f32x4*)(L + L_ML + h * 128 + w * 16); m[2 * w] = t.x; l[2 * w] = t.y; m[2 * w + 1] = t.z; l[2 * w + 1] = t.w; }
#pragma unroll
      for (int w = 4; w < 9; ++w) { m[w] = w < ntask ? m[w] : -__builtin_inff(); l[w] = w < ntask ? l[w] : 0.f; }
      const float M = mx(mx(mx(mx(m[0], m[1]), mx(m[2], m[3])), mx(mx(m[4], m[5]), mx(m[6], m[7]))), m[8]);
      float Ls = 0.f;
#pragma unroll
      for (int w = 0; w < 9; ++w) { f[w] = __builtin_amdgcn_exp2f((m[w] - M) * LOG2E); Ls = fmaf(l[w], f[w], Ls); PIN1(f[w]); }
      if ((lane & 31) == 0) stc2(ws, ml_at(xc, pol) + (ix * 2 + aw - 2) * 128 + (lane >> 5) * 8, v2u{asu(M), asu(Ls)});
    }
    int t1o = t1_at(xc, pol, ((h * 4 + ((tau >> 3) & 3)) * 32 + ix) * 128 + (tau & 7) * 16);
    u32 pa[9];
#pragma unroll
    for (int w = 0; w < 9; ++w) { pa[w] = (u32)(uintptr_t)(L + L_K) + tau * 16 + (w < ntask ? w : 0) * 16384; PIN1(pa[w]); }
    PIN1(t1o);
    __builtin_amdgcn_s_setprio(3);
    BARRIER();
    f32x4 part[9];
#pragma unroll
    for (int w = 0; w < 9; ++w) part[w] = *(const LAS f32x4*)(uintptr_t)pa[w];
    const f32x4 acc = (((part[0] * f[0] + part[1] * f[1]) + (part[2] * f[2] + part[3] * f[3])) + ((part[4] * f[4] + part[5] * f[5]) + (part[6] * f[6] + part[7] * f[7]))) + part[8] * f[8];
    stc(ws, t1o, __builtin_bit_cast(v4u, acc));
    lds_inc(&FL[8], lane);
    return;
    }
    __builtin_amdgcn_s_setprio(3);
  }

  LAS unsigned char* tb = L + L_K + wave * 16384 + 8192;
  if (ix < 16) {
    if (wave < 4) return;
    const int x = wave & 3, hl = ix >> 2, hg = xc * 4 + hl, qd = ix & 3;
    const rsrc_t ro = mkr(p.wo, 4096 * 512 * 2);
    const int col = xc * 64 + rb * 8;
    lds_wait_ge(&FL[2], (u32)ntask);
    v4u w[8];
    {
      const int rho = lane >> 3, vo = (64 * (rho >> 2) + 8 * (rho & 3)) * 1024 + (lane & 7) * 16;
#pragma unroll
      for (int i = 0; i < 8; ++i) w[i] = ld4<NT>(ro, vo, (hg * 128 + 32 * ((i >> 1) & 1) + 2 * qd + (i & 1)) * 1024 + x * 256 + (i >> 2) * 128);
    }
    BARRIER();
    lds_wait_ge(&FL[8], 2u);
    const int pol = __builtin_amdgcn_readfirstlane(FL[5]) >> 1;
    const v2u ma = poll2(ws, ml_at(xc, pol) + ((lane >> 1) * 2 + (hl >> 1)) * 128 + (hl & 1) * 8);
    v4u f[8];
    tile_frags(tb, lane, w, f); tile_frags(tb, lane, w + 4, f + 4);
    int ob = t1_at(xc, pol, ((hl * 4 + qd) * 32 + (lane >> 1)) * 128 + x * 32 + (lane & 1) * 16);
    int t2o = T2_OFF + row_at(xc * 16 + ix, pol, x * 512 + lane * 4);
    float M = asf(ma.x);
    M = mx(M, dpp<XOR2>(M)); M = mx(M, dpp<ROR4>(M)); M = mx(M, dpp<ROR8>(M)); M = tm16(M, M); M = tm32(M, M);
    float fa = __builtin_amdgcn_exp2f((asf(ma.x) - M) * LOG2E);
    float Ls = fa * asf(ma.y);
    Ls = tr32(Ls, Ls); Ls = tr16(Ls, Ls); Ls += dpp<ROR8>(Ls); Ls += dpp<ROR4>(Ls); Ls += dpp<XOR2>(Ls);
    float inv = __builtin_amdgcn_rcpf(Ls);
    u32 oa = (u32)(uintptr_t)(L + L_X) + x * 16 + (lane & 1) * 8 + (lane >> 5) * 4;
    u32 oa2 = (u32)(uintptr_t)(L + L_X) + g * 16;
    fa *= inv;
    PIN1(fa); PIN1(ob); PIN1(t2o); PIN1(oa); PIN1(oa2);
    SCHED();
    const v4u a = poll4(ws, ob);
    float u0 = tr32(fa * asf(a.x), fa * asf(a.z)), u1 = tr32(fa * asf(a.y), fa * asf(a.w));
    u0 = tr16(u0, u0); u1 = tr16(u1, u1);
    u0 += dpp<ROR8>(u0); u1 += dpp<ROR8>(u1);
    u0 += dpp<ROR4>(u0); u1 += dpp<ROR4>(u1);
    u0 += dpp<XOR2>(u0); u1 += dpp<XOR2>(u1);
    if (!(lane & 30)) *(LAS u32*)(uintptr_t)oa = pk(u0, u1);
    BARRIER();
    const v4u av = *(const LAS v4u*)(uintptr_t)oa2;
    const float y0 = gemv<1>(&av, f, g), y1 = gemv<1>(&av, f + 4, g);
    stc_row(ws, t2o, y0, lane); stc_row(ws, t2o + 256, y1, lane);
    if (!chain || wave != 4) return;
    f32x2 xin = up2(*(const GAS u32*)((const GAS u16*)p.x + col + 2 * g));
    u32 gaw, gbw;
    poll1x2<3072>(ws, MR_OFF + (col + 2 * g) * 2, gaw, gbw);
    f32x2 ga = up2(gaw), gb = up2(gbw);
    int tio = T2_OFF + row_at(lane, pol, col * 4), tio2 = tio + 131072, yo = lane < 56 ? Y_OFF + row_at(lane, pol, col * 4) : 0x40000000;
    PIN1(ga); PIN1(gb); PIN1(xin); PIN1(tio); PIN1(tio2); PIN1(yo);
    v4u pa, pb, pc, pd;
    poll4x4(ws, tio, tio2, pa, pb, pc, pd);
    pa = __builtin_bit_cast(v4u, __builtin_bit_cast(f32x4, pa) + __builtin_bit_cast(f32x4, pc));
    pb = __builtin_bit_cast(v4u, __builtin_bit_cast(f32x4, pb) + __builtin_bit_cast(f32x4, pd));
    const u32 x1 = nosent(pk2(xin + up2(pk2(ga * up2(pk2(sum8x64(pa, pb)))))));
    const v4u o = {rdl(x1, 0), rdl(x1, 16), rdl(x1, 32), rdl(x1, 48)};
    if (lane < 2) stc(ws, X1_OFF + lane * 8192 + col * 16, o);
    if (bid == 0 && lane == 0) stc1(ws, EPOCH_OFF, tag);
    f32x2 x1f = up2(x1);
    PIN1(x1f);
    poll4x2<16>(ws, yo, pa, pb);
    const u32 d = pk2(x1f + up2(pk2(gb * up2(pk2(sum8x64(pa, pb))))));
    if (!n) *(GAS u32*)((GAS u16*)p.out + col + 2 * g) = d;
  } else if (gcu) {
    const int gi = xc * 7 + (ix - 16);
    const rsrc_t rd = mkr(p.wd, 1792 * 512 * 2);
    BARRIER();
    lds_wait_ge(&FL[8], 2u);
    const int pol = __builtin_amdgcn_readfirstlane(FL[5]) >> 1;
    v4u w[12];
    {
      const int rho = lane >> 3;
      const GAS unsigned char* src = (const GAS unsigned char*)((lane & 4) ? p.wu : p.wg) + (size_t)(wave * 64 + 8 * (rho >> 2) + (rho & 3)) * 3584 + gi * 64 + (lane & 3) * 16;
#pragma unroll
      for (int i = 0; i < 8; ++i) w[i] = *(const GAS v4u*)(src + (32 * (i >> 2) + 16 * (i & 1) + 4 * ((i >> 1) & 1)) * 3584);
    }
    tile_load<NT>(rd, tile_voff(lane, 1024), gi * 32 * 1024 + wave * 128, 1024, w + 8);
    v4u f[12];
    tile_frags(tb, lane, w, f); tile_frags(tb, lane, w + 4, f + 4); tile_frags(tb, lane, w + 8, f + 8);
    int yo = Y_OFF + row_at(gi, pol, wave * 256 + lane * 4);
    u32 aa = (u32)(uintptr_t)(L + L_X) + wave * 128 + g * 16, ra = (u32)(uintptr_t)(RED + lane * 8 + wave), sa = (u32)(uintptr_t)(RED + ((n >> 3) * 32 + g * 8 + (n & 7)) * 8);
    u32 ha = (u32)(uintptr_t)(L + L_X) + lane * 16 + wave * 4;
    PIN1(yo); PIN1(aa); PIN1(ra); PIN1(sa); PIN1(ha);
    SCHED();
    if (wave < 4) {
#define SEL4(v) ((wave & 2) ? ((wave & 1) ? (v).w : (v).z) : ((wave & 1) ? (v).y : (v).x))
      v4u sbw, cbw;
      poll4x2<1024>(ws, MR_OFF + 1024 + lane * 16, sbw, cbw);
      f32x2 sb = up2(SEL4(sbw)), c1 = one_plus(SEL4(cbw));
      int xb1 = X1_OFF + (((xc >> 2) ^ pol) << 13) + lane * 128;
      PIN1(sb); PIN1(c1); PIN1(xb1);
      const v4u x1v = poll4(ws, xb1);
      float mb, inv;
      ln_stats(x1v, mb, inv);
      *(LAS u32*)(uintptr_t)ha = modulate(ln_pair(SEL4(x1v), mb, inv), c1, sb);
    }
    BARRIER();
    const v4u av[2] = {*(const LAS v4u*)(uintptr_t)aa, *(const LAS v4u*)(uintptr_t)(aa + 64)};
    *(LAS float*)(uintptr_t)ra = gemv<2>(av, f, g);
    BARRIER();
    const float s = rbf(sum8(sa));
    const float gt = s, up = dpp<ROR8>(s);
    const float u2 = (-2.f * 0.7978845608028654f * LOG2E) * (gt + 0.044715f * gt * gt * gt);
    const float gv = rbf(gt * __builtin_amdgcn_rcpf(1.f + __builtin_amdgcn_exp2f(u2))) * up;
    const u32 d = pk(gv, dpp<SHL1>(gv));
    const v4u ag = {dppu<BC + 0>(d), dppu<BC + 2>(d), dppu<BC + 4>(d), dppu<BC + 6>(d)};
    stc_row(ws, yo, gemv<1>(&ag, f + 8, g), lane);
  }
}
}

extern "C" void kernel_launch(void* const* d_in, const int* in_sizes, int n_in, void* d_out, int out_size, void* d_ws, size_t ws_size, hipStream_t stream) {
  Params a;
  Ptrs& p = a.p;
  p.x = (const u16*)d_in[0]; p.K = (const u16*)d_in[1]; p.V = (const u16*)d_in[2]; p.wq = (const u16*)d_in[3]; p.wo = (const u16*)d_in[4];
  p.wg = (const u16*)d_in[6]; p.wu = (const u16*)d_in[7]; p.wd = (const u16*)d_in[8]; p.c = (const u16*)d_in[9]; p.wmod = (const u16*)d_in[10];
  p.out = (u16*)d_out; p.ws = d_ws;
  for (int i = 0; i < 32; ++i) a.tab[i] = (u32)toff_of(i) | ((u32)nhalf_of(i) << 16);
  hipLaunchKernelGGL(fused_kernel, dim3(NWG), dim3(NTHR), 0, stream, a);
}
```

```cpp
#include <hip/hip_runtime.h>
#include <stdint.h>

namespace {
typedef unsigned short u16;
typedef unsigned u32;
typedef unsigned long long u64;
typedef u32 v4u __attribute__((ext_vector_type(4)));
typedef u32 v2u __attribute__((ext_vector_type(2)));
typedef u32 v8u __attribute__((ext_vector_type(8)));
typedef u32 v16u __attribute__((ext_vector_type(16)));
typedef float f32x4 __attribute__((ext_vector_type(4)));
typedef float f32x2 __attribute__((ext_vector_type(2)));
typedef __bf16 bf16x2 __attribute__((ext_vector_type(2)));
typedef __bf16 bf16x8 __attribute__((ext_vector_type(8)));
typedef short s16x4 __attribute__((ext_vector_type(4)));
#define GAS __attribute__((address_space(1)))
#define LAS __attribute__((address_space(3)))

#define KV_SFX " nt"
constexpr int NT = 2;
constexpr int THR_W = 8;
constexpr int THR_Q = 6;
constexpr int H_W = 17, H_G = 18, H_M = 16, H_P = 18;

constexpr int NWG = 256, NTHR = 1024;
constexpr u32 SENT = 0xFFFFFFFFu;
constexpr int SC1 = 16;
constexpr u32 SPIN_CAP = 1u << 17;
constexpr float LOG2E = 1.4426950408889634f;
constexpr int EPOCH_OFF = 0;
constexpr int CF_OFF = 8192;
constexpr int Q_OFF = 16384;
constexpr int H0_OFF = 32768;
constexpr int S_OFF = 65536;
constexpr int VOTE_OFF = CF_OFF + 1024;
constexpr int T1_OFF = S_OFF;
constexpr int ML_OFF = T1_OFF + 524288;
constexpr int T2_OFF = ML_OFF + 65536;
constexpr int Y_OFF = T2_OFF + 262144;
constexpr int X1_OFF = Y_OFF + 114688;
constexpr int MR_OFF = X1_OFF + 16384;
__device__ __forceinline__ int par_of(int par, int x) { return (((par >> (x & 3)) ^ (x >> 2)) & 1) << 13; }
__device__ __forceinline__ int t1_at(int xc, int par, int off) { return T1_OFF + (xc & 3) * 131072 + (off >> 13) * 16384 + par_of(par, xc) + (off & 8191); }
__device__ __forceinline__ int ml_at(int xc, int par) { return ML_OFF + (xc & 3) * 16384 + par_of(par, xc); }
__device__ __forceinline__ int row_at(int prod, int par, int colbyte) { return (prod >> 3) * 16384 + par_of(par, colbyte >> 8) + (prod & 7) * 1024 + (colbyte & 1023); }
constexpr int CLR_KB = 16;
constexpr int S_END = S_OFF + 64 * CLR_KB * 1024;
static_assert(MR_OFF + 4096 <= S_END && S_OFF % 16384 == 0 && ML_OFF % 16384 == 0 && T2_OFF % 16384 == 0 && Y_OFF % 16384 == 0 && X1_OFF % 16384 == 0, "");
constexpr int PROBE_OFF = S_END, WS_BYTES = S_END + 16384;
constexpr int L_K = 0, L_TB = 8 * 16384, L_Q = 9 * 16384, L_FLAG = L_Q + 1280, L_ML = L_FLAG + 64, L_X = L_ML + 512, L_RED = L_X + 1024, L_END = L_RED + 4096;
static_assert(L_END <= 163840 && L_RED % 16 == 0 && L_TB % 16 == 0, "");
constexpr int CH_ALL = 512 - 8 * H_W - 7 * H_G - 4 * H_M - 5 * H_P, H_CH = CH_ALL / 8, CH_HI = CH_ALL - 8 * H_CH;
constexpr int nhalf_of(int ix) { return ix < 8 ? (ix < CH_HI ? H_CH + 1 : H_CH) : ix < 16 ? H_W : ix < 23 ? H_G : ix < 27 ? H_M : H_P; }
constexpr int toff_of(int ix) { int o = 0; for (int i = 0; i < ix; ++i) o += nhalf_of(i); return o; }
static_assert(toff_of(32) == 512 && H_G >= 8 && H_G <= 18 && H_CH >= 8 && H_CH + 1 <= 16 && H_M >= 8 && H_M <= 16 && H_W >= 8 && H_W <= 18 && H_P >= 8 && H_P <= 18, "");

struct Ptrs {
  const u16 *x, *K, *V, *wq, *wo, *wg, *wu, *wd, *c, *wmod;
  u16* out;
  void* ws;
};
struct Params { Ptrs p; u32 tab[32]; };
static_assert(sizeof(Params) == 96 + 128 && L_K == 0, "");

__device__ __forceinline__ float asf(u32 x) { return __builtin_bit_cast(float, x); }
__device__ __forceinline__ u32 asu(float x) { return __builtin_bit_cast(u32, x); }
__device__ __forceinline__ float blo(u32 w) { return asf(w << 16); }
__device__ __forceinline__ float bhi(u32 w) { return asf(w & 0xffff0000u); }
__device__ __forceinline__ u32 pk2(f32x2 v) { return __builtin_bit_cast(u32, __builtin_convertvector(v, bf16x2)); }
__device__ __forceinline__ u32 pk(float lo, float hi) { return pk2(f32x2{lo, hi}); }
__device__ __forceinline__ f32x2 up2(u32 w) { return f32x2{blo(w), bhi(w)}; }
__device__ __forceinline__ float rbf(float f) { return (float)(__bf16)f; }
__device__ __forceinline__ float dot2(u32 a, u32 b, float c) { return __builtin_amdgcn_fdot2_f32_bf16(__builtin_bit_cast(bf16x2, a), __builtin_bit_cast(bf16x2, b), c, false); }
template <int CTRL> __device__ __forceinline__ float dpp(float x) { return asf(__builtin_amdgcn_mov_dpp(asu(x), CTRL, 0xf, 0xf, true)); }
template <int CTRL> __device__ __forceinline__ u32 dppu(u32 x) { return __builtin_amdgcn_mov_dpp(x, CTRL, 0xf, 0xf, true); }
constexpr int XOR1 = 0xB1, XOR2 = 0x4E, XOR7 = 0x141, ROR8 = 0x128, ROR4 = 0x124, SHL1 = 0x101, SHL2 = 0x102, BC = 0x150;
__device__ __forceinline__ float mx(float a, float b) { return __builtin_amdgcn_fmed3f(a, b, __builtin_inff()); }
__device__ __forceinline__ float tr32(float a, float b) { auto s = __builtin_amdgcn_permlane32_swap(asu(a), asu(b), false, false); return asf(s[0]) + asf(s[1]); }
__device__ __forceinline__ float tr16(float a, float b) { auto s = __builtin_amdgcn_permlane16_swap(asu(a), asu(b), false, false); return asf(s[0]) + asf(s[1]); }
__device__ __forceinline__ float tm32(float a, float b) { auto s = __builtin_amdgcn_permlane32_swap(asu(a), asu(b), false, false); return mx(asf(s[0]), asf(s[1])); }
__device__ __forceinline__ float tm16(float a, float b) { auto s = __builtin_amdgcn_permlane16_swap(asu(a), asu(b), false, false); return mx(asf(s[0]), asf(s[1])); }
__device__ __forceinline__ float row_sum(float v) { v += dpp<ROR8>(v); v += dpp<XOR7>(v); v += dpp<XOR2>(v); v += dpp<XOR1>(v); return v; }
__device__ __forceinline__ float row_max(float v) { v = mx(v, dpp<ROR8>(v)); v = mx(v, dpp<XOR7>(v)); v = mx(v, dpp<XOR2>(v)); v = mx(v, dpp<XOR1>(v)); return v; }
__device__ __forceinline__ f32x2 sum8x64(v4u a, v4u b) {
  const float y0 = tr32(asf(a.x), asf(b.x)), y1 = tr32(asf(a.y), asf(b.y)), y2 = tr32(asf(a.z), asf(b.z)), y3 = tr32(asf(a.w), asf(b.w));
  return f32x2{row_sum(tr16(y0, y2)), row_sum(tr16(y1, y3))};
}
__device__ __forceinline__ u32 rdl(u32 v, int l) { return __builtin_amdgcn_readlane(v, l); }
__device__ __forceinline__ float rdlf(float v, int l) { return asf(__builtin_amdgcn_readlane(asu(v), l)); }
__device__ __forceinline__ u32 nosent(u32 w) { return w < 0xFFFFFFFEu ? w : 0xFFFFFFFEu; }
typedef __amdgpu_buffer_rsrc_t rsrc_t;
__device__ __forceinline__ rsrc_t mkr(const void* q, int bytes) { return __builtin_amdgcn_make_buffer_rsrc((void*)q, 0, bytes, 0x00020000); }
template <int AUX> __device__ __forceinline__ v4u ld4(rsrc_t r, int off, int so) { return __builtin_amdgcn_raw_buffer_load_b128(r, off, so, AUX); }
__device__ __forceinline__ u32 ld1(rsrc_t r, int off, int so) { return __builtin_amdgcn_raw_buffer_load_b32(r, off, so, 0); }
__device__ __forceinline__ v4u ldc(rsrc_t r, int off) { return __builtin_amdgcn_raw_buffer_load_b128(r, off, 0, SC1); }
__device__ __forceinline__ u32 ldc1(rsrc_t r, int off) { return __builtin_amdgcn_raw_buffer_load_b32(r, off, 0, SC1); }
__device__ __forceinline__ void stc(rsrc_t r, int off, v4u v) { __builtin_amdgcn_raw_buffer_store_b128(v, r, off, 0, SC1); }
__device__ __forceinline__ void stc2(rsrc_t r, int off, v2u v) { __builtin_amdgcn_raw_buffer_store_b64(v, r, off, 0, SC1); }
__device__ __forceinline__ void stc_row(rsrc_t r, int off, float v, int lane) {
  const v4u o = {asu(v), asu(dpp<0x55>(v)), asu(dpp<0xAA>(v)), asu(dpp<0xFF>(v))};
  if (!(lane & 3)) stc(r, off, o);
}
__device__ __forceinline__ void stc1(rsrc_t r, int off, u32 v) { __builtin_amdgcn_raw_buffer_store_b32(v, r, off, 0, SC1); }
#define MEMFENCE() asm volatile("" ::: "memory")
#define LGKM0() asm volatile("s_waitcnt lgkmcnt(0)" ::: "memory")
#define VM0() asm volatile("s_waitcnt vmcnt(0)" ::: "memory")
#define SCHED() __builtin_amdgcn_sched_barrier(0)
#define BARRIER() do { LGKM0(); SCHED(); __builtin_amdgcn_s_barrier(); MEMFENCE(); SCHED(); } while (0)
#define PIN1(a) asm volatile("" : "+v"(a))
#define PIN4(a) asm volatile("" : "+v"((a)[0]), "+v"((a)[1]), "+v"((a)[2]), "+v"((a)[3]))
#define PIN8(a) asm volatile("" : "+v"((a)[0]), "+v"((a)[1]), "+v"((a)[2]), "+v"((a)[3]), "+v"((a)[4]), "+v"((a)[5]), "+v"((a)[6]), "+v"((a)[7]))

#define POLL_HEAD "s_mov_b32 %[n], %[cap]\n\t1:\n\t"
#define POLL_TAIL "s_cbranch_execz 2f\n\ts_sub_u32 %[n], %[n], 1\n\ts_cbranch_scc0 1b\n\t2:\n\ts_mov_b64 exec, -1\n\ts_nop 2"
__device__ __forceinline__ v4u poll4(rsrc_t r, int off) {
  v4u a; u32 n;
  asm volatile(POLL_HEAD "buffer_load_dwordx4 v[124:127], %[o], %[r], 0 offen sc1\n\ts_waitcnt vmcnt(0)\n\t"
               "v_max_u32 v123, v124, v125\n\tv_max3_u32 v123, v123, v126, v127\n\tv_cmpx_eq_u32 vcc, -1, v123\n\t" POLL_TAIL
               : "=&{v[124:127]}"(a), [n] "=&s"(n) : [o] "v"(off), [r] "s"(r), [cap] "i"(SPIN_CAP) : "memory", "vcc", "scc", "v123");
  return a;
}
template <int D> __device__ __forceinline__ void poll4x2(rsrc_t r, int off, v4u& a, v4u& b) {
  u32 n;
  asm volatile(POLL_HEAD "buffer_load_dwordx4 v[120:123], %[o], %[r], 0 offen sc1\n\tbuffer_load_dwordx4 v[124:127], %[o], %[r], 0 offen offset:%[d] sc1\n\ts_waitcnt vmcnt(1)\n\t"
               "v_max_u32 v119, v120, v121\n\tv_max3_u32 v119, v119, v122, v123\n\ts_waitcnt vmcnt(0)\n\tv_max3_u32 v119, v119, v124, v125\n\tv_max3_u32 v119, v119, v126, v127\n\t"
               "v_cmpx_eq_u32 vcc, -1, v119\n\t" POLL_TAIL
               : "=&{v[120:123]}"(a), "=&{v[124:127]}"(b), [n] "=&s"(n) : [o] "v"(off), [r] "s"(r), [cap] "i"(SPIN_CAP), [d] "i"(D) : "memory", "vcc", "scc", "v119");
}
__device__ __forceinline__ void poll4x4(rsrc_t r, int off, int off2, v4u& a, v4u& b, v4u& c, v4u& d) {
  u32 n;
  asm volatile(POLL_HEAD "buffer_load_dwordx4 v[112:115], %[o], %[r], 0 offen sc1\n\tbuffer_load_dwordx4 v[116:119], %[o], %[r], 0 offen offset:16 sc1\n\t"
               "buffer_load_dwordx4 v[120:123], %[o2], %[r], 0 offen sc1\n\tbuffer_load_dwordx4 v[124:127], %[o2], %[r], 0 offen offset:16 sc1\n\t"
               "s_waitcnt vmcnt(3)\n\tv_max_u32 v110, v112, v113\n\tv_max3_u32 v110, v110, v114, v115\n\t"
               "s_waitcnt vmcnt(2)\n\tv_max_u32 v111, v116, v117\n\tv_max3_u32 v111, v111, v118, v119\n\t"
               "s_waitcnt vmcnt(1)\n\tv_max3_u32 v110, v110, v120, v121\n\tv_max3_u32 v111, v111, v122, v123\n\t"
               "s_waitcnt vmcnt(0)\n\tv_max3_u32 v110, v110, v124, v125\n\tv_max3_u32 v111, v111, v126, v127\n\t"
               "v_max_u32 v110, v110, v111\n\tv_cmpx_eq_u32 vcc, -1, v110\n\t" POLL_TAIL
               : "=&{v[112:115]}"(a), "=&{v[116:119]}"(b), "=&{v[120:123]}"(c), "=&{v[124:127]}"(d), [n] "=&s"(n)
               : [o] "v"(off), [o2] "v"(off2), [r] "s"(r), [cap] "i"(SPIN_CAP) : "memory", "vcc", "scc", "v110", "v111");
}
__device__ __forceinline__ v2u poll2(rsrc_t r, int off) {
  v2u a; u32 n;
  asm volatile(POLL_HEAD "buffer_load_dwordx2 v[126:127], %[o], %[r], 0 offen sc1\n\ts_waitcnt vmcnt(0)\n\t"
               "v_max_u32 v125, v126, v127\n\tv_cmpx_eq_u32 vcc, -1, v125\n\ts_cbranch_execz 2f\n\ts_sleep 1\n\ts_sub_u32 %[n], %[n], 1\n\ts_cbranch_scc0 1b\n\t2:\n\ts_mov_b64 exec, -1\n\ts_nop 2"
               : "=&{v[126:127]}"(a), [n] "=&s"(n) : [o] "v"(off), [r] "s"(r), [cap] "i"(SPIN_CAP) : "memory", "vcc", "scc", "v125");
  return a;
}
template <int D> __device__ __forceinline__ void poll1x2(rsrc_t r, int off, u32& a, u32& b) {
  u32 n;
  asm volatile(POLL_HEAD "buffer_load_dword v126, %[o], %[r], 0 offen sc1\n\tbuffer_load_dword v127, %[o], %[r], 0 offen offset:%[d] sc1\n\ts_waitcnt vmcnt(0)\n\t"
               "v_max_u32 v125, v126, v127\n\tv_cmpx_eq_u32 vcc, -1, v125\n\t" POLL_TAIL
               : "=&{v126}"(a), "=&{v127}"(b), [n] "=&s"(n) : [o] "v"(off), [r] "s"(r), [cap] "i"(SPIN_CAP), [d] "i"(D) : "memory", "vcc", "scc", "v125");
}
__device__ __forceinline__ void pollT4(rsrc_t r, int off, u32 tag, u32 ntag, v4u& a, v4u& b, v4u& c, v4u& d) {
  u32 n;
  asm volatile(POLL_HEAD "buffer_load_dwordx4 v[112:115], %[o], %[r], 0 offen sc1\n\tbuffer_load_dwordx4 v[116:119], %[o], %[r], 0 offen offset:512 sc1\n\t"
               "buffer_load_dwordx4 v[120:123], %[o], %[r], 0 offen offset:1024 sc1\n\tbuffer_load_dwordx4 v[124:127], %[o], %[r], 0 offen offset:1536 sc1\n\t"
               "s_waitcnt vmcnt(3)\n\tv_xor_b32 v110, v112, %[t]\n\tv_xor_b32 v111, v114, %[u]\n\t"
               "s_waitcnt vmcnt(2)\n\tv_bitop3_b32 v110, v116, %[t], v110 bitop3:0xbe\n\tv_bitop3_b32 v111, v118, %[u], v111 bitop3:0xbe\n\t"
               "s_waitcnt vmcnt(1)\n\tv_bitop3_b32 v110, v120, %[t], v110 bitop3:0xbe\n\tv_bitop3_b32 v111, v122, %[u], v111 bitop3:0xbe\n\t"
               "s_waitcnt vmcnt(0)\n\tv_bitop3_b32 v110, v124, %[t], v110 bitop3:0xbe\n\tv_bitop3_b32 v111, v126, %[u], v111 bitop3:0xbe\n\t"
               "v_or_b32 v110, v110, v111\n\tv_cmpx_ne_u32 vcc, 0, v110\n\t" POLL_TAIL
               : "=&{v[112:115]}"(a), "=&{v[116:119]}"(b), "=&{v[120:123]}"(c), "=&{v[124:127]}"(d), [n] "=&s"(n)
               : [o] "v"(off), [r] "s"(r), [cap] "i"(SPIN_CAP), [t] "v"(tag), [u] "v"(ntag) : "memory", "vcc", "scc", "v110", "v111");
}
__device__ __forceinline__ void lds_wait_ge(volatile LAS u32* f, u32 want) {
  _Pragma("nounroll") for (u32 s = 0; s < (SPIN_CAP << 3); ++s) { if (*f >= want) break; __builtin_amdgcn_s_sleep(1); }
  MEMFENCE();
}
__device__ __forceinline__ void lds_inc(volatile LAS u32* f, int lane) {
  asm volatile("s_mov_b64 exec, 1\n\tds_add_u32 %0, %1\n\ts_mov_b64 exec, -1" :: "v"((u32)(uintptr_t)f), "v"(1u) : "memory");
}
__device__ __forceinline__ u32 lds_ticket(volatile LAS u32* f, int lane) {
  u32 old;
  asm volatile("s_mov_b64 exec, 1\n\tds_add_rtn_u32 %0, %1, %2\n\ts_mov_b64 exec, -1\n\ts_waitcnt lgkmcnt(0)" : "=&v"(old) : "v"((u32)(uintptr_t)f), "v"(1u) : "memory");
  return __builtin_amdgcn_readfirstlane(old);
}
__device__ __forceinline__ float sum8(u32 addr) {
  const f32x4 t = *(const LAS f32x4*)(uintptr_t)addr + *(const LAS f32x4*)(uintptr_t)(addr + 16);
  return (t.x + t.z) + (t.y + t.w);
}
__device__ __forceinline__ float silu(float v) { return v / (1.f + __expf(-v)); }
__device__ __forceinline__ u32 silu2(u32 c) { return pk(silu(blo(c)), silu(bhi(c))); }
__device__ __forceinline__ void ln_stats(v4u xv, float& mb, float& inv) {
  constexpr u32 ONE2 = 0x3f803f80u;
  const float s = dot2(xv[0], ONE2, dot2(xv[1], ONE2, dot2(xv[2], ONE2, dot2(xv[3], ONE2, 0.f))));
  const float q = dot2(xv[0], xv[0], dot2(xv[1], xv[1], dot2(xv[2], xv[2], dot2(xv[3], xv[3], 0.f))));
  float t = row_sum(tr32(s, q));
  t = tr16(t, t);
  const float mean = rdlf(t, 0) * (1.f / 512.f), sq = rdlf(t, 32) * (1.f / 512.f);
  float var = fmaf(-mean, mean, sq);
  if (__builtin_expect(!(var * 64.f >= sq), 0)) {
    float d = 0.f;
#pragma unroll
    for (int i = 0; i < 4; ++i) { const float a = blo(xv[i]) - mean, b = bhi(xv[i]) - mean; d = fmaf(a, a, fmaf(b, b, d)); }
    d = row_sum(tr32(d, d));
    var = tr16(d, d) * (1.f / 512.f);
  }
  mb = rbf(mean);
  inv = __builtin_amdgcn_rcpf(rbf(__builtin_amdgcn_sqrtf(rbf(rbf(var) + 1e-5f))));
}
__device__ __forceinline__ u32 ln_pair(u32 x, float mb, float inv) { return pk2(up2(pk2(up2(x) - mb)) * inv); }
__device__ __forceinline__ f32x2 one_plus(u32 c) { return up2(pk2(up2(c) + 1.f)); }
__device__ __forceinline__ u32 modulate(u32 y, f32x2 c1, f32x2 sh) { return pk2(up2(pk2(up2(y) * c1)) + sh); }
__device__ __forceinline__ int tile_voff(int lane, int stride) { const int rho = lane >> 3; return (8 * (rho >> 2) + (rho & 3)) * stride + (lane & 7) * 16; }
template <int AUX> __device__ __forceinline__ void tile_load(rsrc_t r, int voff, int soff, int stride, v4u* w) {
#pragma unroll
  for (int i = 0; i < 4; ++i) w[i] = ld4<AUX>(r, voff, soff + (16 * (i & 1) + 4 * (i >> 1)) * stride);
}
__device__ __forceinline__ void tile_frags(LAS unsigned char* tb, int lane, const v4u* w, v4u* f) {
  const int rho = lane >> 3;
  LAS unsigned char* wr = tb + rho * 128 + (((lane & 7) ^ (rho & 6)) << 4);
  const int row = 4 * (lane >> 4) + ((lane >> 2) & 3);
  const LAS unsigned char* rd = tb + row * 128 + (lane & 3) * 8;
  const int sw = (row >> 1) & 3;
  v2u h[2][4];
#pragma unroll
  for (int t = 0; t < 2; ++t) {
    *(LAS v4u*)wr = w[2 * t]; *(LAS v4u*)(wr + 1024) = w[2 * t + 1];
    MEMFENCE();
#pragma unroll
    for (int ct = 0; ct < 4; ++ct) h[t][ct] = __builtin_bit_cast(v2u, __builtin_amdgcn_ds_read_tr16_b64_v4i16((LAS s16x4*)(rd + ((ct ^ sw) << 5))));
    MEMFENCE();
  }
#pragma unroll
  for (int ct = 0; ct < 4; ++ct) f[ct] = v4u{h[0][ct].x, h[0][ct].y, h[1][ct].x, h[1][ct].y};
  PIN4(f);
}
__device__ __forceinline__ void lds_frags(const LAS unsigned char* tile, int lane, v4u* f) {
  const int row = 4 * (lane >> 4) + ((lane >> 2) & 3);
  const LAS unsigned char* rd = tile + row * 128 + (lane & 3) * 8;
  const int sw = (row >> 1) & 3;
#pragma unroll
  for (int ct = 0; ct < 4; ++ct) {
    const v2u lo = __builtin_bit_cast(v2u, __builtin_amdgcn_ds_read_tr16_b64_v4i16((LAS s16x4*)(rd + ((ct ^ sw) << 5))));
    const v2u hi = __builtin_bit_cast(v2u, __builtin_amdgcn_ds_read_tr16_b64_v4i16((LAS s16x4*)(rd + 2048 + ((ct ^ sw) << 5))));
    f[ct] = v4u{lo.x, lo.y, hi.x, hi.y};
  }
  PIN4(f);
}
__device__ __forceinline__ f32x4 mfma32(v4u a, v4u b, f32x4 c) { return __builtin_amdgcn_mfma_f32_16x16x32_bf16(__builtin_bit_cast(bf16x8, a), __builtin_bit_cast(bf16x8, b), c, 0, 0, 0); }
template <int NS> __device__ __forceinline__ float gemv(const v4u* a, const v4u* f, int g) {
  f32x4 acc[4];
#pragma unroll
  for (int ct = 0; ct < 4; ++ct) acc[ct] = mfma32(a[0], f[ct], f32x4{0.f, 0.f, 0.f, 0.f});
#pragma unroll
  for (int s = 1; s < NS; ++s)
#pragma unroll
    for (int ct = 0; ct < 4; ++ct) acc[ct] = mfma32(a[s], f[4 * s + ct], acc[ct]);
  PIN4(acc);
  const float lo = (g & 1) ? acc[1][0] : acc[0][0], hi = (g & 1) ? acc[3][0] : acc[2][0];
  return (g & 2) ? hi : lo;
}
template <int B> __device__ __forceinline__ v4u bcast4(u32 v) { return v4u{dppu<BC + B>(v), dppu<BC + B + 1>(v), dppu<BC + B + 2>(v), dppu<BC + B + 3>(v)}; }

__global__ void __launch_bounds__(NTHR, 4) fused_kernel(Params) {
  __shared__ __attribute__((aligned(128))) unsigned char lds_raw[L_END];
  LAS unsigned char* L = (LAS unsigned char*)lds_raw;
  v16u ka; v8u kb8;
  int wave; u32 rtt, tab, ko0, ko1, ko2, ko3, ep, d1; v4u xs;
  {
    u32 sa, sb, sc, keep, va, vb; u64 pa;
#define DMA4(P) "global_load_lds_dwordx4 %[k0], " P KV_SFX "\n\tglobal_load_lds_dwordx4 %[k1], " P " offset:1024" KV_SFX "\n\t" \
                "global_load_lds_dwordx4 %[k2], " P " offset:2048" KV_SFX "\n\tglobal_load_lds_dwordx4 %[k3], " P " offset:3072" KV_SFX "\n\t"
    asm volatile("s_load_dwordx16 s[16:31], %[kp], 0x0\n\ts_load_dwordx8 s[36:43], %[kp], 0x40\n\t"
                 "s_lshr_b32 s46, %[bid], 3\n\ts_lshl_b32 s46, s46, 2\n\ts_add_u32 s46, s46, 0x60\n\ts_load_dword s46, %[kp], s46"
                 : "=&{s[16:31]}"(ka), "=&{s[36:43]}"(kb8), "=&{s46}"(tab) : [kp] "s"(__builtin_amdgcn_kernarg_segment_ptr()), [bid] "s"(blockIdx.x) : "scc");
    asm volatile(
      "s_lshr_b32 %[sa], %[bid], 3\n\t"
      "v_readfirstlane_b32 %[w], %[tid]\n\ts_mov_b32 %[keep], m0\n\ts_and_b32 %[sc], %[bid], 7\n\t"
      "s_lshr_b32 %[w], %[w], 6\n\ts_cmp_lt_u32 %[w], 8\n\ts_cbranch_scc1 1f\n\t"
      "v_bfe_u32 %[va], %[tid], 4, 2\n\tv_and_b32 %[vb], 15, %[tid]\n\tv_xor_b32 %[vb], %[vb], %[va]\n\tv_lshlrev_b32 %[vb], 4, %[vb]\n\t"
      "v_lshl_or_b32 %[k0], %[va], 8, %[vb]\n\tv_xor_b32 %[k1], 64, %[k0]\n\tv_xor_b32 %[k2], 0x80, %[k0]\n\tv_xor_b32 %[k3], 0xc0, %[k0]\n\t"
      "s_sub_u32 %[sb], %[w], 8\n\ts_lshl_b32 m0, %[sb], 14\n\ts_lshl_b32 %[sc], %[sc], 8\n\ts_add_u32 %[sb], %[sb], %[sc]\n\ts_lshl_b32 %[sb], %[sb], 1\n\t"
      "s_cmp_gt_u32 %[w], 11\n\ts_cselect_b32 %[sa], 0, %[sa]\n\ts_cmp_lt_u32 %[sa], 8\n\t"
      "s_waitcnt lgkmcnt(0)\n\ts_cbranch_scc1 9f\n\t"
      "s_and_b32 %[sc], s46, 0xffff\n\ts_add_u32 %[sb], %[sb], %[sc]\n\ts_lshl_b32 %[sb], %[sb], 13\n\t"
      "s_add_u32 s44, s18, %[sb]\n\ts_addc_u32 s45, s19, 0\n\t" DMA4("s[44:45]")
      "s_branch 9f\n\t"
      "1:\n\tv_bfe_u32 %[va], %[tid], 3, 3\n\tv_and_b32 %[vb], 6, %[va]\n\tv_xor_b32 %[vb], %[vb], %[tid]\n\tv_and_b32 %[vb], 7, %[vb]\n\tv_lshlrev_b32 %[vb], 4, %[vb]\n\t"
      "v_and_b32 %[k1], 4, %[va]\n\tv_add_u32 %[va], %[va], %[k1]\n\t"
      "v_and_b32 v106, 63, %[tid]\n\tv_lshlrev_b32 v106, 4, v106\n\tv_mov_b32 v104, 0\n\t"
      "v_and_b32 v105, 3, %[tid]\n\tv_lshlrev_b32 v105, 2, v105\n\tv_and_or_b32 v105, %[tid], 48, v105\n\t"
      "s_cmp_lt_u32 %[sa], 8\n\ts_cbranch_scc0 2f\n\t"
      "v_lshl_or_b32 %[k0], %[va], 13, %[vb]\n\tv_add_u32 %[k1], 0x1fc00, %[k0]\n\tv_add_u32 %[k2], 0x7800, %[k0]\n\tv_add_u32 %[k3], 0x27400, %[k0]\n\t"
      "v_bfe_u32 %[va], %[tid], 2, 1\n\tv_lshl_or_b32 v105, %[va], 6, v105\n\ts_lshl_b32 %[sb], %[w], 7\n\tv_add_u32 v105, %[sb], v105\n\t"
      "s_lshl_b32 m0, %[w], 13\n\ts_lshl_b32 %[sb], %[w], 9\n\ts_add_u32 %[sb], %[sb], %[sc]\n\ts_lshl_b32 %[sb], %[sb], 3\n\ts_add_u32 %[sb], %[sb], %[sa]\n\ts_lshl_b32 %[sb], %[sb], 7\n\t"
      "s_waitcnt lgkmcnt(0)\n\t"
      "global_load_dword v105, v105, s[16:17]\n\tglobal_load_dwordx4 v[106:109], v106, s[16:17]\n\tglobal_load_dword v104, v104, s[42:43] sc1\n\t"
      "s_add_u32 s44, s22, %[sb]\n\ts_addc_u32 s45, s23, 0\n\t" DMA4("s[44:45]")
      "s_add_u32 s44, s44, 0x40000\n\ts_addc_u32 s45, s45, 0\n\ts_add_u32 m0, m0, 0x1000\n\ts_nop 0\n\t" DMA4("s[44:45]")
      "s_branch 9f\n\t"
      "2:\n\tv_mul_u32_u24 %[k0], 0x1800, %[va]\n\tv_add_u32 %[k0], %[k0], %[vb]\n\tv_add_u32 %[k1], 0x17c00, %[k0]\n\tv_add_u32 %[k2], 0x5800, %[k0]\n\tv_add_u32 %[k3], 0x1d400, %[k0]\n\t"
      "s_lshr_b32 %[sb], %[sa], 3\n\ts_and_b32 s44, %[w], 5\n\ts_lshl_b32 %[sb], %[sb], 3\n\ts_or_b32 %[sb], %[sb], s44\n\ts_cmp_eq_u32 %[sb], 17\n\ts_cbranch_scc0 5f\n\t"
      "s_lshl_b32 %[sb], %[w], 12\n\ts_and_b32 %[sb], %[sb], 0x2000\n\ts_and_b32 s44, %[bid], 63\n\ts_lshl_b32 s44, s44, 7\n\ts_add_u32 %[sb], %[sb], s44\n\ts_add_u32 %[sb], %[sb], %[pr]\n\t"
      "v_mov_b32 v107, %[sb]\n\ts_mov_b64 exec, 1\n\ts_waitcnt lgkmcnt(0)\n\ts_memtime s[44:45]\n\ts_waitcnt lgkmcnt(0)\n\t"
      "global_store_dword v107, v104, s[42:43] sc1\n\ts_barrier\n\ts_waitcnt vmcnt(0)\n\ts_memtime s[50:51]\n\ts_waitcnt lgkmcnt(0)\n\ts_sub_u32 %[rt], s50, s44\n\ts_mov_b64 exec, -1\n\t"
      "5:\n\ts_sub_u32 %[sb], %[sa], 23\n\ts_cmp_lt_u32 %[sb], 4\n\ts_cbranch_scc0 3f\n\t"
      "v_bfe_u32 %[va], %[tid], 2, 1\n\tv_lshl_or_b32 v105, %[va], 6, v105\n\ts_lshl_b32 s44, %[w], 7\n\tv_add_u32 v105, s44, v105\n\t"
      "s_lshl_b32 s44, %[w], 13\n\ts_add_u32 m0, s44, 0x10000\n\ts_lshl_b32 %[sc], %[sc], 2\n\ts_add_u32 %[sb], %[sb], %[sc]\n\ts_add_u32 %[sb], %[sb], 16\n\ts_lshl_b32 %[sb], %[sb], 7\n\t"
      "s_mul_i32 %[sc], %[w], 0x60000\n\ts_add_u32 %[sb], %[sb], %[sc]\n\t"
      "s_waitcnt lgkmcnt(0)\n\t"
      "global_load_dword v105, v105, s[36:37]\n\tglobal_load_dword v104, v104, s[42:43] sc1\n\t"
      "s_add_u32 s44, s38, %[sb]\n\ts_addc_u32 s45, s39, 0\n\t" DMA4("s[44:45]")
      "s_add_u32 s44, s44, 0x30000\n\ts_addc_u32 s45, s45, 0\n\ts_add_u32 m0, m0, 0x1000\n\ts_nop 0\n\t" DMA4("s[44:45]")
      "s_branch 9f\n\t3:\n\t"
      "s_and_b32 %[sb], %[sa], 3\n\ts_lshl_b32 %[sb], %[sb], 2\n\ts_and_b32 s44, %[w], 3\n\ts_add_u32 %[sb], %[sb], s44\n\t"
      "s_lshl_b32 s44, s44, 12\n\ts_add_u32 m0, s44, %[tb]\n\ts_lshl_b32 s44, %[sb], 6\n\tv_add_u32 v105, s44, v105\n\t"
      "s_mul_i32 %[sb], %[sb], 0x30000\n\ts_lshl_b32 %[sc], %[sc], 8\n\ts_add_u32 %[sb], %[sb], %[sc]\n\ts_bfe_u32 %[sc], %[sa], 0x10002\n\ts_lshl_b32 %[sc], %[sc], 7\n\ts_add_u32 %[sb], %[sb], %[sc]\n\t"
      "s_lshr_b32 %[sc], %[w], 2\n\ts_lshr_b32 %[sa], %[sa], 3\n\ts_cmp_eq_u32 %[sa], 1\n\ts_cselect_b32 %[sa], %[sc], 0\n\ts_cmp_eq_u32 %[sa], 1\n\t"
      "s_waitcnt lgkmcnt(0)\n\t"
      "global_load_dword v105, v105, s[36:37]\n\tglobal_load_dword v104, v104, s[42:43] sc1\n\ts_cbranch_scc0 9f\n\t"
      "s_add_u32 s44, s38, %[sb]\n\ts_addc_u32 s45, s39, 0\n\t" DMA4("s[44:45]")
      "9:\n\ts_mov_b32 m0, %[keep]"
      : "=&{s[44:45]}"(pa), [w] "=&s"(wave), [k0] "=&v"(ko0), [k1] "=&v"(ko1), [k2] "=&v"(ko2), [k3] "=&v"(ko3),
        "=&{v104}"(ep), "=&{v105}"(d1), "=&{v[106:109]}"(xs), [sa] "=&s"(sa), [sb] "=&s"(sb), [sc] "=&s"(sc), [keep] "=&s"(keep), [va] "=&v"(va), [vb] "=&v"(vb), [rt] "=&s"(rtt)
      : "{s[16:31]}"(ka), "{s[36:43]}"(kb8), "{s46}"(tab), [bid] "s"(blockIdx.x), [tid] "v"(threadIdx.x), [tb] "i"(L_TB), [pr] "i"(PROBE_OFF)
      : "memory", "scc", "s50", "s51");
    asm volatile("" : "+{s[16:31]}"(ka), "+{s[36:43]}"(kb8), "+{s46}"(tab));
  }
  volatile LAS u32* FL = (volatile LAS u32*)(L + L_FLAG);
  LAS float* RED = (LAS float*)(L + L_RED);
  const int lane = threadIdx.x & 63;
  const int bid = blockIdx.x, xc = bid & 7, ix = bid >> 3, rb = ix & 7;
  const int g = lane >> 4, n = lane & 15;
  const bool chain = ix < 8, wcu = (ix >> 3) == 1;
  const int toff = tab & 0xffff, nhalf = tab >> 16, ntask = (nhalf + 1) >> 1;
  const bool ninth = wave == 2 && ntask == 9;
  const int aw = ninth ? 8 : wave - 8;
  const bool head = (wave >> 2) == 2 && !chain, p1 = wcu && (wave >> 2) == 1;
  const u32 ldst = (u32)(uintptr_t)L + L_K + aw * 16384;
#define KGROUP(kb, lo) do { u32 keep_; \
    asm volatile("s_mov_b32 %0, m0\n\ts_mov_b32 m0, %6\n\ts_nop 0\n\t" \
                 "global_load_lds_dwordx4 %1, %5" KV_SFX "\n\t" \
                 "global_load_lds_dwordx4 %2, %5 offset:1024" KV_SFX "\n\t" \
                 "global_load_lds_dwordx4 %3, %5 offset:2048" KV_SFX "\n\t" \
                 "global_load_lds_dwordx4 %4, %5 offset:3072" KV_SFX "\n\t" \
                 "s_mov_b32 m0, %0" \
                 : "=&s"(keep_) : "v"(ko0), "v"(ko1), "v"(ko2), "v"(ko3), "s"(kb), "s"(ldst + (lo)) : "memory"); } while (0)
#define PTR(v, i) (((u64)(v)[2 * (i) + 1] << 32) | (v)[2 * (i)])
  Ptrs p;
  p.x = (const u16*)PTR(ka, 0); p.K = (const u16*)PTR(ka, 1); p.V = (const u16*)PTR(ka, 2); p.wq = (const u16*)PTR(ka, 3); p.wo = (const u16*)PTR(ka, 4);
  p.wg = (const u16*)PTR(ka, 5); p.wu = (const u16*)PTR(ka, 6); p.wd = (const u16*)PTR(ka, 7);
  p.c = (const u16*)PTR(kb8, 0); p.wmod = (const u16*)PTR(kb8, 1); p.out = (u16*)PTR(kb8, 2); p.ws = (void*)PTR(kb8, 3);
#define MKWS() const rsrc_t ws = mkr(p.ws, WS_BYTES)
  if (wave == 0 && lane < 16) FL[lane] = (lane == 0 && ix >= 16) ? 1u : 0u;
  if (!((ix >> 3) == 2 && (wave == 1 || wave == 3))) BARRIER();

  MKWS();
  const bool gcu = ix >= 16 && ix < 23, mcu = ix >= 23 && ix < 27;
  u32 tag = 0;
  if (__builtin_expect(wave < 8 && !ninth, 1)) {
  if (wave >= 4 && !(chain || wcu || gcu || mcu)) return;
  __builtin_amdgcn_s_setprio(3);
  asm volatile("s_waitcnt vmcnt(8)\n\ts_cmp_eq_u32 %3, 2\n\ts_cbranch_scc1 1f\n\ts_waitcnt vmcnt(4)\n\ts_cmp_eq_u32 %3, 1\n\ts_cbranch_scc1 1f\n\ts_waitcnt vmcnt(0)\n\t1:"
               : "+{v104}"(ep), "+{v105}"(d1), "+{v[106:109]}"(xs) : "s"(__builtin_amdgcn_readfirstlane((chain || mcu) ? 2 : p1 ? 1 : 0)) : "memory", "scc");
  const int cg = xc * 2 + (rb >> 2), ks = rb & 3;
  const int pi = ((n >> 2) & 1) * 16 + g * 4 + (n & 3);
  tag = ep + 1u;
  if (__builtin_expect(p1, 1)) {
    v4u a = bcast4<0>(silu2(d1)), f[4];
    int ho = H0_OFF + (cg * 4 + ks) * 512 + lane * 8;
    PIN1(a); PIN1(ho);
    VM0();
    lds_frags(L + L_TB + (wave & 3) * 4096, lane, f);
    RED[(wave & 3) * 64 + lane] = gemv<1>(&a, f, g);
    if (lds_ticket(&FL[3], lane) == 3u) {
      const float s = (RED[lane] + RED[64 + lane]) + (RED[128 + lane] + RED[192 + lane]);
      const float s_hi = dpp<SHL1>(s);
      if (!(lane & 1)) stc(ws, ho, v4u{tag, asu(s), ~tag, asu(s_hi)});
      MEMFENCE();
      if (lane == 0) FL[0] = 1u;
    }
  }
  if (__builtin_expect(chain, 1)) {
    float mb, inv;
    ln_stats(xs, mb, inv);
    f32x2 y = up2(ln_pair(d1, mb, inv));
    PIN1(y);
    VM0();
    v4u f[8];
    lds_frags(L + L_K + wave * 8192, lane, f); lds_frags(L + L_K + wave * 8192 + 4096, lane, f + 4);
    SCHED();
    v4u h0, h1, h2, h3;
    pollT4(ws, H0_OFF + (wave + (n >> 3) * 8) * 2048 + pi * 16, tag, ~tag, h0, h1, h2, h3);
    const u32 md = pk((asf(h0.y) + asf(h1.y)) + (asf(h2.y) + asf(h3.y)), (asf(h0.w) + asf(h1.w)) + (asf(h2.w) + asf(h3.w)));
    const u32 ot = dppu<ROR8>(md), sa = (n & 8) ? ot : md, ca = (n & 8) ? md : ot;
    const u32 hp = pk2(up2(pk2(y * one_plus(ca))) + up2(sa));
    const v4u a[2] = {bcast4<0>(hp), bcast4<4>(hp)};
    RED[lane * 8 + wave] = gemv<2>(a, f, g);
    if (lds_ticket(&FL[7], lane) == 7u) {
      const float s = sum8((u32)(uintptr_t)(RED + lane * 8));
      const u32 d = pk(s, dpp<SHL1>(s)), d2 = dppu<SHL2>(d);
      if ((lane & 3) == 0) stc(ws, Q_OFF + (xc * 8 + rb) * 256 + lane * 4, v4u{tag, d, ~tag, d2});
      MEMFENCE();
      if (lane == 0) FL[0] = 1u;
    }
  }
  if ((ix >> 3) == 2 && (wave == 1 || wave == 3)) {
    if (wave == 3) { if (lane == 0) FL[12] = rtt | 1u; }
    else {
      lds_wait_ge(&FL[12], 1u);
      const u32 half = (__builtin_amdgcn_s_getreg(20 | (3 << 11)) >> 2) & 1u;
      const u32 vote = (rtt > FL[12] ? 1u : 0u) ^ half;
      if (lane == 0) stc2(ws, VOTE_OFF + (xc * 8 + rb) * 16, v2u{tag, ~tag ^ (vote | (half << 1))});
    }
  }
  if (mcu) {
    const u32 sp = silu2(d1);
    const v4u a[2] = {bcast4<0>(sp), bcast4<4>(sp)};
    VM0();
    v4u f[8];
    lds_frags(L + 65536 + wave * 8192, lane, f); lds_frags(L + 65536 + wave * 8192 + 4096, lane, f + 4);
    RED[lane * 8 + wave] = gemv<2>(a, f, g);
    MEMFENCE();
    lds_inc(&FL[3], lane);
    if (wave == 1) {
      lds_wait_ge(&FL[3], 8u);
      const float s = sum8((u32)(uintptr_t)(RED + lane * 8));
      const u32 d = nosent(pk(s, dpp<SHL1>(s)));
      lds_wait_ge(&FL[5], 1u);
      if (!(lane & 1)) stc1(ws, MR_OFF + (xc * 4 + (ix - 23)) * 128 + lane * 2, d);
    }
  }
  if (wcu && wave == 1) {
    lds_wait_ge(&FL[0], 1u);
    const v4u sv = {SENT, SENT, SENT, SENT};
#pragma unroll
    for (int j = 0; j < CLR_KB; ++j) stc(ws, S_OFF + (xc * 8 + rb) * (CLR_KB * 1024) + j * 1024 + lane * 16, sv);
    VM0();
    if (lane == 0) stc2(ws, CF_OFF + (xc * 8 + rb) * 16, v2u{tag, ~tag});
  }
  if (wave == 0) {
    const int qb = Q_OFF + xc * 2048 + lane * 16;
    if (!chain && !mcu) __builtin_amdgcn_s_sleep(100);
    _Pragma("nounroll") for (u32 s = 0; s < SPIN_CAP; ++s) {
      const v4u q0 = ldc(ws, qb), q1 = ldc(ws, qb + 1024);
      SCHED();
      if (__all((((q0.x ^ tag) | (q1.x ^ tag)) | ((q0.z ^ ~tag) | (q1.z ^ ~tag))) == 0u)) {
        LAS unsigned char* qd = L + L_Q + (lane >> 5) * 320 + (lane & 31) * 8;
        *(LAS v2u*)qd = v2u{q0.y, q0.w};
        *(LAS v2u*)(qd + 640) = v2u{q1.y, q1.w};
        LGKM0();
        if (lane == 0) FL[1] = 1u;
        break;
      }
      MEMFENCE();
    }
  }
  if (wave == 3) {
    {
      u32 pv = 0;
      __builtin_amdgcn_s_sleep(100);
      _Pragma("nounroll") for (u32 s = 0; s < SPIN_CAP; ++s) {
        const v2u cf = __builtin_amdgcn_raw_buffer_load_b64(ws, CF_OFF + lane * 16, 0, SC1), vt = __builtin_amdgcn_raw_buffer_load_b64(ws, VOTE_OFF + lane * 16, 0, SC1);
        SCHED();
        pv = vt.y ^ ~tag;
        if (__all(((cf.x ^ tag) | (cf.y ^ ~tag) | (vt.x ^ tag) | (pv & ~3u)) == 0u)) break;
        __builtin_amdgcn_s_sleep(16);
        MEMFENCE();
      }
      const u32 yes = __builtin_popcountll(__ballot(pv & 1u));
      const u32 hv = (u32)__ballot(pv & 2u);
      const u32 par = ((hv & 1u) | ((hv >> 7) & 2u) | ((hv >> 14) & 4u) | ((hv >> 21) & 8u)) ^ (yes > 32u ? 15u : 0u);
      if (lane == 0) FL[5] = 1u | (par << 1);
    }
  }
  }
  else {
    const bool early = aw < 4;
    if (ninth) {
      VM0();
      const u32 kv0 = g * 256 + ((n ^ g) << 4);
      ko0 = kv0; ko1 = kv0 ^ 64u; ko2 = kv0 ^ 128u; ko3 = kv0 ^ 192u;
    }
    if (aw >= ntask) return;
    if (chain) lds_wait_ge(&FL[0], 1u);
    if (!early) {
      lds_wait_ge(&FL[3], (ix >= 23 && ix < 27) ? 8u : (wcu && ninth) ? 4u : 0u);
      _Pragma("nounroll") for (u32 s = 0; s < (SPIN_CAP << 3); ++s) { if (FL[2] >= 4u || FL[1] != 0u) break; __builtin_amdgcn_s_sleep(1); }
      MEMFENCE();
    }
    const int hh = n >> 2, e2 = n & 3;
    u32 qseen = (!early && ix >= 16) ? 1u : 0u;
    const u32 key0 = (u32)(xc * 16384 + toff * 32 + aw * 64);
    const bool halft = 2 * aw + 1 == nhalf;
#define THROTTLE() do { if (!qseen) { const v2u fl_ = *(volatile LAS v2u*)FL; qseen = __builtin_amdgcn_readfirstlane(fl_.y); \
      if (!qseen) { if (__builtin_amdgcn_readfirstlane(fl_.x)) asm volatile("s_waitcnt vmcnt(%0)" :: "i"(THR_W - 4) : "memory"); else asm volatile("s_waitcnt vmcnt(%0)" :: "i"(THR_Q - 4) : "memory"); } } SCHED(); } while (0)
    const u64 kb = (u64)p.K + (u64)key0 * 256;
    if (!head) KGROUP(kb, 0);
    THROTTLE(); KGROUP(kb + 4096, 4096);
    THROTTLE();
    if (!halft) { KGROUP(kb + 8192, 8192); THROTTLE(); KGROUP(kb + 12288, 12288); THROTTLE(); }
    v4u vv[16];
    {
      const rsrc_t rv = mkr(p.V, 8 * 16384 * 256);
      const int vo = (halft && g >= 2) ? 0x40000000 : g * 4096 + n * 16;
#pragma unroll
      for (int i = 0; i < 16; ++i) { vv[i] = ld4<NT>(rv, vo + i * 256, key0 * 256); if ((i & 3) == 3 && i < 15) THROTTLE(); }
    }
    SCHED();
    lds_inc(&FL[2], lane);
    lds_wait_ge(&FL[1], 1u);
    asm volatile("s_waitcnt vmcnt(16)" ::: "memory");
    SCHED();
    float s2[4], pe[4];
    {
      const LAS unsigned char* kbase = L + L_K + aw * 16384 + n * 256;
      const LAS unsigned char* qa = L + L_Q + (n & 3) * 320 + g * 16;
      f32x4 sa[4];
#pragma unroll
      for (int t = 0; t < 4; ++t) sa[t] = f32x4{0.f, 0.f, 0.f, 0.f};
#pragma unroll
      for (int s = 0; s < 4; ++s) {
        const v4u a = *(const LAS v4u*)(qa + 64 * s);
#pragma unroll
        for (int t = 0; t < 4; ++t) sa[t] = mfma32(a, *(const LAS v4u*)(kbase + t * 4096 + (((4 * s + g) ^ n) << 4)), sa[t]);
      }
      const f32x4 lo = (g & 1) ? sa[1] : sa[0], hi = (g & 1) ? sa[3] : sa[2], sacc = (g & 2) ? hi : lo;
      constexpr float SC = 0.08838834764831845f;
      const f32x2 c = up2(pk2(up2(pk(sacc[0], sacc[1])) * SC)), d = up2(pk2(up2(pk(sacc[2], sacc[3])) * SC));
      const bool dead = halft && g >= 2;
      s2[0] = dead ? -__builtin_inff() : c.x; s2[1] = dead ? -__builtin_inff() : c.y; s2[2] = dead ? -__builtin_inff() : d.x; s2[3] = dead ? -__builtin_inff() : d.y;
    }
    const float mrow = row_max(tm16(tm32(s2[0], s2[2]), tm32(s2[1], s2[3])));
#pragma unroll
    for (int h2 = 0; h2 < 4; ++h2) pe[h2] = __builtin_amdgcn_exp2f((s2[h2] - rdlf(mrow, 16 * h2)) * LOG2E);
    const u32 w0 = pk(pe[0], pe[1]), w1 = pk(pe[2], pe[3]);
    const float lrow = row_sum(tr16(tr32(pe[0], pe[2]), tr32(pe[1], pe[3])));
    if (n == 0) *(LAS f32x2*)(L + L_ML + g * 128 + aw * 8) = f32x2{mrow, lrow};
    MEMFENCE();
    lds_inc(&FL[4], lane);
    const u32 two = (u32)(2 * hh) | ((u32)(2 * hh + 1) << 8);
    const u32 selv = (e2 & 1) ? ((two << 16) | 0x0C0Cu) : (0x0C0C0000u | two);
    const u32 sel0 = (e2 >> 1) == 0 ? selv : 0x0C0C0C0Cu, sel1 = (e2 >> 1) == 1 ? selv : 0x0C0C0C0Cu;
    v2u xb[16];
#define PV_PREP(i) { const u32 b0_ = dppu<BC + (i)>(w0), b1_ = dppu<BC + (i)>(w1); xb[i] = v2u{__builtin_amdgcn_perm(b1_, b0_, sel0), __builtin_amdgcn_perm(b1_, b0_, sel1)}; }
    PV_PREP(0) PV_PREP(1) PV_PREP(2) PV_PREP(3) PV_PREP(4) PV_PREP(5) PV_PREP(6) PV_PREP(7)
    PV_PREP(8) PV_PREP(9) PV_PREP(10) PV_PREP(11) PV_PREP(12) PV_PREP(13) PV_PREP(14) PV_PREP(15)
#undef PV_PREP
    u32 obw = (u32)(uintptr_t)L + L_K + aw * 16384 + (hh * 128 + e2 * 16 + 4 * g) * 4;
    PIN1(obw);
    SCHED();
    f32x4 o1 = {0.f, 0.f, 0.f, 0.f}, o2 = {0.f, 0.f, 0.f, 0.f};
#pragma unroll
    for (int i = 0; i < 16; ++i) {
      o1 = __builtin_amdgcn_mfma_f32_16x16x16bf16_1k(__builtin_bit_cast(s16x4, v2u{vv[i].x, vv[i].y}), __builtin_bit_cast(s16x4, xb[i]), o1, 0, 0, 0);
      o2 = __builtin_amdgcn_mfma_f32_16x16x16bf16_1k(__builtin_bit_cast(s16x4, v2u{vv[i].z, vv[i].w}), __builtin_bit_cast(s16x4, xb[i]), o2, 0, 0, 0);
    }
    *(LAS f32x4*)(uintptr_t)obw = o1; *(LAS f32x4*)(uintptr_t)(obw + 256) = o2;
    if (aw < 2 || (aw > 3 && aw < 8) || (ninth && !gcu)) { BARRIER(); return; }
    if (!ninth) {
    const int tau = (aw - 2) * 64 + lane, h = tau >> 5;
    float f[9];
    lds_wait_ge(&FL[5], 1u);
    const int par = __builtin_amdgcn_readfirstlane(FL[5]) >> 1;
    lds_wait_ge(&FL[4], (u32)ntask);
    {
      float m[10], l[10];
#pragma unroll
      for (int w = 0; w < 5; ++w) { const f32x4 t = *(const LAS f32x4*)(L + L_ML + h * 128 + w * 16); m[2 * w] = t.x; l[2 * w] = t.y; m[2 * w + 1] = t.z; l[2 * w + 1] = t.w; }
#pragma unroll
      for (int w = 4; w < 9; ++w) { m[w] = w < ntask ? m[w] : -__builtin_inff(); l[w] = w < ntask ? l[w] : 0.f; }
      const float M = mx(mx(mx(mx(m[0], m[1]), mx(m[2], m[3])), mx(mx(m[4], m[5]), mx(m[6], m[7]))), m[8]);
      float Ls = 0.f;
#pragma unroll
      for (int w = 0; w < 9; ++w) { f[w] = __builtin_amdgcn_exp2f((m[w] - M) * LOG2E); Ls = fmaf(l[w], f[w], Ls); PIN1(f[w]); }
      if ((lane & 31) == 0) stc2(ws, ml_at(xc, par) + (ix * 2 + aw - 2) * 128 + (lane >> 5) * 8, v2u{asu(M), asu(Ls)});
    }
    int t1o = t1_at(xc, par, ((h * 4 + ((tau >> 3) & 3)) * 32 + ix) * 128 + (tau & 7) * 16);
    u32 pa[9];
#pragma unroll
    for (int w = 0; w < 9; ++w) { pa[w] = (u32)(uintptr_t)(L + L_K) + tau * 16 + (w < ntask ? w : 0) * 16384; PIN1(pa[w]); }
    PIN1(t1o);
    __builtin_amdgcn_s_setprio(3);
    BARRIER();
    f32x4 part[9];
#pragma unroll
    for (int w = 0; w < 9; ++w) part[w] = *(const LAS f32x4*)(uintptr_t)pa[w];
    const f32x4 acc = (((part[0] * f[0] + part[1] * f[1]) + (part[2] * f[2] + part[3] * f[3])) + ((part[4] * f[4] + part[5] * f[5]) + (part[6] * f[6] + part[7] * f[7]))) + part[8] * f[8];
    stc(ws, t1o, __builtin_bit_cast(v4u, acc));
    lds_inc(&FL[8], lane);
    return;
    }
    __builtin_amdgcn_s_setprio(3);
  }

  LAS unsigned char* tb = L + L_K + wave * 16384 + 8192;
  if (ix < 16) {
    if (wave < 4) return;
    const int x = wave & 3, hl = ix >> 2, hg = xc * 4 + hl, qd = ix & 3;
    const rsrc_t ro = mkr(p.wo, 4096 * 512 * 2);
    const int col = xc * 64 + rb * 8;
    lds_wait_ge(&FL[2], (u32)ntask);
    v4u w[8];
    {
      const int rho = lane >> 3, vo = (64 * (rho >> 2) + 8 * (rho & 3)) * 1024 + (lane & 7) * 16;
#pragma unroll
      for (int i = 0; i < 8; ++i) w[i] = ld4<NT>(ro, vo, (hg * 128 + 32 * ((i >> 1) & 1) + 2 * qd + (i & 1)) * 1024 + x * 256 + (i >> 2) * 128);
    }
    BARRIER();
    lds_wait_ge(&FL[8], 2u);
    const int par = __builtin_amdgcn_readfirstlane(FL[5]) >> 1;
    const v2u ma = poll2(ws, ml_at(xc, par) + ((lane >> 1) * 2 + (hl >> 1)) * 128 + (hl & 1) * 8);
    v4u f[8];
    tile_frags(tb, lane, w, f); tile_frags(tb, lane, w + 4, f + 4);
    int ob = t1_at(xc, par, ((hl * 4 + qd) * 32 + (lane >> 1)) * 128 + x * 32 + (lane & 1) * 16);
    int t2o = T2_OFF + row_at(xc * 16 + ix, par, x * 512 + lane * 4), t2p = T2_OFF + row_at(xc * 16 + ix, par, x * 512 + 256 + lane * 4);
    float M = asf(ma.x);
    M = mx(M, dpp<XOR2>(M)); M = mx(M, dpp<ROR4>(M)); M = mx(M, dpp<ROR8>(M)); M = tm16(M, M); M = tm32(M, M);
    float fa = __builtin_amdgcn_exp2f((asf(ma.x) - M) * LOG2E);
    float Ls = fa * asf(ma.y);
    Ls = tr32(Ls, Ls); Ls = tr16(Ls, Ls); Ls += dpp<ROR8>(Ls); Ls += dpp<ROR4>(Ls); Ls += dpp<XOR2>(Ls);
    float inv = __builtin_amdgcn_rcpf(Ls);
    u32 oa = (u32)(uintptr_t)(L + L_X) + x * 16 + (lane & 1) * 8 + (lane >> 5) * 4;
    u32 oa2 = (u32)(uintptr_t)(L + L_X) + g * 16;
    fa *= inv;
    PIN1(fa); PIN1(ob); PIN1(t2o); PIN1(t2p); PIN1(oa); PIN1(oa2);
    SCHED();
    const v4u a = poll4(ws, ob);
    float u0 = tr32(fa * asf(a.x), fa * asf(a.z)), u1 = tr32(fa * asf(a.y), fa * asf(a.w));
    u0 = tr16(u0, u0); u1 = tr16(u1, u1);
    u0 += dpp<ROR8>(u0); u1 += dpp<ROR8>(u1);
    u0 += dpp<ROR4>(u0); u1 += dpp<ROR4>(u1);
    u0 += dpp<XOR2>(u0); u1 += dpp<XOR2>(u1);
    if (!(lane & 30)) *(LAS u32*)(uintptr_t)oa = pk(u0, u1);
    BARRIER();
    const v4u av = *(const LAS v4u*)(uintptr_t)oa2;
    const float y0 = gemv<1>(&av, f, g), y1 = gemv<1>(&av, f + 4, g);
    stc_row(ws, t2o, y0, lane); stc_row(ws, t2p, y1, lane);
    if (!chain || wave != 4) return;
    f32x2 xin = up2(*(const GAS u32*)((const GAS u16*)p.x + col + 2 * g));
    u32 gaw, gbw;
    poll1x2<3072>(ws, MR_OFF + (col + 2 * g) * 2, gaw, gbw);
    f32x2 ga = up2(gaw), gb = up2(gbw);
    int tio = T2_OFF + row_at(lane, par, col * 4), tio2 = tio + 131072, yo = lane < 56 ? Y_OFF + row_at(lane, par, col * 4) : 0x40000000;
    PIN1(ga); PIN1(gb); PIN1(xin); PIN1(tio); PIN1(tio2); PIN1(yo);
    v4u pa, pb, pc, pd;
    poll4x4(ws, tio, tio2, pa, pb, pc, pd);
    pa = __builtin_bit_cast(v4u, __builtin_bit_cast(f32x4, pa) + __builtin_bit_cast(f32x4, pc));
    pb = __builtin_bit_cast(v4u, __builtin_bit_cast(f32x4, pb) + __builtin_bit_cast(f32x4, pd));
    const u32 x1 = nosent(pk2(xin + up2(pk2(ga * up2(pk2(sum8x64(pa, pb)))))));
    const v4u o = {rdl(x1, 0), rdl(x1, 16), rdl(x1, 32), rdl(x1, 48)};
    if (lane < 2) stc(ws, X1_OFF + lane * 8192 + col * 16, o);
    if (bid == 0 && lane == 0) stc1(ws, EPOCH_OFF, tag);
    f32x2 x1f = up2(x1);
    PIN1(x1f);
    poll4x2<16>(ws, yo, pa, pb);
    const u32 d = pk2(x1f + up2(pk2(gb * up2(pk2(sum8x64(pa, pb))))));
    if (!n) *(GAS u32*)((GAS u16*)p.out + col + 2 * g) = d;
  } else if (gcu) {
    const int gi = xc * 7 + (ix - 16);
    const rsrc_t rd = mkr(p.wd, 1792 * 512 * 2);
    BARRIER();
    lds_wait_ge(&FL[8], 2u);
    const int par = __builtin_amdgcn_readfirstlane(FL[5]) >> 1;
    v4u w[12];
    {
      const int rho = lane >> 3;
      const GAS unsigned char* src = (const GAS unsigned char*)((lane & 4) ? p.wu : p.wg) + (size_t)(wave * 64 + 8 * (rho >> 2) + (rho & 3)) * 3584 + gi * 64 + (lane & 3) * 16;
#pragma unroll
      for (int i = 0; i < 8; ++i) w[i] = *(const GAS v4u*)(src + (32 * (i >> 2) + 16 * (i & 1) + 4 * ((i >> 1) & 1)) * 3584);
    }
    tile_load<NT>(rd, tile_voff(lane, 1024), gi * 32 * 1024 + wave * 128, 1024, w + 8);
    v4u f[12];
    tile_frags(tb, lane, w, f); tile_frags(tb, lane, w + 4, f + 4); tile_frags(tb, lane, w + 8, f + 8);
    int yo = Y_OFF + row_at(gi, par, wave * 256 + lane * 4);
    u32 aa = (u32)(uintptr_t)(L + L_X) + wave * 128 + g * 16, ra = (u32)(uintptr_t)(RED + lane * 8 + wave), sa = (u32)(uintptr_t)(RED + ((n >> 3) * 32 + g * 8 + (n & 7)) * 8);
    u32 ha = (u32)(uintptr_t)(L + L_X) + lane * 16 + wave * 4;
    PIN1(yo); PIN1(aa); PIN1(ra); PIN1(sa); PIN1(ha);
    SCHED();
    if (wave < 4) {
#define SEL4(v) ((wave & 2) ? ((wave & 1) ? (v).w : (v).z) : ((wave & 1) ? (v).y : (v).x))
      v4u sbw, cbw;
      poll4x2<1024>(ws, MR_OFF + 1024 + lane * 16, sbw, cbw);
      f32x2 sb = up2(SEL4(sbw)), c1 = one_plus(SEL4(cbw));
      int xb1 = X1_OFF + par_of(par, xc) + lane * 128;
      PIN1(sb); PIN1(c1); PIN1(xb1);
      const v4u x1v = poll4(ws, xb1);
      float mb, inv;
      ln_stats(x1v, mb, inv);
      *(LAS u32*)(uintptr_t)ha = modulate(ln_pair(SEL4(x1v), mb, inv), c1, sb);
    }
    BARRIER();
    const v4u av[2] = {*(const LAS v4u*)(uintptr_t)aa, *(const LAS v4u*)(uintptr_t)(aa + 64)};
    *(LAS float*)(uintptr_t)ra = gemv<2>(av, f, g);
    BARRIER();
    const float s = rbf(sum8(sa));
    const float gt = s, up = dpp<ROR8>(s);
    const float u2 = (-2.f * 0.7978845608028654f * LOG2E) * (gt + 0.044715f * gt * gt * gt);
    const float gv = rbf(gt * __builtin_amdgcn_rcpf(1.f + __builtin_amdgcn_exp2f(u2))) * up;
    const u32 d = pk(gv, dpp<SHL1>(gv));
    const v4u ag = {dppu<BC + 0>(d), dppu<BC + 2>(d), dppu<BC + 4>(d), dppu<BC + 6>(d)};
    stc_row(ws, yo, gemv<1>(&ag, f + 8, g), lane);
  }
}
}

extern "C" void kernel_launch(void* const* d_in, const int* in_sizes, int n_in, void* d_out, int out_size, void* d_ws, size_t ws_size, hipStream_t stream) {
  Params a;
  Ptrs& p = a.p;
  p.x = (const u16*)d_in[0]; p.K = (const u16*)d_in[1]; p.V = (const u16*)d_in[2]; p.wq = (const u16*)d_in[3]; p.wo = (const u16*)d_in[4];
  p.wg = (const u16*)d_in[6]; p.wu = (const u16*)d_in[7]; p.wd = (const u16*)d_in[8]; p.c = (const u16*)d_in[9]; p.wmod = (const u16*)d_in[10];
  p.out = (u16*)d_out; p.ws = d_ws;
  for (int i = 0; i < 32; ++i) a.tab[i] = (u32)toff_of(i) | ((u32)nhalf_of(i) << 16);
  hipLaunchKernelGGL(fused_kernel, dim3(NWG), dim3(NTHR), 0, stream, a);
}
```

```cpp
#include <hip/hip_runtime.h>
#include <stdint.h>

namespace {
typedef unsigned short u16;
typedef unsigned u32;
typedef unsigned long long u64;
typedef u32 v4u __attribute__((ext_vector_type(4)));
typedef u32 v2u __attribute__((ext_vector_type(2)));
typedef u32 v8u __attribute__((ext_vector_type(8)));
typedef u32 v16u __attribute__((ext_vector_type(16)));
typedef float f32x4 __attribute__((ext_vector_type(4)));
typedef float f32x2 __attribute__((ext_vector_type(2)));
typedef __bf16 bf16x2 __attribute__((ext_vector_type(2)));
typedef __bf16 bf16x8 __attribute__((ext_vector_type(8)));
typedef short s16x4 __attribute__((ext_vector_type(4)));
#define GAS __attribute__((address_space(1)))
#define LAS __attribute__((address_space(3)))

#define KV_SFX " nt"
constexpr int NT = 2;
constexpr int THR_W = 16;
constexpr int THR_Q = 6;
constexpr int H_W = 17, H_G = 17, H_M = 16, H_P = 17;

constexpr int NWG = 256, NTHR = 1024;
constexpr u32 SENT = 0xFFFFFFFFu;
constexpr int SC1 = 16;
constexpr u32 SPIN_CAP = 1u << 17;
constexpr float LOG2E = 1.4426950408889634f;
constexpr int EPOCH_OFF = 0;
constexpr int CF_OFF = 8192;
constexpr int Q_OFF = 16384;
constexpr int H0_OFF = 32768;
constexpr int S_OFF = 65536;
constexpr int VOTE_OFF = CF_OFF + 1024;
constexpr int T1_OFF = S_OFF;
constexpr int ML_OFF = T1_OFF + 524288;
constexpr int T2_OFF = ML_OFF + 65536;
constexpr int Y_OFF = T2_OFF + 262144;
constexpr int X1_OFF = Y_OFF + 114688;
constexpr int MR_OFF = X1_OFF + 16384;
__device__ __forceinline__ int par_of(int par, int x) { return (((par >> (x & 3)) ^ (x >> 2)) & 1) << 13; }
__device__ __forceinline__ int t1_at(int xc, int par, int off) { return T1_OFF + (xc & 3) * 131072 + (off >> 13) * 16384 + par_of(par, xc) + (off & 8191); }
__device__ __forceinline__ int ml_at(int xc, int par) { return ML_OFF + (xc & 3) * 16384 + par_of(par, xc); }
__device__ __forceinline__ int row_at(int prod, int par, int colbyte) { return (prod >> 3) * 16384 + par_of(par, colbyte >> 8) + (prod & 7) * 1024 + (colbyte & 1023); }
constexpr int CLR_KB = 16;
constexpr int S_END = S_OFF + 64 * CLR_KB * 1024;
static_assert(MR_OFF + 4096 <= S_END && S_OFF % 16384 == 0 && ML_OFF % 16384 == 0 && T2_OFF % 16384 == 0 && Y_OFF % 16384 == 0 && X1_OFF % 16384 == 0, "");
constexpr int PROBE_OFF = S_END, WS_BYTES = S_END + 16384;
constexpr int L_K = 0, L_TB = 8 * 16384, L_Q = 9 * 16384, L_FLAG = L_Q + 1280, L_ML = L_FLAG + 64, L_X = L_ML + 512, L_RED = L_X + 1024, L_END = L_RED + 4096;
static_assert(L_END <= 163840 && L_RED % 16 == 0 && L_TB % 16 == 0, "");
constexpr int CH_ALL = 512 - 8 * H_W - 7 * H_G - 4 * H_M - 5 * H_P, H_CH = CH_ALL / 8, CH_HI = CH_ALL - 8 * H_CH;
constexpr int nhalf_of(int ix) { return ix < 8 ? (ix < CH_HI ? H_CH + 1 : H_CH) : ix < 16 ? H_W : ix < 23 ? H_G : ix < 27 ? H_M : H_P; }
constexpr int toff_of(int ix) { int o = 0; for (int i = 0; i < ix; ++i) o += nhalf_of(i); return o; }
static_assert(toff_of(32) == 512 && H_G >= 8 && H_G <= 18 && H_CH >= 8 && H_CH + 1 <= 16 && H_M >= 8 && H_M <= 16 && H_W >= 8 && H_W <= 18 && H_P >= 8 && H_P <= 18, "");

struct Ptrs {
  const u16 *x, *K, *V, *wq, *wo, *wg, *wu, *wd, *c, *wmod;
  u16* out;
  void* ws;
};
struct Params { Ptrs p; u32 tab[32]; };
static_assert(sizeof(Params) == 96 + 128 && L_K == 0, "");

__device__ __forceinline__ float asf(u32 x) { return __builtin_bit_cast(float, x); }
__device__ __forceinline__ u32 asu(float x) { return __builtin_bit_cast(u32, x); }
__device__ __forceinline__ float blo(u32 w) { return asf(w << 16); }
__device__ __forceinline__ float bhi(u32 w) { return asf(w & 0xffff0000u); }
__device__ __forceinline__ u32 pk2(f32x2 v) { return __builtin_bit_cast(u32, __builtin_convertvector(v, bf16x2)); }
__device__ __forceinline__ u32 pk(float lo, float hi) { return pk2(f32x2{lo, hi}); }
__device__ __forceinline__ f32x2 up2(u32 w) { return f32x2{blo(w), bhi(w)}; }
__device__ __forceinline__ float rbf(float f) { return (float)(__bf16)f; }
__device__ __forceinline__ float dot2(u32 a, u32 b, float c) { return __builtin_amdgcn_fdot2_f32_bf16(__builtin_bit_cast(bf16x2, a), __builtin_bit_cast(bf16x2, b), c, false); }
template <int CTRL> __device__ __forceinline__ float dpp(float x) { return asf(__builtin_amdgcn_mov_dpp(asu(x), CTRL, 0xf, 0xf, true)); }
template <int CTRL> __device__ __forceinline__ u32 dppu(u32 x) { return __builtin_amdgcn_mov_dpp(x, CTRL, 0xf, 0xf, true); }
constexpr int XOR1 = 0xB1, XOR2 = 0x4E, XOR7 = 0x141, ROR8 = 0x128, ROR4 = 0x124, SHL1 = 0x101, SHL2 = 0x102, BC = 0x150;
__device__ __forceinline__ float mx(float a, float b) { return __builtin_amdgcn_fmed3f(a, b, __builtin_inff()); }
__device__ __forceinline__ float tr32(float a, float b) { auto s = __builtin_amdgcn_permlane32_swap(asu(a), asu(b), false, false); return asf(s[0]) + asf(s[1]); }
__device__ __forceinline__ float tr16(float a, float b) { auto s = __builtin_amdgcn_permlane16_swap(asu(a), asu(b), false, false); return asf(s[0]) + asf(s[1]); }
__device__ __forceinline__ float tm32(float a, float b) { auto s = __builtin_amdgcn_permlane32_swap(asu(a), asu(b), false, false); return mx(asf(s[0]), asf(s[1])); }
__device__ __forceinline__ float tm16(float a, float b) { auto s = __builtin_amdgcn_permlane16_swap(asu(a), asu(b), false, false); return mx(asf(s[0]), asf(s[1])); }
__device__ __forceinline__ float row_sum(float v) { v += dpp<ROR8>(v); v += dpp<XOR7>(v); v += dpp<XOR2>(v); v += dpp<XOR1>(v); return v; }
__device__ __forceinline__ float row_max(float v) { v = mx(v, dpp<ROR8>(v)); v = mx(v, dpp<XOR7>(v)); v = mx(v, dpp<XOR2>(v)); v = mx(v, dpp<XOR1>(v)); return v; }
__device__ __forceinline__ f32x2 sum8x64(v4u a, v4u b) {
  const float y0 = tr32(asf(a.x), asf(b.x)), y1 = tr32(asf(a.y), asf(b.y)), y2 = tr32(asf(a.z), asf(b.z)), y3 = tr32(asf(a.w), asf(b.w));
  return f32x2{row_sum(tr16(y0, y2)), row_sum(tr16(y1, y3))};
}
__device__ __forceinline__ u32 rdl(u32 v, int l) { return __builtin_amdgcn_readlane(v, l); }
__device__ __forceinline__ float rdlf(float v, int l) { return asf(__builtin_amdgcn_readlane(asu(v), l)); }
__device__ __forceinline__ u32 nosent(u32 w) { return w < 0xFFFFFFFEu ? w : 0xFFFFFFFEu; }
typedef __amdgpu_buffer_rsrc_t rsrc_t;
__device__ __forceinline__ rsrc_t mkr(const void* q, int bytes) { return __builtin_amdgcn_make_buffer_rsrc((void*)q, 0, bytes, 0x00020000); }
template <int AUX> __device__ __forceinline__ v4u ld4(rsrc_t r, int off, int so) { return __builtin_amdgcn_raw_buffer_load_b128(r, off, so, AUX); }
__device__ __forceinline__ v4u ldc(rsrc_t r, int off) { return __builtin_amdgcn_raw_buffer_load_b128(r, off, 0, SC1); }
__device__ __forceinline__ void stc(rsrc_t r, int off, v4u v) { __builtin_amdgcn_raw_buffer_store_b128(v, r, off, 0, SC1); }
__device__ __forceinline__ void stc2(rsrc_t r, int off, v2u v) { __builtin_amdgcn_raw_buffer_store_b64(v, r, off, 0, SC1); }
__device__ __forceinline__ void stc_row(rsrc_t r, int off, float v, int lane) {
  const v4u o = {asu(v), asu(dpp<0x55>(v)), asu(dpp<0xAA>(v)), asu(dpp<0xFF>(v))};
  if (!(lane & 3)) stc(r, off, o);
}
__device__ __forceinline__ void stc1(rsrc_t r, int off, u32 v) { __builtin_amdgcn_raw_buffer_store_b32(v, r, off, 0, SC1); }
#define MEMFENCE() asm volatile("" ::: "memory")
#define LGKM0() asm volatile("s_waitcnt lgkmcnt(0)" ::: "memory")
#define VM0() asm volatile("s_waitcnt vmcnt(0)" ::: "memory")
#define SCHED() __builtin_amdgcn_sched_barrier(0)
#define BARRIER() do { LGKM0(); SCHED(); __builtin_amdgcn_s_barrier(); MEMFENCE(); SCHED(); } while (0)
#define PIN1(a) asm volatile("" : "+v"(a))
#define PIN4(a) asm volatile("" : "+v"((a)[0]), "+v"((a)[1]), "+v"((a)[2]), "+v"((a)[3]))

#define POLL_HEAD "s_mov_b32 %[n], %[cap]\n\t1:\n\t"
#define POLL_TAIL "s_cbranch_execz 2f\n\ts_sub_u32 %[n], %[n], 1\n\ts_cbranch_scc0 1b\n\t2:\n\ts_mov_b64 exec, -1\n\ts_nop 2"
__device__ __forceinline__ v4u poll4(rsrc_t r, int off) {
  v4u a; u32 n;
  asm volatile(POLL_HEAD "buffer_load_dwordx4 v[124:127], %[o], %[r], 0 offen sc1\n\ts_waitcnt vmcnt(0)\n\t"
               "v_max_u32 v123, v124, v125\n\tv_max3_u32 v123, v123, v126, v127\n\tv_cmpx_eq_u32 vcc, -1, v123\n\t" POLL_TAIL
               : "=&{v[124:127]}"(a), [n] "=&s"(n) : [o] "v"(off), [r] "s"(r), [cap] "i"(SPIN_CAP) : "memory", "vcc", "scc", "v123");
  return a;
}
template <int D> __device__ __forceinline__ void poll4x2(rsrc_t r, int off, v4u& a, v4u& b) {
  u32 n;
  asm volatile(POLL_HEAD "buffer_load_dwordx4 v[120:123], %[o], %[r], 0 offen sc1\n\tbuffer_load_dwordx4 v[124:127], %[o], %[r], 0 offen offset:%[d] sc1\n\ts_waitcnt vmcnt(1)\n\t"
               "v_max_u32 v119, v120, v121\n\tv_max3_u32 v119, v119, v122, v123\n\ts_waitcnt vmcnt(0)\n\tv_max3_u32 v119, v119, v124, v125\n\tv_max3_u32 v119, v119, v126, v127\n\t"
               "v_cmpx_eq_u32 vcc, -1, v119\n\t" POLL_TAIL
               : "=&{v[120:123]}"(a), "=&{v[124:127]}"(b), [n] "=&s"(n) : [o] "v"(off), [r] "s"(r), [cap] "i"(SPIN_CAP), [d] "i"(D) : "memory", "vcc", "scc", "v119");
}
__device__ __forceinline__ void poll4x4(rsrc_t r, int off, int off2, v4u& a, v4u& b, v4u& c, v4u& d) {
  u32 n;
  asm volatile(POLL_HEAD "buffer_load_dwordx4 v[112:115], %[o], %[r], 0 offen sc1\n\tbuffer_load_dwordx4 v[116:119], %[o], %[r], 0 offen offset:16 sc1\n\t"
               "buffer_load_dwordx4 v[120:123], %[o2], %[r], 0 offen sc1\n\tbuffer_load_dwordx4 v[124:127], %[o2], %[r], 0 offen offset:16 sc1\n\t"
               "s_waitcnt vmcnt(3)\n\tv_max_u32 v110, v112, v113\n\tv_max3_u32 v110, v110, v114, v115\n\t"
               "s_waitcnt vmcnt(2)\n\tv_max_u32 v111, v116, v117\n\tv_max3_u32 v111, v111, v118, v119\n\t"
               "s_waitcnt vmcnt(1)\n\tv_max3_u32 v110, v110, v120, v121\n\tv_max3_u32 v111, v111, v122, v123\n\t"
               "s_waitcnt vmcnt(0)\n\tv_max3_u32 v110, v110, v124, v125\n\tv_max3_u32 v111, v111, v126, v127\n\t"
               "v_max_u32 v110, v110, v111\n\tv_cmpx_eq_u32 vcc, -1, v110\n\t" POLL_TAIL
               : "=&{v[112:115]}"(a), "=&{v[116:119]}"(b), "=&{v[120:123]}"(c), "=&{v[124:127]}"(d), [n] "=&s"(n)
               : [o] "v"(off), [o2] "v"(off2), [r] "s"(r), [cap] "i"(SPIN_CAP) : "memory", "vcc", "scc", "v110", "v111");
}
__device__ __forceinline__ v2u poll2(rsrc_t r, int off) {
  v2u a; u32 n;
  asm volatile(POLL_HEAD "buffer_load_dwordx2 v[126:127], %[o], %[r], 0 offen sc1\n\ts_waitcnt vmcnt(0)\n\t"
               "v_max_u32 v125, v126, v127\n\tv_cmpx_eq_u32 vcc, -1, v125\n\ts_cbranch_execz 2f\n\ts_sleep 1\n\ts_sub_u32 %[n], %[n], 1\n\ts_cbranch_scc0 1b\n\t2:\n\ts_mov_b64 exec, -1\n\ts_nop 2"
               : "=&{v[126:127]}"(a), [n] "=&s"(n) : [o] "v"(off), [r] "s"(r), [cap] "i"(SPIN_CAP) : "memory", "vcc", "scc", "v125");
  return a;
}
template <int D> __device__ __forceinline__ void poll1x2(rsrc_t r, int off, u32& a, u32& b) {
  u32 n;
  asm volatile(POLL_HEAD "buffer_load_dword v126, %[o], %[r], 0 offen sc1\n\tbuffer_load_dword v127, %[o], %[r], 0 offen offset:%[d] sc1\n\ts_waitcnt vmcnt(0)\n\t"
               "v_max_u32 v125, v126, v127\n\tv_cmpx_eq_u32 vcc, -1, v125\n\t" POLL_TAIL
               : "=&{v126}"(a), "=&{v127}"(b), [n] "=&s"(n) : [o] "v"(off), [r] "s"(r), [cap] "i"(SPIN_CAP), [d] "i"(D) : "memory", "vcc", "scc", "v125");
}
__device__ __forceinline__ void pollT4(rsrc_t r, int off, u32 tag, u32 ntag, v4u& a, v4u& b, v4u& c, v4u& d) {
  u32 n;
  asm volatile(POLL_HEAD "buffer_load_dwordx4 v[112:115], %[o], %[r], 0 offen sc1\n\tbuffer_load_dwordx4 v[116:119], %[o], %[r], 0 offen offset:512 sc1\n\t"
               "buffer_load_dwordx4 v[120:123], %[o], %[r], 0 offen offset:1024 sc1\n\tbuffer_load_dwordx4 v[124:127], %[o], %[r], 0 offen offset:1536 sc1\n\t"
               "s_waitcnt vmcnt(3)\n\tv_xor_b32 v110, v112, %[t]\n\tv_xor_b32 v111, v114, %[u]\n\t"
               "s_waitcnt vmcnt(2)\n\tv_bitop3_b32 v110, v116, %[t], v110 bitop3:0xbe\n\tv_bitop3_b32 v111, v118, %[u], v111 bitop3:0xbe\n\t"
               "s_waitcnt vmcnt(1)\n\tv_bitop3_b32 v110, v120, %[t], v110 bitop3:0xbe\n\tv_bitop3_b32 v111, v122, %[u], v111 bitop3:0xbe\n\t"
               "s_waitcnt vmcnt(0)\n\tv_bitop3_b32 v110, v124, %[t], v110 bitop3:0xbe\n\tv_bitop3_b32 v111, v126, %[u], v111 bitop3:0xbe\n\t"
               "v_or_b32 v110, v110, v111\n\tv_cmpx_ne_u32 vcc, 0, v110\n\t" POLL_TAIL
               : "=&{v[112:115]}"(a), "=&{v[116:119]}"(b), "=&{v[120:123]}"(c), "=&{v[124:127]}"(d), [n] "=&s"(n)
               : [o] "v"(off), [r] "s"(r), [cap] "i"(SPIN_CAP), [t] "v"(tag), [u] "v"(ntag) : "memory", "vcc", "scc", "v110", "v111");
}
__device__ __forceinline__ void lds_wait_ge(volatile LAS u32* f, u32 want) {
  _Pragma("nounroll") for (u32 s = 0; s < (SPIN_CAP << 3); ++s) { if (*f >= want) break; __builtin_amdgcn_s_sleep(1); }
  MEMFENCE();
}
__device__ __forceinline__ void lds_inc(volatile LAS u32* f, int lane) {
  asm volatile("s_mov_b64 exec, 1\n\tds_add_u32 %0, %1\n\ts_mov_b64 exec, -1" :: "v"((u32)(uintptr_t)f), "v"(1u) : "memory");
}
__device__ __forceinline__ u32 lds_ticket(volatile LAS u32* f, int lane) {
  u32 old;
  asm volatile("s_mov_b64 exec, 1\n\tds_add_rtn_u32 %0, %1, %2\n\ts_mov_b64 exec, -1\n\ts_waitcnt lgkmcnt(0)" : "=&v"(old) : "v"((u32)(uintptr_t)f), "v"(1u) : "memory");
  return __builtin_amdgcn_readfirstlane(old);
}
__device__ __forceinline__ float sum8(u32 addr) {
  const f32x4 t = *(const LAS f32x4*)(uintptr_t)addr + *(const LAS f32x4*)(uintptr_t)(addr + 16);
  return (t.x + t.z) + (t.y + t.w);
}
__device__ __forceinline__ float silu(float v) { return v / (1.f + __expf(-v)); }
__device__ __forceinline__ u32 silu2(u32 c) { return pk(silu(blo(c)), silu(bhi(c))); }
__device__ __forceinline__ void ln_stats(v4u xv, float& mb, float& inv) {
  constexpr u32 ONE2 = 0x3f803f80u;
  const float s = dot2(xv[0], ONE2, dot2(xv[1], ONE2, dot2(xv[2], ONE2, dot2(xv[3], ONE2, 0.f))));
  const float q = dot2(xv[0], xv[0], dot2(xv[1], xv[1], dot2(xv[2], xv[2], dot2(xv[3], xv[3], 0.f))));
  float t = row_sum(tr32(s, q));
  t = tr16(t, t);
  const float mean = rdlf(t, 0) * (1.f / 512.f), sq = rdlf(t, 32) * (1.f / 512.f);
  float var = fmaf(-mean, mean, sq);
  if (__builtin_expect(!(var * 64.f >= sq), 0)) {
    float d = 0.f;
#pragma unroll
    for (int i = 0; i < 4; ++i) { const float a = blo(xv[i]) - mean, b = bhi(xv[i]) - mean; d = fmaf(a, a, fmaf(b, b, d)); }
    d = row_sum(tr32(d, d));
    var = tr16(d, d) * (1.f / 512.f);
  }
  mb = rbf(mean);
  inv = __builtin_amdgcn_rcpf(rbf(__builtin_amdgcn_sqrtf(rbf(rbf(var) + 1e-5f))));
}
__device__ __forceinline__ u32 ln_pair(u32 x, float mb, float inv) { return pk2(up2(pk2(up2(x) - mb)) * inv); }
__device__ __forceinline__ f32x2 one_plus(u32 c) { return up2(pk2(up2(c) + 1.f)); }
__device__ __forceinline__ u32 modulate(u32 y, f32x2 c1, f32x2 sh) { return pk2(up2(pk2(up2(y) * c1)) + sh); }
__device__ __forceinline__ int tile_voff(int lane, int stride) { const int rho = lane >> 3; return (8 * (rho >> 2) + (rho & 3)) * stride + (lane & 7) * 16; }
template <int AUX> __device__ __forceinline__ void tile_load(rsrc_t r, int voff, int soff, int stride, v4u* w) {
#pragma unroll
  for (int i = 0; i < 4; ++i) w[i] = ld4<AUX>(r, voff, soff + (16 * (i & 1) + 4 * (i >> 1)) * stride);
}
__device__ __forceinline__ void tile_frags(LAS unsigned char* tb, int lane, const v4u* w, v4u* f) {
  const int rho = lane >> 3;
  LAS unsigned char* wr = tb + rho * 128 + (((lane & 7) ^ (rho & 6)) << 4);
  const int row = 4 * (lane >> 4) + ((lane >> 2) & 3);
  const LAS unsigned char* rd = tb + row * 128 + (lane & 3) * 8;
  const int sw = (row >> 1) & 3;
  v2u h[2][4];
#pragma unroll
  for (int t = 0; t < 2; ++t) {
    *(LAS v4u*)wr = w[2 * t]; *(LAS v4u*)(wr + 1024) = w[2 * t + 1];
    MEMFENCE();
#pragma unroll
    for (int ct = 0; ct < 4; ++ct) h[t][ct] = __builtin_bit_cast(v2u, __builtin_amdgcn_ds_read_tr16_b64_v4i16((LAS s16x4*)(rd + ((ct ^ sw) << 5))));
    MEMFENCE();
  }
#pragma unroll
  for (int ct = 0; ct < 4; ++ct) f[ct] = v4u{h[0][ct].x, h[0][ct].y, h[1][ct].x, h[1][ct].y};
  PIN4(f);
}
__device__ __forceinline__ void lds_frags(const LAS unsigned char* tile, int lane, v4u* f) {
  const int row = 4 * (lane >> 4) + ((lane >> 2) & 3);
  const LAS unsigned char* rd = tile + row * 128 + (lane & 3) * 8;
  const int sw = (row >> 1) & 3;
#pragma unroll
  for (int ct = 0; ct < 4; ++ct) {
    const v2u lo = __builtin_bit_cast(v2u, __builtin_amdgcn_ds_read_tr16_b64_v4i16((LAS s16x4*)(rd + ((ct ^ sw) << 5))));
    const v2u hi = __builtin_bit_cast(v2u, __builtin_amdgcn_ds_read_tr16_b64_v4i16((LAS s16x4*)(rd + 2048 + ((ct ^ sw) << 5))));
    f[ct] = v4u{lo.x, lo.y, hi.x, hi.y};
  }
  PIN4(f);
}
__device__ __forceinline__ f32x4 mfma32(v4u a, v4u b, f32x4 c) { return __builtin_amdgcn_mfma_f32_16x16x32_bf16(__builtin_bit_cast(bf16x8, a), __builtin_bit_cast(bf16x8, b), c, 0, 0, 0); }
template <int NS> __device__ __forceinline__ float gemv(const v4u* a, const v4u* f, int g) {
  f32x4 acc[4];
#pragma unroll
  for (int ct = 0; ct < 4; ++ct) acc[ct] = mfma32(a[0], f[ct], f32x4{0.f, 0.f, 0.f, 0.f});
#pragma unroll
  for (int s = 1; s < NS; ++s)
#pragma unroll
    for (int ct = 0; ct < 4; ++ct) acc[ct] = mfma32(a[s], f[4 * s + ct], acc[ct]);
  PIN4(acc);
  const float lo = (g & 1) ? acc[1][0] : acc[0][0], hi = (g & 1) ? acc[3][0] : acc[2][0];
  return (g & 2) ? hi : lo;
}
template <int B> __device__ __forceinline__ v4u bcast4(u32 v) { return v4u{dppu<BC + B>(v), dppu<BC + B + 1>(v), dppu<BC + B + 2>(v), dppu<BC + B + 3>(v)}; }

__global__ void __launch_bounds__(NTHR, 4) fused_kernel(Params) {
  __shared__ __attribute__((aligned(128))) unsigned char lds_raw[L_END];
  LAS unsigned char* L = (LAS unsigned char*)lds_raw;
  v16u ka; v8u kb8;
  int wave; u32 rtt, tab, ko0, ko1, ko2, ko3, ep, d1; v4u xs;
  {
    u32 sa, sb, sc, keep, va, vb; u64 pa;
#define DMA4(P) "global_load_lds_dwordx4 %[k0], " P KV_SFX "\n\tglobal_load_lds_dwordx4 %[k1], " P " offset:1024" KV_SFX "\n\t" \
                "global_load_lds_dwordx4 %[k2], " P " offset:2048" KV_SFX "\n\tglobal_load_lds_dwordx4 %[k3], " P " offset:3072" KV_SFX "\n\t"
    asm volatile("s_load_dwordx16 s[16:31], %[kp], 0x0\n\ts_load_dwordx8 s[36:43], %[kp], 0x40\n\t"
                 "s_lshr_b32 s46, %[bid], 3\n\ts_lshl_b32 s46, s46, 2\n\ts_add_u32 s46, s46, 0x60\n\ts_load_dword s46, %[kp], s46"
                 : "=&{s[16:31]}"(ka), "=&{s[36:43]}"(kb8), "=&{s46}"(tab) : [kp] "s"(__builtin_amdgcn_kernarg_segment_ptr()), [bid] "s"(blockIdx.x) : "scc");
    asm volatile(
      "s_lshr_b32 %[sa], %[bid], 3\n\t"
      "v_readfirstlane_b32 %[w], %[tid]\n\ts_mov_b32 %[keep], m0\n\ts_and_b32 %[sc], %[bid], 7\n\t"
      "s_lshr_b32 %[w], %[w], 6\n\ts_cmp_lt_u32 %[w], 8\n\ts_cbranch_scc1 1f\n\t"
      "v_bfe_u32 %[va], %[tid], 4, 2\n\tv_and_b32 %[vb], 15, %[tid]\n\tv_xor_b32 %[vb], %[vb], %[va]\n\tv_lshlrev_b32 %[vb], 4, %[vb]\n\t"
      "v_lshl_or_b32 %[k0], %[va], 8, %[vb]\n\tv_xor_b32 %[k1], 64, %[k0]\n\tv_xor_b32 %[k2], 0x80, %[k0]\n\tv_xor_b32 %[k3], 0xc0, %[k0]\n\t"
      "s_sub_u32 %[sb], %[w], 8\n\ts_lshl_b32 m0, %[sb], 14\n\ts_lshl_b32 %[sc], %[sc], 8\n\ts_add_u32 %[sb], %[sb], %[sc]\n\ts_lshl_b32 %[sb], %[sb], 1\n\t"
      "s_cmp_gt_u32 %[w], 11\n\ts_cselect_b32 %[sa], 0, %[sa]\n\ts_cmp_lt_u32 %[sa], 8\n\t"
      "s_waitcnt lgkmcnt(0)\n\ts_cbranch_scc1 9f\n\t"
      "s_and_b32 %[sc], s46, 0xffff\n\ts_add_u32 %[sb], %[sb], %[sc]\n\ts_lshl_b32 %[sb], %[sb], 13\n\t"
      "s_add_u32 s44, s18, %[sb]\n\ts_addc_u32 s45, s19, 0\n\t" DMA4("s[44:45]")
      "s_branch 9f\n\t"
      "1:\n\tv_bfe_u32 %[va], %[tid], 3, 3\n\tv_and_b32 %[vb], 6, %[va]\n\tv_xor_b32 %[vb], %[vb], %[tid]\n\tv_and_b32 %[vb], 7, %[vb]\n\tv_lshlrev_b32 %[vb], 4, %[vb]\n\t"
      "v_and_b32 %[k1], 4, %[va]\n\tv_add_u32 %[va], %[va], %[k1]\n\t"
      "v_and_b32 v106, 63, %[tid]\n\tv_lshlrev_b32 v106, 4, v106\n\tv_mov_b32 v104, 0\n\t"
      "v_and_b32 v105, 3, %[tid]\n\tv_lshlrev_b32 v105, 2, v105\n\tv_and_or_b32 v105, %[tid], 48, v105\n\t"
      "s_cmp_lt_u32 %[sa], 8\n\ts_cbranch_scc0 2f\n\t"
      "v_lshl_or_b32 %[k0], %[va], 13, %[vb]\n\tv_add_u32 %[k1], 0x1fc00, %[k0]\n\tv_add_u32 %[k2], 0x7800, %[k0]\n\tv_add_u32 %[k3], 0x27400, %[k0]\n\t"
      "v_bfe_u32 %[va], %[tid], 2, 1\n\tv_lshl_or_b32 v105, %[va], 6, v105\n\ts_lshl_b32 %[sb], %[w], 7\n\tv_add_u32 v105, %[sb], v105\n\t"
      "s_lshl_b32 m0, %[w], 13\n\ts_lshl_b32 %[sb], %[w], 9\n\ts_add_u32 %[sb], %[sb], %[sc]\n\ts_lshl_b32 %[sb], %[sb], 3\n\ts_add_u32 %[sb], %[sb], %[sa]\n\ts_lshl_b32 %[sb], %[sb], 7\n\t"
      "s_waitcnt lgkmcnt(0)\n\t"
      "global_load_dword v105, v105, s[16:17]\n\tglobal_load_dwordx4 v[106:109], v106, s[16:17]\n\tglobal_load_dword v104, v104, s[42:43] sc1\n\t"
      "s_add_u32 s44, s22, %[sb]\n\ts_addc_u32 s45, s23, 0\n\t" DMA4("s[44:45]")
      "s_add_u32 s44, s44, 0x40000\n\ts_addc_u32 s45, s45, 0\n\ts_add_u32 m0, m0, 0x1000\n\ts_nop 0\n\t" DMA4("s[44:45]")
      "s_branch 9f\n\t"
      "2:\n\tv_mul_u32_u24 %[k0], 0x1800, %[va]\n\tv_add_u32 %[k0], %[k0], %[vb]\n\tv_add_u32 %[k1], 0x17c00, %[k0]\n\tv_add_u32 %[k2], 0x5800, %[k0]\n\tv_add_u32 %[k3], 0x1d400, %[k0]\n\t"
      "s_lshr_b32 %[sb], %[sa], 3\n\ts_and_b32 s44, %[w], 5\n\ts_lshl_b32 %[sb], %[sb], 3\n\ts_or_b32 %[sb], %[sb], s44\n\ts_cmp_eq_u32 %[sb], 17\n\ts_cbranch_scc0 5f\n\t"
      "s_lshl_b32 %[sb], %[w], 12\n\ts_and_b32 %[sb], %[sb], 0x2000\n\ts_and_b32 s44, %[bid], 63\n\ts_lshl_b32 s44, s44, 7\n\ts_add_u32 %[sb], %[sb], s44\n\ts_add_u32 %[sb], %[sb], %[pr]\n\t"
      "v_mov_b32 v107, %[sb]\n\ts_mov_b64 exec, 1\n\ts_waitcnt lgkmcnt(0)\n\ts_memtime s[44:45]\n\ts_waitcnt lgkmcnt(0)\n\t"
      "global_store_dword v107, v104, s[42:43] sc1\n\ts_barrier\n\ts_waitcnt vmcnt(0)\n\ts_memtime s[50:51]\n\ts_waitcnt lgkmcnt(0)\n\ts_sub_u32 %[rt], s50, s44\n\ts_mov_b64 exec, -1\n\t"
      "5:\n\ts_sub_u32 %[sb], %[sa], 23\n\ts_cmp_lt_u32 %[sb], 4\n\ts_cbranch_scc0 3f\n\t"
      "v_bfe_u32 %[va], %[tid], 2, 1\n\tv_lshl_or_b32 v105, %[va], 6, v105\n\ts_lshl_b32 s44, %[w], 7\n\tv_add_u32 v105, s44, v105\n\t"
      "s_lshl_b32 s44, %[w], 13\n\ts_add_u32 m0, s44, 0x10000\n\ts_lshl_b32 %[sc], %[sc], 2\n\ts_add_u32 %[sb], %[sb], %[sc]\n\ts_add_u32 %[sb], %[sb], 16\n\ts_lshl_b32 %[sb], %[sb], 7\n\t"
      "s_mul_i32 %[sc], %[w], 0x60000\n\ts_add_u32 %[sb], %[sb], %[sc]\n\t"
      "s_waitcnt lgkmcnt(0)\n\t"
      "global_load_dword v105, v105, s[36:37]\n\tglobal_load_dword v104, v104, s[42:43] sc1\n\t"
      "s_add_u32 s44, s38, %[sb]\n\ts_addc_u32 s45, s39, 0\n\t" DMA4("s[44:45]")
      "s_add_u32 s44, s44, 0x30000\n\ts_addc_u32 s45, s45, 0\n\ts_add_u32 m0, m0, 0x1000\n\ts_nop 0\n\t" DMA4("s[44:45]")
      "s_branch 9f\n\t3:\n\t"
      "s_and_b32 %[sb], %[sa], 3\n\ts_lshl_b32 %[sb], %[sb], 2\n\ts_and_b32 s44, %[w], 3\n\ts_add_u32 %[sb], %[sb], s44\n\t"
      "s_lshl_b32 s44, s44, 12\n\ts_add_u32 m0, s44, %[tb]\n\ts_lshl_b32 s44, %[sb], 6\n\tv_add_u32 v105, s44, v105\n\t"
      "s_mul_i32 %[sb], %[sb], 0x30000\n\ts_lshl_b32 %[sc], %[sc], 8\n\ts_add_u32 %[sb], %[sb], %[sc]\n\ts_bfe_u32 %[sc], %[sa], 0x10002\n\ts_lshl_b32 %[sc], %[sc], 7\n\ts_add_u32 %[sb], %[sb], %[sc]\n\t"
      "s_lshr_b32 %[sc], %[w], 2\n\ts_lshr_b32 %[sa], %[sa], 3\n\ts_cmp_eq_u32 %[sa], 1\n\ts_cselect_b32 %[sa], %[sc], 0\n\ts_cmp_eq_u32 %[sa], 1\n\t"
      "s_waitcnt lgkmcnt(0)\n\t"
      "global_load_dword v105, v105, s[36:37]\n\tglobal_load_dword v104, v104, s[42:43] sc1\n\ts_cbranch_scc0 9f\n\t"
      "s_add_u32 s44, s38, %[sb]\n\ts_addc_u32 s45, s39, 0\n\t" DMA4("s[44:45]")
      "9:\n\ts_mov_b32 m0, %[keep]"
      : "=&{s[44:45]}"(pa), [w] "=&s"(wave), [k0] "=&v"(ko0), [k1] "=&v"(ko1), [k2] "=&v"(ko2), [k3] "=&v"(ko3),
        "=&{v104}"(ep), "=&{v105}"(d1), "=&{v[106:109]}"(xs), [sa] "=&s"(sa), [sb] "=&s"(sb), [sc] "=&s"(sc), [keep] "=&s"(keep), [va] "=&v"(va), [vb] "=&v"(vb), [rt] "=&s"(rtt)
      : "{s[16:31]}"(ka), "{s[36:43]}"(kb8), "{s46}"(tab), [bid] "s"(blockIdx.x), [tid] "v"(threadIdx.x), [tb] "i"(L_TB), [pr] "i"(PROBE_OFF)
      : "memory", "scc", "s50", "s51");
    asm volatile("" : "+{s[16:31]}"(ka), "+{s[36:43]}"(kb8), "+{s46}"(tab));
  }
  volatile LAS u32* FL = (volatile LAS u32*)(L + L_FLAG);
  LAS float* RED = (LAS float*)(L + L_RED);
  const int lane = threadIdx.x & 63;
  const int bid = blockIdx.x, xc = bid & 7, ix = bid >> 3, rb = ix & 7;
  const int g = lane >> 4, n = lane & 15;
  const bool chain = ix < 8, wcu = (ix >> 3) == 1;
  const int toff = tab & 0xffff, nhalf = tab >> 16, ntask = (nhalf + 1) >> 1;
  const bool ninth = wave == 2 && ntask == 9;
  const int aw = ninth ? 8 : wave - 8;
  const bool head = (wave >> 2) == 2 && !chain, p1 = wcu && (wave >> 2) == 1;
  const u32 ldst = (u32)(uintptr_t)L + L_K + aw * 16384;
#define KGROUP(kb, lo) do { u32 keep_; \
    asm volatile("s_mov_b32 %0, m0\n\ts_mov_b32 m0, %6\n\ts_nop 0\n\t" \
                 "global_load_lds_dwordx4 %1, %5" KV_SFX "\n\t" \
                 "global_load_lds_dwordx4 %2, %5 offset:1024" KV_SFX "\n\t" \
                 "global_load_lds_dwordx4 %3, %5 offset:2048" KV_SFX "\n\t" \
                 "global_load_lds_dwordx4 %4, %5 offset:3072" KV_SFX "\n\t" \
                 "s_mov_b32 m0, %0" \
                 : "=&s"(keep_) : "v"(ko0), "v"(ko1), "v"(ko2), "v"(ko3), "s"(kb), "s"(ldst + (lo)) : "memory"); } while (0)
#define PTR(v, i) (((u64)(v)[2 * (i) + 1] << 32) | (v)[2 * (i)])
  Ptrs p;
  p.x = (const u16*)PTR(ka, 0); p.K = (const u16*)PTR(ka, 1); p.V = (const u16*)PTR(ka, 2); p.wq = (const u16*)PTR(ka, 3); p.wo = (const u16*)PTR(ka, 4);
  p.wg = (const u16*)PTR(ka, 5); p.wu = (const u16*)PTR(ka, 6); p.wd = (const u16*)PTR(ka, 7);
  p.c = (const u16*)PTR(kb8, 0); p.wmod = (const u16*)PTR(kb8, 1); p.out = (u16*)PTR(kb8, 2); p.ws = (void*)PTR(kb8, 3);
#define MKWS() const rsrc_t ws = mkr(p.ws, WS_BYTES)
  if (wave == 0 && lane < 16) FL[lane] = (lane == 0 && ix >= 16) ? 1u : 0u;
  if (!((ix >> 3) == 2 && (wave == 1 || wave == 3))) BARRIER();

  MKWS();
  const bool gcu = ix >= 16 && ix < 23, mcu = ix >= 23 && ix < 27;
  u32 tag = 0;
  if (__builtin_expect(wave < 8 && !ninth, 1)) {
  if (wave >= 4 && !(chain || wcu || gcu || mcu)) return;
  __builtin_amdgcn_s_setprio(3);
  asm volatile("s_waitcnt vmcnt(8)\n\ts_cmp_eq_u32 %3, 2\n\ts_cbranch_scc1 1f\n\ts_waitcnt vmcnt(4)\n\ts_cmp_eq_u32 %3, 1\n\ts_cbranch_scc1 1f\n\ts_waitcnt vmcnt(0)\n\t1:"
               : "+{v104}"(ep), "+{v105}"(d1), "+{v[106:109]}"(xs) : "s"(__builtin_amdgcn_readfirstlane((chain || mcu) ? 2 : p1 ? 1 : 0)) : "memory", "scc");
  const int cg = xc * 2 + (rb >> 2), ks = rb & 3;
  const int pi = ((n >> 2) & 1) * 16 + g * 4 + (n & 3);
  tag = ep + 1u;
  if (__builtin_expect(p1, 1)) {
    v4u a = bcast4<0>(silu2(d1)), f[4];
    int ho = H0_OFF + (cg * 4 + ks) * 512 + lane * 8;
    PIN1(a); PIN1(ho);
    VM0();
    lds_frags(L + L_TB + (wave & 3) * 4096, lane, f);
    RED[(wave & 3) * 64 + lane] = gemv<1>(&a, f, g);
    if (lds_ticket(&FL[3], lane) == 3u) {
      const float s = (RED[lane] + RED[64 + lane]) + (RED[128 + lane] + RED[192 + lane]);
      const float s_hi = dpp<SHL1>(s);
      if (!(lane & 1)) stc(ws, ho, v4u{tag, asu(s), ~tag, asu(s_hi)});
      MEMFENCE();
      if (lane == 0) FL[0] = 1u;
    }
  }
  if (__builtin_expect(chain, 1)) {
    float mb, inv;
    ln_stats(xs, mb, inv);
    f32x2 y = up2(ln_pair(d1, mb, inv));
    PIN1(y);
    VM0();
    v4u f[8];
    lds_frags(L + L_K + wave * 8192, lane, f); lds_frags(L + L_K + wave * 8192 + 4096, lane, f + 4);
    SCHED();
    v4u h0, h1, h2, h3;
    pollT4(ws, H0_OFF + (wave + (n >> 3) * 8) * 2048 + pi * 16, tag, ~tag, h0, h1, h2, h3);
    const u32 md = pk((asf(h0.y) + asf(h1.y)) + (asf(h2.y) + asf(h3.y)), (asf(h0.w) + asf(h1.w)) + (asf(h2.w) + asf(h3.w)));
    const u32 ot = dppu<ROR8>(md), sa = (n & 8) ? ot : md, ca = (n & 8) ? md : ot;
    const u32 hp = pk2(up2(pk2(y * one_plus(ca))) + up2(sa));
    const v4u a[2] = {bcast4<0>(hp), bcast4<4>(hp)};
    RED[lane * 8 + wave] = gemv<2>(a, f, g);
    if (lds_ticket(&FL[7], lane) == 7u) {
      const float s = sum8((u32)(uintptr_t)(RED + lane * 8));
      const u32 d = pk(s, dpp<SHL1>(s)), d2 = dppu<SHL2>(d);
      if ((lane & 3) == 0) stc(ws, Q_OFF + (xc * 8 + rb) * 256 + lane * 4, v4u{tag, d, ~tag, d2});
      MEMFENCE();
      if (lane == 0) FL[0] = 1u;
    }
  }
  if ((ix >> 3) == 2 && (wave == 1 || wave == 3)) {
    if (wave == 3) { if (lane == 0) FL[12] = rtt | 1u; }
    else {
      lds_wait_ge(&FL[12], 1u);
      const u32 half = (__builtin_amdgcn_s_getreg(20 | (3 << 11)) >> 2) & 1u;
      const u32 vote = (rtt > FL[12] ? 1u : 0u) ^ half;
      if (lane == 0) stc2(ws, VOTE_OFF + (xc * 8 + rb) * 16, v2u{tag, ~tag ^ (vote | (half << 1))});
    }
  }
  if (mcu) {
    const u32 sp = silu2(d1);
    const v4u a[2] = {bcast4<0>(sp), bcast4<4>(sp)};
    VM0();
    v4u f[8];
    lds_frags(L + 65536 + wave * 8192, lane, f); lds_frags(L + 65536 + wave * 8192 + 4096, lane, f + 4);
    RED[lane * 8 + wave] = gemv<2>(a, f, g);
    MEMFENCE();
    lds_inc(&FL[3], lane);
    if (wave == 1) {
      lds_wait_ge(&FL[3], 8u);
      const float s = sum8((u32)(uintptr_t)(RED + lane * 8));
      const u32 d = nosent(pk(s, dpp<SHL1>(s)));
      lds_wait_ge(&FL[5], 1u);
      if (!(lane & 1)) stc1(ws, MR_OFF + (xc * 4 + (ix - 23)) * 128 + lane * 2, d);
    }
  }
  if (wcu && wave == 1) {
    lds_wait_ge(&FL[0], 1u);
    const v4u sv = {SENT, SENT, SENT, SENT};
#pragma unroll
    for (int j = 0; j < CLR_KB; ++j) stc(ws, S_OFF + (xc * 8 + rb) * (CLR_KB * 1024) + j * 1024 + lane * 16, sv);
    VM0();
    if (lane == 0) stc2(ws, CF_OFF + (xc * 8 + rb) * 16, v2u{tag, ~tag});
  }
  if (wave == 0) {
    const int qb = Q_OFF + xc * 2048 + lane * 16;
    if (!chain && !mcu) __builtin_amdgcn_s_sleep(100);
    _Pragma("nounroll") for (u32 s = 0; s < SPIN_CAP; ++s) {
      const v4u q0 = ldc(ws, qb), q1 = ldc(ws, qb + 1024);
      SCHED();
      if (__all((((q0.x ^ tag) | (q1.x ^ tag)) | ((q0.z ^ ~tag) | (q1.z ^ ~tag))) == 0u)) {
        LAS unsigned char* qd = L + L_Q + (lane >> 5) * 320 + (lane & 31) * 8;
        *(LAS v2u*)qd = v2u{q0.y, q0.w};
        *(LAS v2u*)(qd + 640) = v2u{q1.y, q1.w};
        LGKM0();
        if (lane == 0) FL[1] = 1u;
        break;
      }
      MEMFENCE();
    }
  }
  if (wave == 3) {
    {
      u32 pv = 0;
      __builtin_amdgcn_s_sleep(100);
      _Pragma("nounroll") for (u32 s = 0; s < SPIN_CAP; ++s) {
        const v2u cf = __builtin_amdgcn_raw_buffer_load_b64(ws, CF_OFF + lane * 16, 0, SC1), vt = __builtin_amdgcn_raw_buffer_load_b64(ws, VOTE_OFF + lane * 16, 0, SC1);
        SCHED();
        pv = vt.y ^ ~tag;
        if (__all(((cf.x ^ tag) | (cf.y ^ ~tag) | (vt.x ^ tag) | (pv & ~3u)) == 0u)) break;
        __builtin_amdgcn_s_sleep(16);
        MEMFENCE();
      }
      const u32 yes = __builtin_popcountll(__ballot(pv & 1u));
      const u32 hv = (u32)__ballot(pv & 2u);
      const u32 par = ((hv & 1u) | ((hv >> 7) & 2u) | ((hv >> 14) & 4u) | ((hv >> 21) & 8u)) ^ (yes > 32u ? 15u : 0u);
      if (lane == 0) FL[5] = 1u | (par << 1);
    }
  }
  }
  else {
    const bool early = aw < 4;
    if (ninth) {
      VM0();
      const u32 kv0 = g * 256 + ((n ^ g) << 4);
      ko0 = kv0; ko1 = kv0 ^ 64u; ko2 = kv0 ^ 128u; ko3 = kv0 ^ 192u;
    }
    if (aw >= ntask) return;
    if (chain) lds_wait_ge(&FL[0], 1u);
    if (!early) {
      lds_wait_ge(&FL[3], (ix >= 23 && ix < 27) ? 8u : (wcu && ninth) ? 4u : 0u);
      _Pragma("nounroll") for (u32 s = 0; s < (SPIN_CAP << 3); ++s) { if (FL[2] >= 4u || FL[1] != 0u) break; __builtin_amdgcn_s_sleep(1); }
      MEMFENCE();
    }
    const int hh = n >> 2, e2 = n & 3;
    u32 qseen = (!early && ix >= 16) ? 1u : 0u;
    const u32 key0 = (u32)(xc * 16384 + toff * 32 + aw * 64);
    const bool halft = 2 * aw + 1 == nhalf;
#define THROTTLE() do { if (!qseen) { const v2u fl_ = *(volatile LAS v2u*)FL; qseen = __builtin_amdgcn_readfirstlane(fl_.y); \
      if (!qseen) { if (__builtin_amdgcn_readfirstlane(fl_.x)) asm volatile("s_waitcnt vmcnt(%0)" :: "i"(THR_W - 4) : "memory"); else asm volatile("s_waitcnt vmcnt(%0)" :: "i"(THR_Q - 4) : "memory"); } } SCHED(); } while (0)
    const u64 kb = (u64)p.K + (u64)key0 * 256;
    if (!head) KGROUP(kb, 0);
    THROTTLE(); KGROUP(kb + 4096, 4096);
    THROTTLE();
    if (!halft) { KGROUP(kb + 8192, 8192); THROTTLE(); KGROUP(kb + 12288, 12288); THROTTLE(); }
    v4u vv[16];
    {
      const rsrc_t rv = mkr(p.V, 8 * 16384 * 256);
      const int vo = (halft && g >= 2) ? 0x40000000 : g * 4096 + n * 16;
#pragma unroll
      for (int i = 0; i < 16; ++i) { vv[i] = ld4<NT>(rv, vo + i * 256, key0 * 256); if ((i & 3) == 3 && i < 15) THROTTLE(); }
    }
    SCHED();
    lds_inc(&FL[2], lane);
    lds_wait_ge(&FL[1], 1u);
    asm volatile("s_waitcnt vmcnt(16)" ::: "memory");
    SCHED();
    float s2[4], pe[4];
    {
      const LAS unsigned char* kbase = L + L_K + aw * 16384 + n * 256;
      const LAS unsigned char* qa = L + L_Q + (n & 3) * 320 + g * 16;
      f32x4 sa[4];
#pragma unroll
      for (int t = 0; t < 4; ++t) sa[t] = f32x4{0.f, 0.f, 0.f, 0.f};
#pragma unroll
      for (int s = 0; s < 4; ++s) {
        const v4u a = *(const LAS v4u*)(qa + 64 * s);
#pragma unroll
        for (int t = 0; t < 4; ++t) sa[t] = mfma32(a, *(const LAS v4u*)(kbase + t * 4096 + (((4 * s + g) ^ n) << 4)), sa[t]);
      }
      const f32x4 lo = (g & 1) ? sa[1] : sa[0], hi = (g & 1) ? sa[3] : sa[2], sacc = (g & 2) ? hi : lo;
      constexpr float SC = 0.08838834764831845f;
      const f32x2 c = up2(pk2(up2(pk(sacc[0], sacc[1])) * SC)), d = up2(pk2(up2(pk(sacc[2], sacc[3])) * SC));
      const bool dead = halft && g >= 2;
      s2[0] = dead ? -__builtin_inff() : c.x; s2[1] = dead ? -__builtin_inff() : c.y; s2[2] = dead ? -__builtin_inff() : d.x; s2[3] = dead ? -__builtin_inff() : d.y;
    }
    const float mrow = row_max(tm16(tm32(s2[0], s2[2]), tm32(s2[1], s2[3])));
#pragma unroll
    for (int h2 = 0; h2 < 4; ++h2) pe[h2] = __builtin_amdgcn_exp2f((s2[h2] - rdlf(mrow, 16 * h2)) * LOG2E);
    const u32 w0 = pk(pe[0], pe[1]), w1 = pk(pe[2], pe[3]);
    const float lrow = row_sum(tr16(tr32(pe[0], pe[2]), tr32(pe[1], pe[3])));
    if (n == 0) *(LAS f32x2*)(L + L_ML + g * 128 + aw * 8) = f32x2{mrow, lrow};
    MEMFENCE();
    lds_inc(&FL[4], lane);
    const u32 two = (u32)(2 * hh) | ((u32)(2 * hh + 1) << 8);
    const u32 selv = (e2 & 1) ? ((two << 16) | 0x0C0Cu) : (0x0C0C0000u | two);
    const u32 sel0 = (e2 >> 1) == 0 ? selv : 0x0C0C0C0Cu, sel1 = (e2 >> 1) == 1 ? selv : 0x0C0C0C0Cu;
    v2u xb[16];
#define PV_PREP(i) { const u32 b0_ = dppu<BC + (i)>(w0), b1_ = dppu<BC + (i)>(w1); xb[i] = v2u{__builtin_amdgcn_perm(b1_, b0_, sel0), __builtin_amdgcn_perm(b1_, b0_, sel1)}; }
    PV_PREP(0) PV_PREP(1) PV_PREP(2) PV_PREP(3) PV_PREP(4) PV_PREP(5) PV_PREP(6) PV_PREP(7)
    PV_PREP(8) PV_PREP(9) PV_PREP(10) PV_PREP(11) PV_PREP(12) PV_PREP(13) PV_PREP(14) PV_PREP(15)
#undef PV_PREP
    u32 obw = (u32)(uintptr_t)L + L_K + aw * 16384 + (hh * 128 + e2 * 16 + 4 * g) * 4;
    PIN1(obw);
    SCHED();
    f32x4 o1 = {0.f, 0.f, 0.f, 0.f}, o2 = {0.f, 0.f, 0.f, 0.f};
#pragma unroll
    for (int i = 0; i < 16; ++i) {
      o1 = __builtin_amdgcn_mfma_f32_16x16x16bf16_1k(__builtin_bit_cast(s16x4, v2u{vv[i].x, vv[i].y}), __builtin_bit_cast(s16x4, xb[i]), o1, 0, 0, 0);
      o2 = __builtin_amdgcn_mfma_f32_16x16x16bf16_1k(__builtin_bit_cast(s16x4, v2u{vv[i].z, vv[i].w}), __builtin_bit_cast(s16x4, xb[i]), o2, 0, 0, 0);
    }
    *(LAS f32x4*)(uintptr_t)obw = o1; *(LAS f32x4*)(uintptr_t)(obw + 256) = o2;
    if (aw < 2 || (aw > 3 && aw < 8) || (ninth && !gcu)) { BARRIER(); return; }
    if (!ninth) {
    const int tau = (aw - 2) * 64 + lane, h = tau >> 5;
    float f[9];
    lds_wait_ge(&FL[5], 1u);
    const int par = __builtin_amdgcn_readfirstlane(FL[5]) >> 1;
    lds_wait_ge(&FL[4], (u32)ntask);
    {
      float m[10], l[10];
#pragma unroll
      for (int w = 0; w < 5; ++w) { const f32x4 t = *(const LAS f32x4*)(L + L_ML + h * 128 + w * 16); m[2 * w] = t.x; l[2 * w] = t.y; m[2 * w + 1] = t.z; l[2 * w + 1] = t.w; }
#pragma unroll
      for (int w = 4; w < 9; ++w) { m[w] = w < ntask ? m[w] : -__builtin_inff(); l[w] = w < ntask ? l[w] : 0.f; }
      const float M = mx(mx(mx(mx(m[0], m[1]), mx(m[2], m[3])), mx(mx(m[4], m[5]), mx(m[6], m[7]))), m[8]);
      float Ls = 0.f;
#pragma unroll
      for (int w = 0; w < 9; ++w) { f[w] = __builtin_amdgcn_exp2f((m[w] - M) * LOG2E); Ls = fmaf(l[w], f[w], Ls); PIN1(f[w]); }
      if ((lane & 31) == 0) stc2(ws, ml_at(xc, par) + (ix * 2 + aw - 2) * 128 + (lane >> 5) * 8, v2u{asu(M), asu(Ls)});
    }
    int t1o = t1_at(xc, par, ((h * 4 + ((tau >> 3) & 3)) * 32 + ix) * 128 + (tau & 7) * 16);
    u32 pa[9];
#pragma unroll
    for (int w = 0; w < 9; ++w) { pa[w] = (u32)(uintptr_t)(L + L_K) + tau * 16 + (w < ntask ? w : 0) * 16384; PIN1(pa[w]); }
    PIN1(t1o);
    __builtin_amdgcn_s_setprio(3);
    BARRIER();
    f32x4 part[9];
#pragma unroll
    for (int w = 0; w < 9; ++w) part[w] = *(const LAS f32x4*)(uintptr_t)pa[w];
    const f32x4 acc = (((part[0] * f[0] + part[1] * f[1]) + (part[2] * f[2] + part[3] * f[3])) + ((part[4] * f[4] + part[5] * f[5]) + (part[6] * f[6] + part[7] * f[7]))) + part[8] * f[8];
    stc(ws, t1o, __builtin_bit_cast(v4u, acc));
    lds_inc(&FL[8], lane);
    return;
    }
    __builtin_amdgcn_s_setprio(3);
  }

  LAS unsigned char* tb = L + L_K + wave * 16384 + 8192;
  if (ix < 16) {
    if (wave < 4) return;
    const int x = wave & 3, hl = ix >> 2, hg = xc * 4 + hl, qd = ix & 3;
    const rsrc_t ro = mkr(p.wo, 4096 * 512 * 2);
    const int col = xc * 64 + rb * 8;
    lds_wait_ge(&FL[2], (u32)ntask);
    v4u w[8];
    {
      const int rho = lane >> 3, vo = (64 * (rho >> 2) + 8 * (rho & 3)) * 1024 + (lane & 7) * 16;
#pragma unroll
      for (int i = 0; i < 8; ++i) w[i] = ld4<NT>(ro, vo, (hg * 128 + 32 * ((i >> 1) & 1) + 2 * qd + (i & 1)) * 1024 + x * 256 + (i >> 2) * 128);
    }
    BARRIER();
    lds_wait_ge(&FL[8], 2u);
    const int par = __builtin_amdgcn_readfirstlane(FL[5]) >> 1;
    v4u f[8];
    tile_frags(tb, lane, w, f); tile_frags(tb, lane, w + 4, f + 4);
    const v2u ma = poll2(ws, ml_at(xc, par) + ((lane >> 1) * 2 + (hl >> 1)) * 128 + (hl & 1) * 8);
    int ob = t1_at(xc, par, ((hl * 4 + qd) * 32 + (lane >> 1)) * 128 + x * 32 + (lane & 1) * 16);
    int t2o = T2_OFF + row_at(xc * 16 + ix, par, x * 512 + lane * 4), t2p = T2_OFF + row_at(xc * 16 + ix, par, x * 512 + 256 + lane * 4);
    float M = asf(ma.x);
    M = mx(M, dpp<XOR2>(M)); M = mx(M, dpp<ROR4>(M)); M = mx(M, dpp<ROR8>(M)); M = tm16(M, M); M = tm32(M, M);
    float fa = __builtin_amdgcn_exp2f((asf(ma.x) - M) * LOG2E);
    float Ls = fa * asf(ma.y);
    Ls = tr32(Ls, Ls); Ls = tr16(Ls, Ls); Ls += dpp<ROR8>(Ls); Ls += dpp<ROR4>(Ls); Ls += dpp<XOR2>(Ls);
    float inv = __builtin_amdgcn_rcpf(Ls);
    u32 oa = (u32)(uintptr_t)(L + L_X) + x * 16 + (lane & 1) * 8 + (lane >> 5) * 4;
    u32 oa2 = (u32)(uintptr_t)(L + L_X) + g * 16;
    fa *= inv;
    PIN1(fa); PIN1(ob); PIN1(t2o); PIN1(t2p); PIN1(oa); PIN1(oa2);
    SCHED();
    const v4u a = poll4(ws, ob);
    float u0 = tr32(fa * asf(a.x), fa * asf(a.z)), u1 = tr32(fa * asf(a.y), fa * asf(a.w));
    u0 = tr16(u0, u0); u1 = tr16(u1, u1);
    u0 += dpp<ROR8>(u0); u1 += dpp<ROR8>(u1);
    u0 += dpp<ROR4>(u0); u1 += dpp<ROR4>(u1);
    u0 += dpp<XOR2>(u0); u1 += dpp<XOR2>(u1);
    if (!(lane & 30)) *(LAS u32*)(uintptr_t)oa = pk(u0, u1);
    BARRIER();
    const v4u av = *(const LAS v4u*)(uintptr_t)oa2;
    const float y0 = gemv<1>(&av, f, g), y1 = gemv<1>(&av, f + 4, g);
    stc_row(ws, t2o, y0, lane); stc_row(ws, t2p, y1, lane);
    if (!chain || wave != 4) return;
    f32x2 xin = up2(*(const GAS u32*)((const GAS u16*)p.x + col + 2 * g));
    u32 gaw, gbw;
    poll1x2<3072>(ws, MR_OFF + (col + 2 * g) * 2, gaw, gbw);
    f32x2 ga = up2(gaw), gb = up2(gbw);
    int tio = T2_OFF + row_at(lane, par, col * 4), tio2 = tio + 131072, yo = lane < 56 ? Y_OFF + row_at(lane, par, col * 4) : 0x40000000;
    PIN1(ga); PIN1(gb); PIN1(xin); PIN1(tio); PIN1(tio2); PIN1(yo);
    v4u pa, pb, pc, pd;
    poll4x4(ws, tio, tio2, pa, pb, pc, pd);
    pa = __builtin_bit_cast(v4u, __builtin_bit_cast(f32x4, pa) + __builtin_bit_cast(f32x4, pc));
    pb = __builtin_bit_cast(v4u, __builtin_bit_cast(f32x4, pb) + __builtin_bit_cast(f32x4, pd));
    const u32 x1 = nosent(pk2(xin + up2(pk2(ga * up2(pk2(sum8x64(pa, pb)))))));
    const v4u o = {rdl(x1, 0), rdl(x1, 16), rdl(x1, 32), rdl(x1, 48)};
    if (lane < 2) stc(ws, X1_OFF + lane * 8192 + col * 16, o);
    if (bid == 0 && lane == 0) stc1(ws, EPOCH_OFF, tag);
    f32x2 x1f = up2(x1);
    PIN1(x1f);
    poll4x2<16>(ws, yo, pa, pb);
    const u32 d = pk2(x1f + up2(pk2(gb * up2(pk2(sum8x64(pa, pb))))));
    if (!n) *(GAS u32*)((GAS u16*)p.out + col + 2 * g) = d;
  } else if (gcu) {
    const int gi = xc * 7 + (ix - 16);
    const rsrc_t rd = mkr(p.wd, 1792 * 512 * 2);
    BARRIER();
    lds_wait_ge(&FL[8], 2u);
    const int par = __builtin_amdgcn_readfirstlane(FL[5]) >> 1;
    v4u w[12];
    {
      const int rho = lane >> 3;
      const GAS unsigned char* src = (const GAS unsigned char*)((lane & 4) ? p.wu : p.wg) + (size_t)(wave * 64 + 8 * (rho >> 2) + (rho & 3)) * 3584 + gi * 64 + (lane & 3) * 16;
#pragma unroll
      for (int i = 0; i < 8; ++i) w[i] = *(const GAS v4u*)(src + (32 * (i >> 2) + 16 * (i & 1) + 4 * ((i >> 1) & 1)) * 3584);
    }
    tile_load<NT>(rd, tile_voff(lane, 1024), gi * 32 * 1024 + wave * 128, 1024, w + 8);
    v4u f[12];
    tile_frags(tb, lane, w, f); tile_frags(tb, lane, w + 4, f + 4); tile_frags(tb, lane, w + 8, f + 8);
    int yo = Y_OFF + row_at(gi, par, wave * 256 + lane * 4);
    u32 aa = (u32)(uintptr_t)(L + L_X) + wave * 128 + g * 16, ra = (u32)(uintptr_t)(RED + lane * 8 + wave), sa = (u32)(uintptr_t)(RED + ((n >> 3) * 32 + g * 8 + (n & 7)) * 8);
    u32 ha = (u32)(uintptr_t)(L + L_X) + lane * 16 + wave * 4;
    PIN1(yo); PIN1(aa); PIN1(ra); PIN1(sa); PIN1(ha);
    SCHED();
    if (wave < 4) {
#define SEL4(v) ((wave & 2) ? ((wave & 1) ? (v).w : (v).z) : ((wave & 1) ? (v).y : (v).x))
      v4u sbw, cbw;
      poll4x2<1024>(ws, MR_OFF + 1024 + lane * 16, sbw, cbw);
      f32x2 sb = up2(SEL4(sbw)), c1 = one_plus(SEL4(cbw));
      int xb1 = X1_OFF + par_of(par, xc) + lane * 128;
      PIN1(sb); PIN1(c1); PIN1(xb1);
      const v4u x1v = poll4(ws, xb1);
      float mb, inv;
      ln_stats(x1v, mb, inv);
      *(LAS u32*)(uintptr_t)ha = modulate(ln_pair(SEL4(x1v), mb, inv), c1, sb);
    }
    BARRIER();
    const v4u av[2] = {*(const LAS v4u*)(uintptr_t)aa, *(const LAS v4u*)(uintptr_t)(aa + 64)};
    *(LAS float*)(uintptr_t)ra = gemv<2>(av, f, g);
    BARRIER();
    const float s = rbf(sum8(sa));
    const float gt = s, up = dpp<ROR8>(s);
    const float u2 = (-2.f * 0.7978845608028654f * LOG2E) * (gt + 0.044715f * gt * gt * gt);
    const float gv = rbf(gt * __builtin_amdgcn_rcpf(1.f + __builtin_amdgcn_exp2f(u2))) * up;
    const u32 d = pk(gv, dpp<SHL1>(gv));
    const v4u ag = {dppu<BC + 0>(d), dppu<BC + 2>(d), dppu<BC + 4>(d), dppu<BC + 6>(d)};
    stc_row(ws, yo, gemv<1>(&ag, f + 8, g), lane);
  }
}
}

extern "C" void kernel_launch(void* const* d_in, const int* in_sizes, int n_in, void* d_out, int out_size, void* d_ws, size_t ws_size, hipStream_t stream) {
  Params a;
  Ptrs& p = a.p;
  p.x = (const u16*)d_in[0]; p.K = (const u16*)d_in[1]; p.V = (const u16*)d_in[2]; p.wq = (const u16*)d_in[3]; p.wo = (const u16*)d_in[4];
  p.wg = (const u16*)d_in[6]; p.wu = (const u16*)d_in[7]; p.wd = (const u16*)d_in[8]; p.c = (const u16*)d_in[9]; p.wmod = (const u16*)d_in[10];
  p.out = (u16*)d_out; p.ws = d_ws;
  for (int i = 0; i < 32; ++i) a.tab[i] = (u32)toff_of(i) | ((u32)nhalf_of(i) << 16);
  hipLaunchKernelGGL(fused_kernel, dim3(NWG), dim3(NTHR), 0, stream, a);
}
```

```cpp
#include <hip/hip_runtime.h>
#include <stdint.h>

namespace {
typedef unsigned short u16;
typedef unsigned u32;
typedef unsigned long long u64;
typedef u32 v4u __attribute__((ext_vector_type(4)));
typedef u32 v2u __attribute__((ext_vector_type(2)));
typedef u32 v8u __attribute__((ext_vector_type(8)));
typedef u32 v16u __attribute__((ext_vector_type(16)));
typedef float f32x4 __attribute__((ext_vector_type(4)));
typedef float f32x2 __attribute__((ext_vector_type(2)));
typedef __bf16 bf16x2 __attribute__((ext_vector_type(2)));
typedef __bf16 bf16x8 __attribute__((ext_vector_type(8)));
typedef short s16x4 __attribute__((ext_vector_type(4)));
#define GAS __attribute__((address_space(1)))
#define LAS __attribute__((address_space(3)))

#define KV_SFX " nt"
constexpr int NT = 2;
constexpr int THR_W = 16;
constexpr int THR_Q = 6;
constexpr int H_W = 17, H_G = 17, H_M = 16, H_P = 17;

constexpr int NWG = 256, NTHR = 1024;
constexpr u32 SENT = 0xFFFFFFFFu;
constexpr int SC1 = 16;
constexpr u32 SPIN_CAP = 1u << 17;
constexpr float LOG2E = 1.4426950408889634f;
constexpr int EPOCH_OFF = 0;
constexpr int CF_OFF = 8192;
constexpr int Q_OFF = 16384;
constexpr int H0_OFF = 32768;
constexpr int S_OFF = 65536;
constexpr int VOTE_OFF = CF_OFF + 1024;
constexpr int T1_OFF = S_OFF;
constexpr int ML_OFF = T1_OFF + 524288;
constexpr int T2_OFF = ML_OFF + 65536;
constexpr int Y_OFF = T2_OFF + 262144;
constexpr int X1_OFF = Y_OFF + 114688;
constexpr int MR_OFF = X1_OFF + 16384;
__device__ __forceinline__ int par_of(int par, int x) { return (((par >> (x & 3)) ^ (x >> 2)) & 1) << 13; }
__device__ __forceinline__ int t1_at(int xc, int par, int off) { return T1_OFF + (xc & 3) * 131072 + (off >> 13) * 16384 + par_of(par, xc) + (off & 8191); }
__device__ __forceinline__ int ml_at(int xc, int par) { return ML_OFF + (xc & 3) * 16384 + par_of(par, xc); }
__device__ __forceinline__ int row_at(int prod, int par, int colbyte) { return (prod >> 3) * 16384 + par_of(par, colbyte >> 8) + (prod & 7) * 1024 + (colbyte & 1023); }
constexpr int CLR_KB = 16;
constexpr int S_END = S_OFF + 64 * CLR_KB * 1024;
static_assert(MR_OFF + 4096 <= S_END && S_OFF % 16384 == 0 && ML_OFF % 16384 == 0 && T2_OFF % 16384 == 0 && Y_OFF % 16384 == 0 && X1_OFF % 16384 == 0, "");
constexpr int PROBE_OFF = S_END, WS_BYTES = S_END + 16384;
constexpr int L_K = 0, L_TB = 8 * 16384, L_Q = 9 * 16384, L_FLAG = L_Q + 1280, L_ML = L_FLAG + 64, L_X = L_ML + 512, L_RED = L_X + 1024, L_END = L_RED + 4096;
static_assert(L_END <= 163840 && L_RED % 16 == 0 && L_TB % 16 == 0, "");
constexpr int CH_ALL = 512 - 8 * H_W - 7 * H_G - 4 * H_M - 5 * H_P, H_CH = CH_ALL / 8, CH_HI = CH_ALL - 8 * H_CH;
constexpr int nhalf_of(int ix) { return ix < 8 ? (ix < CH_HI ? H_CH + 1 : H_CH) : ix < 16 ? H_W : ix < 23 ? H_G : ix < 27 ? H_M : H_P; }
constexpr int toff_of(int ix) { int o = 0; for (int i = 0; i < ix; ++i) o += nhalf_of(i); return o; }
static_assert(toff_of(32) == 512 && H_G >= 8 && H_G <= 18 && H_CH >= 8 && H_CH + 1 <= 16 && H_M >= 8 && H_M <= 16 && H_W >= 8 && H_W <= 18 && H_P >= 8 && H_P <= 18, "");

struct Ptrs {
  const u16 *x, *K, *V, *wq, *wo, *wg, *wu, *wd, *c, *wmod;
  u16* out;
  void* ws;
};
struct Params { Ptrs p; u32 tab[32]; };
static_assert(sizeof(Params) == 96 + 128 && L_K == 0, "");

__device__ __forceinline__ float asf(u32 x) { return __builtin_bit_cast(float, x); }
__device__ __forceinline__ u32 asu(float x) { return __builtin_bit_cast(u32, x); }
__device__ __forceinline__ float blo(u32 w) { return asf(w << 16); }
__device__ __forceinline__ float bhi(u32 w) { return asf(w & 0xffff0000u); }
__device__ __forceinline__ u32 pk2(f32x2 v) { return __builtin_bit_cast(u32, __builtin_convertvector(v, bf16x2)); }
__device__ __forceinline__ u32 pk(float lo, float hi) { return pk2(f32x2{lo, hi}); }
__device__ __forceinline__ f32x2 up2(u32 w) { return f32x2{blo(w), bhi(w)}; }
__device__ __forceinline__ float rbf(float f) { return (float)(__bf16)f; }
__device__ __forceinline__ float dot2(u32 a, u32 b, float c) { return __builtin_amdgcn_fdot2_f32_bf16(__builtin_bit_cast(bf16x2, a), __builtin_bit_cast(bf16x2, b), c, false); }
template <int CTRL> __device__ __forceinline__ float dpp(float x) { return asf(__builtin_amdgcn_mov_dpp(asu(x), CTRL, 0xf, 0xf, true)); }
template <int CTRL> __device__ __forceinline__ u32 dppu(u32 x) { return __builtin_amdgcn_mov_dpp(x, CTRL, 0xf, 0xf, true); }
constexpr int XOR1 = 0xB1, XOR2 = 0x4E, XOR7 = 0x141, ROR8 = 0x128, ROR4 = 0x124, SHL1 = 0x101, SHL2 = 0x102, BC = 0x150;
__device__ __forceinline__ float mx(float a, float b) { return __builtin_amdgcn_fmed3f(a, b, __builtin_inff()); }
__device__ __forceinline__ float tr32(float a, float b) { auto s = __builtin_amdgcn_permlane32_swap(asu(a), asu(b), false, false); return asf(s[0]) + asf(s[1]); }
__device__ __forceinline__ float tr16(float a, float b) { auto s = __builtin_amdgcn_permlane16_swap(asu(a), asu(b), false, false); return asf(s[0]) + asf(s[1]); }
__device__ __forceinline__ float tm32(float a, float b) { auto s = __builtin_amdgcn_permlane32_swap(asu(a), asu(b), false, false); return mx(asf(s[0]), asf(s[1])); }
__device__ __forceinline__ float tm16(float a, float b) { auto s = __builtin_amdgcn_permlane16_swap(asu(a), asu(b), false, false); return mx(asf(s[0]), asf(s[1])); }
__device__ __forceinline__ float row_sum(float v) { v += dpp<ROR8>(v); v += dpp<XOR7>(v); v += dpp<XOR2>(v); v += dpp<XOR1>(v); return v; }
__device__ __forceinline__ float row_max(float v) { v = mx(v, dpp<ROR8>(v)); v = mx(v, dpp<XOR7>(v)); v = mx(v, dpp<XOR2>(v)); v = mx(v, dpp<XOR1>(v)); return v; }
__device__ __forceinline__ f32x2 sum8x64(v4u a, v4u b) {
  const float y0 = tr32(asf(a.x), asf(b.x)), y1 = tr32(asf(a.y), asf(b.y)), y2 = tr32(asf(a.z), asf(b.z)), y3 = tr32(asf(a.w), asf(b.w));
  return f32x2{row_sum(tr16(y0, y2)), row_sum(tr16(y1, y3))};
}
__device__ __forceinline__ u32 rdl(u32 v, int l) { return __builtin_amdgcn_readlane(v, l); }
__device__ __forceinline__ float rdlf(float v, int l) { return asf(__builtin_amdgcn_readlane(asu(v), l)); }
__device__ __forceinline__ u32 nosent(u32 w) { return w < 0xFFFFFFFEu ? w : 0xFFFFFFFEu; }
typedef __amdgpu_buffer_rsrc_t rsrc_t;
__device__ __forceinline__ rsrc_t mkr(const void* q, int bytes) { return __builtin_amdgcn_make_buffer_rsrc((void*)q, 0, bytes, 0x00020000); }
template <int AUX> __device__ __forceinline__ v4u ld4(rsrc_t r, int off, int so) { return __builtin_amdgcn_raw_buffer_load_b128(r, off, so, AUX); }
__device__ __forceinline__ v4u ldc(rsrc_t r, int off) { return __builtin_amdgcn_raw_buffer_load_b128(r, off, 0, SC1); }
__device__ __forceinline__ void stc(rsrc_t r, int off, v4u v) { __builtin_amdgcn_raw_buffer_store_b128(v, r, off, 0, SC1); }
__device__ __forceinline__ void stc2(rsrc_t r, int off, v2u v) { __builtin_amdgcn_raw_buffer_store_b64(v, r, off, 0, SC1); }
__device__ __forceinline__ void stc_row(rsrc_t r, int off, float v, int lane) {
  const v4u o = {asu(v), asu(dpp<0x55>(v)), asu(dpp<0xAA>(v)), asu(dpp<0xFF>(v))};
  if (!(lane & 3)) stc(r, off, o);
}
__device__ __forceinline__ void stc1(rsrc_t r, int off, u32 v) { __builtin_amdgcn_raw_buffer_store_b32(v, r, off, 0, SC1); }
#define MEMFENCE() asm volatile("" ::: "memory")
#define LGKM0() asm volatile("s_waitcnt lgkmcnt(0)" ::: "memory")
#define VM0() asm volatile("s_waitcnt vmcnt(0)" ::: "memory")
#define SCHED() __builtin_amdgcn_sched_barrier(0)
#define BARRIER() do { LGKM0(); SCHED(); __builtin_amdgcn_s_barrier(); MEMFENCE(); SCHED(); } while (0)
#define PIN1(a) asm volatile("" : "+v"(a))
#define PIN4(a) asm volatile("" : "+v"((a)[0]), "+v"((a)[1]), "+v"((a)[2]), "+v"((a)[3]))

#define POLL_HEAD "s_mov_b32 %[n], %[cap]\n\t1:\n\t"
#define POLL_TAIL "s_cbranch_execz 2f\n\ts_sub_u32 %[n], %[n], 1\n\ts_cbranch_scc0 1b\n\t2:\n\ts_mov_b64 exec, -1\n\ts_nop 2"
__device__ __forceinline__ v4u poll4(rsrc_t r, int off) {
  v4u a; u32 n;
  asm volatile(POLL_HEAD "buffer_load_dwordx4 v[124:127], %[o], %[r], 0 offen sc1\n\ts_waitcnt vmcnt(0)\n\t"
               "v_max_u32 v123, v124, v125\n\tv_max3_u32 v123, v123, v126, v127\n\tv_cmpx_eq_u32 vcc, -1, v123\n\t" POLL_TAIL
               : "=&{v[124:127]}"(a), [n] "=&s"(n) : [o] "v"(off), [r] "s"(r), [cap] "i"(SPIN_CAP) : "memory", "vcc", "scc", "v123");
  return a;
}
template <int D> __device__ __forceinline__ void poll4x2(rsrc_t r, int off, v4u& a, v4u& b) {
  u32 n;
  asm volatile(POLL_HEAD "buffer_load_dwordx4 v[120:123], %[o], %[r], 0 offen sc1\n\tbuffer_load_dwordx4 v[124:127], %[o], %[r], 0 offen offset:%[d] sc1\n\ts_waitcnt vmcnt(1)\n\t"
               "v_max_u32 v119, v120, v121\n\tv_max3_u32 v119, v119, v122, v123\n\ts_waitcnt vmcnt(0)\n\tv_max3_u32 v119, v119, v124, v125\n\tv_max3_u32 v119, v119, v126, v127\n\t"
               "v_cmpx_eq_u32 vcc, -1, v119\n\t" POLL_TAIL
               : "=&{v[120:123]}"(a), "=&{v[124:127]}"(b), [n] "=&s"(n) : [o] "v"(off), [r] "s"(r), [cap] "i"(SPIN_CAP), [d] "i"(D) : "memory", "vcc", "scc", "v119");
}
__device__ __forceinline__ void poll4x4(rsrc_t r, int off, int off2, v4u& a, v4u& b, v4u& c, v4u& d) {
  u32 n;
  asm volatile(POLL_HEAD "buffer_load_dwordx4 v[112:115], %[o], %[r], 0 offen sc1\n\tbuffer_load_dwordx4 v[116:119], %[o], %[r], 0 offen offset:16 sc1\n\t"
               "buffer_load_dwordx4 v[120:123], %[o2], %[r], 0 offen sc1\n\tbuffer_load_dwordx4 v[124:127], %[o2], %[r], 0 offen offset:16 sc1\n\t"
               "s_waitcnt vmcnt(3)\n\tv_max_u32 v110, v112, v113\n\tv_max3_u32 v110, v110, v114, v115\n\t"
               "s_waitcnt vmcnt(2)\n\tv_max_u32 v111, v116, v117\n\tv_max3_u32 v111, v111, v118, v119\n\t"
               "s_waitcnt vmcnt(1)\n\tv_max3_u32 v110, v110, v120, v121\n\tv_max3_u32 v111, v111, v122, v123\n\t"
               "s_waitcnt vmcnt(0)\n\tv_max3_u32 v110, v110, v124, v125\n\tv_max3_u32 v111, v111, v126, v127\n\t"
               "v_max_u32 v110, v110, v111\n\tv_cmpx_eq_u32 vcc, -1, v110\n\t" POLL_TAIL
               : "=&{v[112:115]}"(a), "=&{v[116:119]}"(b), "=&{v[120:123]}"(c), "=&{v[124:127]}"(d), [n] "=&s"(n)
               : [o] "v"(off), [o2] "v"(off2), [r] "s"(r), [cap] "i"(SPIN_CAP) : "memory", "vcc", "scc", "v110", "v111");
}
__device__ __forceinline__ v2u poll2(rsrc_t r, int off) {
  v2u a; u32 n;
  asm volatile(POLL_HEAD "buffer_load_dwordx2 v[126:127], %[o], %[r], 0 offen sc1\n\ts_waitcnt vmcnt(0)\n\t"
               "v_max_u32 v125, v126, v127\n\tv_cmpx_eq_u32 vcc, -1, v125\n\ts_cbranch_execz 2f\n\ts_sleep 1\n\ts_sub_u32 %[n], %[n], 1\n\ts_cbranch_scc0 1b\n\t2:\n\ts_mov_b64 exec, -1\n\ts_nop 2"
               : "=&{v[126:127]}"(a), [n] "=&s"(n) : [o] "v"(off), [r] "s"(r), [cap] "i"(SPIN_CAP) : "memory", "vcc", "scc", "v125");
  return a;
}
template <int D> __device__ __forceinline__ void poll1x2(rsrc_t r, int off, u32& a, u32& b) {
  u32 n;
  asm volatile(POLL_HEAD "buffer_load_dword v126, %[o], %[r], 0 offen sc1\n\tbuffer_load_dword v127, %[o], %[r], 0 offen offset:%[d] sc1\n\ts_waitcnt vmcnt(0)\n\t"
               "v_max_u32 v125, v126, v127\n\tv_cmpx_eq_u32 vcc, -1, v125\n\t" POLL_TAIL
               : "=&{v126}"(a), "=&{v127}"(b), [n] "=&s"(n) : [o] "v"(off), [r] "s"(r), [cap] "i"(SPIN_CAP), [d] "i"(D) : "memory", "vcc", "scc", "v125");
}
__device__ __forceinline__ void pollT4(rsrc_t r, int off, u32 tag, u32 ntag, v4u& a, v4u& b, v4u& c, v4u& d) {
  u32 n;
  asm volatile(POLL_HEAD "buffer_load_dwordx4 v[112:115], %[o], %[r], 0 offen sc1\n\tbuffer_load_dwordx4 v[116:119], %[o], %[r], 0 offen offset:512 sc1\n\t"
               "buffer_load_dwordx4 v[120:123], %[o], %[r], 0 offen offset:1024 sc1\n\tbuffer_load_dwordx4 v[124:127], %[o], %[r], 0 offen offset:1536 sc1\n\t"
               "s_waitcnt vmcnt(3)\n\tv_xor_b32 v110, v112, %[t]\n\tv_xor_b32 v111, v114, %[u]\n\t"
               "s_waitcnt vmcnt(2)\n\tv_bitop3_b32 v110, v116, %[t], v110 bitop3:0xbe\n\tv_bitop3_b32 v111, v118, %[u], v111 bitop3:0xbe\n\t"
               "s_waitcnt vmcnt(1)\n\tv_bitop3_b32 v110, v120, %[t], v110 bitop3:0xbe\n\tv_bitop3_b32 v111, v122, %[u], v111 bitop3:0xbe\n\t"
               "s_waitcnt vmcnt(0)\n\tv_bitop3_b32 v110, v124, %[t], v110 bitop3:0xbe\n\tv_bitop3_b32 v111, v126, %[u], v111 bitop3:0xbe\n\t"
               "v_or_b32 v110, v110, v111\n\tv_cmpx_ne_u32 vcc, 0, v110\n\t" POLL_TAIL
               : "=&{v[112:115]}"(a), "=&{v[116:119]}"(b), "=&{v[120:123]}"(c), "=&{v[124:127]}"(d), [n] "=&s"(n)
               : [o] "v"(off), [r] "s"(r), [cap] "i"(SPIN_CAP), [t] "v"(tag), [u] "v"(ntag) : "memory", "vcc", "scc", "v110", "v111");
}
__device__ __forceinline__ void lds_wait_ge(volatile LAS u32* f, u32 want) {
  _Pragma("nounroll") for (u32 s = 0; s < (SPIN_CAP << 3); ++s) { if (*f >= want) break; __builtin_amdgcn_s_sleep(1); }
  MEMFENCE();
}
__device__ __forceinline__ void lds_inc(volatile LAS u32* f, int lane) {
  asm volatile("s_mov_b64 exec, 1\n\tds_add_u32 %0, %1\n\ts_mov_b64 exec, -1" :: "v"((u32)(uintptr_t)f), "v"(1u) : "memory");
}
__device__ __forceinline__ u32 lds_ticket(volatile LAS u32* f, int lane) {
  u32 old;
  asm volatile("s_mov_b64 exec, 1\n\tds_add_rtn_u32 %0, %1, %2\n\ts_mov_b64 exec, -1\n\ts_waitcnt lgkmcnt(0)" : "=&v"(old) : "v"((u32)(uintptr_t)f), "v"(1u) : "memory");
  return __builtin_amdgcn_readfirstlane(old);
}
__device__ __forceinline__ float sum8(u32 addr) {
  const f32x4 t = *(const LAS f32x4*)(uintptr_t)addr + *(const LAS f32x4*)(uintptr_t)(addr + 16);
  return (t.x + t.z) + (t.y + t.w);
}
__device__ __forceinline__ float silu(float v) { return v / (1.f + __expf(-v)); }
__device__ __forceinline__ u32 silu2(u32 c) { return pk(silu(blo(c)), silu(bhi(c))); }
__device__ __forceinline__ void ln_stats(v4u xv, float& mb, float& inv) {
  constexpr u32 ONE2 = 0x3f803f80u;
  const float s = dot2(xv[0], ONE2, dot2(xv[1], ONE2, dot2(xv[2], ONE2, dot2(xv[3], ONE2, 0.f))));
  const float q = dot2(xv[0], xv[0], dot2(xv[1], xv[1], dot2(xv[2], xv[2], dot2(xv[3], xv[3], 0.f))));
  float t = row_sum(tr32(s, q));
  t = tr16(t, t);
  const float mean = rdlf(t, 0) * (1.f / 512.f), sq = rdlf(t, 32) * (1.f / 512.f);
  float var = fmaf(-mean, mean, sq);
  if (__builtin_expect(!(var * 64.f >= sq), 0)) {
    float d = 0.f;
#pragma unroll
    for (int i = 0; i < 4; ++i) { const float a = blo(xv[i]) - mean, b = bhi(xv[i]) - mean; d = fmaf(a, a, fmaf(b, b, d)); }
    d = row_sum(tr32(d, d));
    var = tr16(d, d) * (1.f / 512.f);
  }
  mb = rbf(mean);
  inv = __builtin_amdgcn_rcpf(rbf(__builtin_amdgcn_sqrtf(rbf(rbf(var) + 1e-5f))));
}
__device__ __forceinline__ u32 ln_pair(u32 x, float mb, float inv) { return pk2(up2(pk2(up2(x) - mb)) * inv); }
__device__ __forceinline__ f32x2 one_plus(u32 c) { return up2(pk2(up2(c) + 1.f)); }
__device__ __forceinline__ u32 modulate(u32 y, f32x2 c1, f32x2 sh) { return pk2(up2(pk2(up2(y) * c1)) + sh); }
__device__ __forceinline__ int tile_voff(int lane, int stride) { const int rho = lane >> 3; return (8 * (rho >> 2) + (rho & 3)) * stride + (lane & 7) * 16; }
template <int AUX> __device__ __forceinline__ void tile_load(rsrc_t r, int voff, int soff, int stride, v4u* w) {
#pragma unroll
  for (int i = 0; i < 4; ++i) w[i] = ld4<AUX>(r, voff, soff + (16 * (i & 1) + 4 * (i >> 1)) * stride);
}
__device__ __forceinline__ void tile_frags(LAS unsigned char* tb, int lane, const v4u* w, v4u* f) {
  const int rho = lane >> 3;
  LAS unsigned char* wr = tb + rho * 128 + (((lane & 7) ^ (rho & 6)) << 4);
  const int row = 4 * (lane >> 4) + ((lane >> 2) & 3);
  const LAS unsigned char* rd = tb + row * 128 + (lane & 3) * 8;
  const int sw = (row >> 1) & 3;
  v2u h[2][4];
#pragma unroll
  for (int t = 0; t < 2; ++t) {
    *(LAS v4u*)wr = w[2 * t]; *(LAS v4u*)(wr + 1024) = w[2 * t + 1];
    MEMFENCE();
#pragma unroll
    for (int ct = 0; ct < 4; ++ct) h[t][ct] = __builtin_bit_cast(v2u, __builtin_amdgcn_ds_read_tr16_b64_v4i16((LAS s16x4*)(rd + ((ct ^ sw) << 5))));
    MEMFENCE();
  }
#pragma unroll
  for (int ct = 0; ct < 4; ++ct) f[ct] = v4u{h[0][ct].x, h[0][ct].y, h[1][ct].x, h[1][ct].y};
  PIN4(f);
}
__device__ __forceinline__ void lds_frags(const LAS unsigned char* tile, int lane, v4u* f) {
  const int row = 4 * (lane >> 4) + ((lane >> 2) & 3);
  const LAS unsigned char* rd = tile + row * 128 + (lane & 3) * 8;
  const int sw = (row >> 1) & 3;
#pragma unroll
  for (int ct = 0; ct < 4; ++ct) {
    const v2u lo = __builtin_bit_cast(v2u, __builtin_amdgcn_ds_read_tr16_b64_v4i16((LAS s16x4*)(rd + ((ct ^ sw) << 5))));
    const v2u hi = __builtin_bit_cast(v2u, __builtin_amdgcn_ds_read_tr16_b64_v4i16((LAS s16x4*)(rd + 2048 + ((ct ^ sw) << 5))));
    f[ct] = v4u{lo.x, lo.y, hi.x, hi.y};
  }
  PIN4(f);
}
__device__ __forceinline__ f32x4 mfma32(v4u a, v4u b, f32x4 c) { return __builtin_amdgcn_mfma_f32_16x16x32_bf16(__builtin_bit_cast(bf16x8, a), __builtin_bit_cast(bf16x8, b), c, 0, 0, 0); }
template <int NS> __device__ __forceinline__ float gemv(const v4u* a, const v4u* f, int g) {
  f32x4 acc[4];
#pragma unroll
  for (int ct = 0; ct < 4; ++ct) acc[ct] = mfma32(a[0], f[ct], f32x4{0.f, 0.f, 0.f, 0.f});
#pragma unroll
  for (int s = 1; s < NS; ++s)
#pragma unroll
    for (int ct = 0; ct < 4; ++ct) acc[ct] = mfma32(a[s], f[4 * s + ct], acc[ct]);
  PIN4(acc);
  const float lo = (g & 1) ? acc[1][0] : acc[0][0], hi = (g & 1) ? acc[3][0] : acc[2][0];
  return (g & 2) ? hi : lo;
}
template <int B> __device__ __forceinline__ v4u bcast4(u32 v) { return v4u{dppu<BC + B>(v), dppu<BC + B + 1>(v), dppu<BC + B + 2>(v), dppu<BC + B + 3>(v)}; }

__global__ void __launch_bounds__(NTHR, 4) fused_kernel(Params) {
  __shared__ __attribute__((aligned(128))) unsigned char lds_raw[L_END];
  LAS unsigned char* L = (LAS unsigned char*)lds_raw;
  v16u ka; v8u kb8;
  int wave; u32 rtt, tab, ko0, ko1, ko2, ko3, ep, d1; v4u xs;
  {
    u32 sa, sb, sc, keep, va, vb; u64 pa;
#define DMA4(P) "global_load_lds_dwordx4 %[k0], " P KV_SFX "\n\tglobal_load_lds_dwordx4 %[k1], " P " offset:1024" KV_SFX "\n\t" \
                "global_load_lds_dwordx4 %[k2], " P " offset:2048" KV_SFX "\n\tglobal_load_lds_dwordx4 %[k3], " P " offset:3072" KV_SFX "\n\t"
    asm volatile("s_load_dwordx16 s[16:31], %[kp], 0x0\n\ts_load_dwordx8 s[36:43], %[kp], 0x40\n\t"
                 "s_lshr_b32 s46, %[bid], 3\n\ts_lshl_b32 s46, s46, 2\n\ts_add_u32 s46, s46, 0x60\n\ts_load_dword s46, %[kp], s46"
                 : "=&{s[16:31]}"(ka), "=&{s[36:43]}"(kb8), "=&{s46}"(tab) : [kp] "s"(__builtin_amdgcn_kernarg_segment_ptr()), [bid] "s"(blockIdx.x) : "scc");
    asm volatile(
      "s_lshr_b32 %[sa], %[bid], 3\n\t"
      "v_readfirstlane_b32 %[w], %[tid]\n\ts_mov_b32 %[keep], m0\n\ts_and_b32 %[sc], %[bid], 7\n\t"
      "s_lshr_b32 %[w], %[w], 6\n\ts_cmp_lt_u32 %[w], 8\n\ts_cbranch_scc1 1f\n\t"
      "v_bfe_u32 %[va], %[tid], 4, 2\n\tv_and_b32 %[vb], 15, %[tid]\n\tv_xor_b32 %[vb], %[vb], %[va]\n\tv_lshlrev_b32 %[vb], 4, %[vb]\n\t"
      "v_lshl_or_b32 %[k0], %[va], 8, %[vb]\n\tv_xor_b32 %[k1], 64, %[k0]\n\tv_xor_b32 %[k2], 0x80, %[k0]\n\tv_xor_b32 %[k3], 0xc0, %[k0]\n\t"
      "s_sub_u32 %[sb], %[w], 8\n\ts_lshl_b32 m0, %[sb], 14\n\ts_lshl_b32 %[sc], %[sc], 8\n\ts_add_u32 %[sb], %[sb], %[sc]\n\ts_lshl_b32 %[sb], %[sb], 1\n\t"
      "s_cmp_gt_u32 %[w], 11\n\ts_cselect_b32 %[sa], 0, %[sa]\n\ts_cmp_lt_u32 %[sa], 8\n\t"
      "s_waitcnt lgkmcnt(0)\n\ts_cbranch_scc1 9f\n\t"
      "s_and_b32 %[sc], s46, 0xffff\n\ts_add_u32 %[sb], %[sb], %[sc]\n\ts_lshl_b32 %[sb], %[sb], 13\n\t"
      "s_add_u32 s44, s18, %[sb]\n\ts_addc_u32 s45, s19, 0\n\t" DMA4("s[44:45]")
      "s_branch 9f\n\t"
      "1:\n\tv_bfe_u32 %[va], %[tid], 3, 3\n\tv_and_b32 %[vb], 6, %[va]\n\tv_xor_b32 %[vb], %[vb], %[tid]\n\tv_and_b32 %[vb], 7, %[vb]\n\tv_lshlrev_b32 %[vb], 4, %[vb]\n\t"
      "v_and_b32 %[k1], 4, %[va]\n\tv_add_u32 %[va], %[va], %[k1]\n\t"
      "v_and_b32 v106, 63, %[tid]\n\tv_lshlrev_b32 v106, 4, v106\n\tv_mov_b32 v104, 0\n\t"
      "v_and_b32 v105, 3, %[tid]\n\tv_lshlrev_b32 v105, 2, v105\n\tv_and_or_b32 v105, %[tid], 48, v105\n\t"
      "s_cmp_lt_u32 %[sa], 8\n\ts_cbranch_scc0 2f\n\t"
      "v_lshl_or_b32 %[k0], %[va], 13, %[vb]\n\tv_add_u32 %[k1], 0x1fc00, %[k0]\n\tv_add_u32 %[k2], 0x7800, %[k0]\n\tv_add_u32 %[k3], 0x27400, %[k0]\n\t"
      "v_bfe_u32 %[va], %[tid], 2, 1\n\tv_lshl_or_b32 v105, %[va], 6, v105\n\ts_lshl_b32 %[sb], %[w], 7\n\tv_add_u32 v105, %[sb], v105\n\t"
      "s_lshl_b32 m0, %[w], 13\n\ts_lshl_b32 %[sb], %[w], 9\n\ts_add_u32 %[sb], %[sb], %[sc]\n\ts_lshl_b32 %[sb], %[sb], 3\n\ts_add_u32 %[sb], %[sb], %[sa]\n\ts_lshl_b32 %[sb], %[sb], 7\n\t"
      "s_waitcnt lgkmcnt(0)\n\t"
      "global_load_dword v105, v105, s[16:17]\n\tglobal_load_dwordx4 v[106:109], v106, s[16:17]\n\tglobal_load_dword v104, v104, s[42:43] sc1\n\t"
      "s_add_u32 s44, s22, %[sb]\n\ts_addc_u32 s45, s23, 0\n\t" DMA4("s[44:45]")
      "s_add_u32 s44, s44, 0x40000\n\ts_addc_u32 s45, s45, 0\n\ts_add_u32 m0, m0, 0x1000\n\ts_nop 0\n\t" DMA4("s[44:45]")
      "s_branch 9f\n\t"
      "2:\n\tv_mul_u32_u24 %[k0], 0x1800, %[va]\n\tv_add_u32 %[k0], %[k0], %[vb]\n\tv_add_u32 %[k1], 0x17c00, %[k0]\n\tv_add_u32 %[k2], 0x5800, %[k0]\n\tv_add_u32 %[k3], 0x1d400, %[k0]\n\t"
      "s_lshr_b32 %[sb], %[sa], 3\n\ts_and_b32 s44, %[w], 5\n\ts_lshl_b32 %[sb], %[sb], 3\n\ts_or_b32 %[sb], %[sb], s44\n\ts_cmp_eq_u32 %[sb], 17\n\ts_cbranch_scc0 5f\n\t"
      "s_lshl_b32 %[sb], %[w], 12\n\ts_and_b32 %[sb], %[sb], 0x2000\n\ts_and_b32 s44, %[bid], 63\n\ts_lshl_b32 s44, s44, 7\n\ts_add_u32 %[sb], %[sb], s44\n\ts_add_u32 %[sb], %[sb], %[pr]\n\t"
      "v_mov_b32 v107, %[sb]\n\ts_mov_b64 exec, 1\n\ts_waitcnt lgkmcnt(0)\n\ts_memtime s[44:45]\n\ts_waitcnt lgkmcnt(0)\n\t"
      "global_store_dword v107, v104, s[42:43] sc1\n\ts_barrier\n\ts_waitcnt vmcnt(0)\n\ts_memtime s[50:51]\n\ts_waitcnt lgkmcnt(0)\n\ts_sub_u32 %[rt], s50, s44\n\t"
      "global_store_dword v107, v104, s[42:43] sc1\n\ts_waitcnt vmcnt(0)\n\ts_memtime s[44:45]\n\ts_waitcnt lgkmcnt(0)\n\ts_sub_u32 s44, s44, s50\n\ts_min_u32 %[rt], %[rt], s44\n\t"
      "s_mov_b64 exec, -1\n\t"
      "5:\n\ts_sub_u32 %[sb], %[sa], 23\n\ts_cmp_lt_u32 %[sb], 4\n\ts_cbranch_scc0 3f\n\t"
      "v_bfe_u32 %[va], %[tid], 2, 1\n\tv_lshl_or_b32 v105, %[va], 6, v105\n\ts_lshl_b32 s44, %[w], 7\n\tv_add_u32 v105, s44, v105\n\t"
      "s_lshl_b32 s44, %[w], 13\n\ts_add_u32 m0, s44, 0x10000\n\ts_lshl_b32 %[sc], %[sc], 2\n\ts_add_u32 %[sb], %[sb], %[sc]\n\ts_add_u32 %[sb], %[sb], 16\n\ts_lshl_b32 %[sb], %[sb], 7\n\t"
      "s_mul_i32 %[sc], %[w], 0x60000\n\ts_add_u32 %[sb], %[sb], %[sc]\n\t"
      "s_waitcnt lgkmcnt(0)\n\t"
      "global_load_dword v105, v105, s[36:37]\n\tglobal_load_dword v104, v104, s[42:43] sc1\n\t"
      "s_add_u32 s44, s38, %[sb]\n\ts_addc_u32 s45, s39, 0\n\t" DMA4("s[44:45]")
      "s_add_u32 s44, s44, 0x30000\n\ts_addc_u32 s45, s45, 0\n\ts_add_u32 m0, m0, 0x1000\n\ts_nop 0\n\t" DMA4("s[44:45]")
      "s_branch 9f\n\t3:\n\t"
      "s_and_b32 %[sb], %[sa], 3\n\ts_lshl_b32 %[sb], %[sb], 2\n\ts_and_b32 s44, %[w], 3\n\ts_add_u32 %[sb], %[sb], s44\n\t"
      "s_lshl_b32 s44, s44, 12\n\ts_add_u32 m0, s44, %[tb]\n\ts_lshl_b32 s44, %[sb], 6\n\tv_add_u32 v105, s44, v105\n\t"
      "s_mul_i32 %[sb], %[sb], 0x30000\n\ts_lshl_b32 %[sc], %[sc], 8\n\ts_add_u32 %[sb], %[sb], %[sc]\n\ts_bfe_u32 %[sc], %[sa], 0x10002\n\ts_lshl_b32 %[sc], %[sc], 7\n\ts_add_u32 %[sb], %[sb], %[sc]\n\t"
      "s_lshr_b32 %[sc], %[w], 2\n\ts_lshr_b32 %[sa], %[sa], 3\n\ts_cmp_eq_u32 %[sa], 1\n\ts_cselect_b32 %[sa], %[sc], 0\n\ts_cmp_eq_u32 %[sa], 1\n\t"
      "s_waitcnt lgkmcnt(0)\n\t"
      "global_load_dword v105, v105, s[36:37]\n\tglobal_load_dword v104, v104, s[42:43] sc1\n\ts_cbranch_scc0 9f\n\t"
      "s_add_u32 s44, s38, %[sb]\n\ts_addc_u32 s45, s39, 0\n\t" DMA4("s[44:45]")
      "9:\n\ts_mov_b32 m0, %[keep]"
      : "=&{s[44:45]}"(pa), [w] "=&s"(wave), [k0] "=&v"(ko0), [k1] "=&v"(ko1), [k2] "=&v"(ko2), [k3] "=&v"(ko3),
        "=&{v104}"(ep), "=&{v105}"(d1), "=&{v[106:109]}"(xs), [sa] "=&s"(sa), [sb] "=&s"(sb), [sc] "=&s"(sc), [keep] "=&s"(keep), [va] "=&v"(va), [vb] "=&v"(vb), [rt] "=&s"(rtt)
      : "{s[16:31]}"(ka), "{s[36:43]}"(kb8), "{s46}"(tab), [bid] "s"(blockIdx.x), [tid] "v"(threadIdx.x), [tb] "i"(L_TB), [pr] "i"(PROBE_OFF)
      : "memory", "scc", "s50", "s51");
    asm volatile("" : "+{s[16:31]}"(ka), "+{s[36:43]}"(kb8), "+{s46}"(tab));
  }
  volatile LAS u32* FL = (volatile LAS u32*)(L + L_FLAG);
  LAS float* RED = (LAS float*)(L + L_RED);
  const int lane = threadIdx.x & 63;
  const int bid = blockIdx.x, xc = bid & 7, ix = bid >> 3, rb = ix & 7;
  const int g = lane >> 4, n = lane & 15;
  const bool chain = ix < 8, wcu = (ix >> 3) == 1;
  const int toff = tab & 0xffff, nhalf = tab >> 16, ntask = (nhalf + 1) >> 1;
  const bool ninth = wave == 2 && ntask == 9;
  const int aw = ninth ? 8 : wave - 8;
  const bool head = (wave >> 2) == 2 && !chain, p1 = wcu && (wave >> 2) == 1;
  const u32 ldst = (u32)(uintptr_t)L + L_K + aw * 16384;
#define KGROUP(kb, lo) do { u32 keep_; \
    asm volatile("s_mov_b32 %0, m0\n\ts_mov_b32 m0, %6\n\ts_nop 0\n\t" \
                 "global_load_lds_dwordx4 %1, %5" KV_SFX "\n\t" \
                 "global_load_lds_dwordx4 %2, %5 offset:1024" KV_SFX "\n\t" \
                 "global_load_lds_dwordx4 %3, %5 offset:2048" KV_SFX "\n\t" \
                 "global_load_lds_dwordx4 %4, %5 offset:3072" KV_SFX "\n\t" \
                 "s_mov_b32 m0, %0" \
                 : "=&s"(keep_) : "v"(ko0), "v"(ko1), "v"(ko2), "v"(ko3), "s"(kb), "s"(ldst + (lo)) : "memory"); } while (0)
#define PTR(v, i) (((u64)(v)[2 * (i) + 1] << 32) | (v)[2 * (i)])
  Ptrs p;
  p.x = (const u16*)PTR(ka, 0); p.K = (const u16*)PTR(ka, 1); p.V = (const u16*)PTR(ka, 2); p.wq = (const u16*)PTR(ka, 3); p.wo = (const u16*)PTR(ka, 4);
  p.wg = (const u16*)PTR(ka, 5); p.wu = (const u16*)PTR(ka, 6); p.wd = (const u16*)PTR(ka, 7);
  p.c = (const u16*)PTR(kb8, 0); p.wmod = (const u16*)PTR(kb8, 1); p.out = (u16*)PTR(kb8, 2); p.ws = (void*)PTR(kb8, 3);
#define MKWS() const rsrc_t ws = mkr(p.ws, WS_BYTES)
  if (wave == 0 && lane < 16) FL[lane] = (lane == 0 && ix >= 16) ? 1u : 0u;
  if (!((ix >> 3) == 2 && (wave == 1 || wave == 3))) BARRIER();

  MKWS();
  const bool gcu = ix >= 16 && ix < 23, mcu = ix >= 23 && ix < 27;
  u32 tag = 0;
  if (__builtin_expect(wave < 8 && !ninth, 1)) {
  if (wave >= 4 && !(chain || wcu || gcu || mcu)) return;
  __builtin_amdgcn_s_setprio(3);
  asm volatile("s_waitcnt vmcnt(8)\n\ts_cmp_eq_u32 %3, 2\n\ts_cbranch_scc1 1f\n\ts_waitcnt vmcnt(4)\n\ts_cmp_eq_u32 %3, 1\n\ts_cbranch_scc1 1f\n\ts_waitcnt vmcnt(0)\n\t1:"
               : "+{v104}"(ep), "+{v105}"(d1), "+{v[106:109]}"(xs) : "s"(__builtin_amdgcn_readfirstlane((chain || mcu) ? 2 : p1 ? 1 : 0)) : "memory", "scc");
  const int cg = xc * 2 + (rb >> 2), ks = rb & 3;
  const int pi = ((n >> 2) & 1) * 16 + g * 4 + (n & 3);
  tag = ep + 1u;
  if (__builtin_expect(p1, 1)) {
    v4u a = bcast4<0>(silu2(d1)), f[4];
    int ho = H0_OFF + (cg * 4 + ks) * 512 + lane * 8;
    PIN1(a); PIN1(ho);
    VM0();
    lds_frags(L + L_TB + (wave & 3) * 4096, lane, f);
    RED[(wave & 3) * 64 + lane] = gemv<1>(&a, f, g);
    if (lds_ticket(&FL[3], lane) == 3u) {
      const float s = (RED[lane] + RED[64 + lane]) + (RED[128 + lane] + RED[192 + lane]);
      const float s_hi = dpp<SHL1>(s);
      if (!(lane & 1)) stc(ws, ho, v4u{tag, asu(s), ~tag, asu(s_hi)});
      MEMFENCE();
      if (lane == 0) FL[0] = 1u;
    }
  }
  if (__builtin_expect(chain, 1)) {
    float mb, inv;
    ln_stats(xs, mb, inv);
    f32x2 y = up2(ln_pair(d1, mb, inv));
    PIN1(y);
    VM0();
    v4u f[8];
    lds_frags(L + L_K + wave * 8192, lane, f); lds_frags(L + L_K + wave * 8192 + 4096, lane, f + 4);
    SCHED();
    v4u h0, h1, h2, h3;
    pollT4(ws, H0_OFF + (wave + (n >> 3) * 8) * 2048 + pi * 16, tag, ~tag, h0, h1, h2, h3);
    const u32 md = pk((asf(h0.y) + asf(h1.y)) + (asf(h2.y) + asf(h3.y)), (asf(h0.w) + asf(h1.w)) + (asf(h2.w) + asf(h3.w)));
    const u32 ot = dppu<ROR8>(md), sa = (n & 8) ? ot : md, ca = (n & 8) ? md : ot;
    const u32 hp = pk2(up2(pk2(y * one_plus(ca))) + up2(sa));
    const v4u a[2] = {bcast4<0>(hp), bcast4<4>(hp)};
    RED[lane * 8 + wave] = gemv<2>(a, f, g);
    if (lds_ticket(&FL[7], lane) == 7u) {
      const float s = sum8((u32)(uintptr_t)(RED + lane * 8));
      const u32 d = pk(s, dpp<SHL1>(s)), d2 = dppu<SHL2>(d);
      if ((lane & 3) == 0) stc(ws, Q_OFF + (xc * 8 + rb) * 256 + lane * 4, v4u{tag, d, ~tag, d2});
      MEMFENCE();
      if (lane == 0) FL[0] = 1u;
    }
  }
  if ((ix >> 3) == 2 && (wave == 1 || wave == 3)) {
    if (wave == 3) { if (lane == 0) FL[12] = rtt | 1u; }
    else {
      lds_wait_ge(&FL[12], 1u);
      const u32 half = (__builtin_amdgcn_s_getreg(20 | (3 << 11)) >> 2) & 1u;
      const u32 vote = (rtt > FL[12] ? 1u : 0u) ^ half;
      if (lane == 0) stc2(ws, VOTE_OFF + (xc * 8 + rb) * 16, v2u{tag, ~tag ^ (vote | (half << 1))});
    }
  }
  if (mcu) {
    const u32 sp = silu2(d1);
    const v4u a[2] = {bcast4<0>(sp), bcast4<4>(sp)};
    VM0();
    v4u f[8];
    lds_frags(L + 65536 + wave * 8192, lane, f); lds_frags(L + 65536 + wave * 8192 + 4096, lane, f + 4);
    RED[lane * 8 + wave] = gemv<2>(a, f, g);
    MEMFENCE();
    lds_inc(&FL[3], lane);
    if (wave == 1) {
      lds_wait_ge(&FL[3], 8u);
      const float s = sum8((u32)(uintptr_t)(RED + lane * 8));
      const u32 d = nosent(pk(s, dpp<SHL1>(s)));
      lds_wait_ge(&FL[5], 1u);
      if (!(lane & 1)) stc1(ws, MR_OFF + (xc * 4 + (ix - 23)) * 128 + lane * 2, d);
    }
  }
  if (wcu && wave == 1) {
    lds_wait_ge(&FL[0], 1u);
    const v4u sv = {SENT, SENT, SENT, SENT};
#pragma unroll
    for (int j = 0; j < CLR_KB; ++j) stc(ws, S_OFF + (xc * 8 + rb) * (CLR_KB * 1024) + j * 1024 + lane * 16, sv);
    VM0();
    if (lane == 0) stc2(ws, CF_OFF + (xc * 8 + rb) * 16, v2u{tag, ~tag});
  }
  if (wave == 0) {
    const int qb = Q_OFF + xc * 2048 + lane * 16;
    if (!chain && !mcu) __builtin_amdgcn_s_sleep(100);
    _Pragma("nounroll") for (u32 s = 0; s < SPIN_CAP; ++s) {
      const v4u q0 = ldc(ws, qb), q1 = ldc(ws, qb + 1024);
      SCHED();
      if (__all((((q0.x ^ tag) | (q1.x ^ tag)) | ((q0.z ^ ~tag) | (q1.z ^ ~tag))) == 0u)) {
        LAS unsigned char* qd = L + L_Q + (lane >> 5) * 320 + (lane & 31) * 8;
        *(LAS v2u*)qd = v2u{q0.y, q0.w};
        *(LAS v2u*)(qd + 640) = v2u{q1.y, q1.w};
        LGKM0();
        if (lane == 0) FL[1] = 1u;
        break;
      }
      MEMFENCE();
    }
  }
  if (wave == 3) {
    {
      u32 pv = 0;
      __builtin_amdgcn_s_sleep(100);
      _Pragma("nounroll") for (u32 s = 0; s < SPIN_CAP; ++s) {
        const v2u cf = __builtin_amdgcn_raw_buffer_load_b64(ws, CF_OFF + lane * 16, 0, SC1), vt = __builtin_amdgcn_raw_buffer_load_b64(ws, VOTE_OFF + lane * 16, 0, SC1);
        SCHED();
        pv = vt.y ^ ~tag;
        if (__all(((cf.x ^ tag) | (cf.y ^ ~tag) | (vt.x ^ tag) | (pv & ~3u)) == 0u)) break;
        __builtin_amdgcn_s_sleep(16);
        MEMFENCE();
      }
      const u32 yes = __builtin_popcountll(__ballot(pv & 1u));
      const u32 hv = (u32)__ballot(pv & 2u);
      const u32 par = ((hv & 1u) | ((hv >> 7) & 2u) | ((hv >> 14) & 4u) | ((hv >> 21) & 8u)) ^ (yes > 32u ? 15u : 0u);
      if (lane == 0) FL[5] = 1u | (par << 1);
    }
  }
  }
  else {
    const bool early = aw < 4;
    if (ninth) {
      VM0();
      const u32 kv0 = g * 256 + ((n ^ g) << 4);
      ko0 = kv0; ko1 = kv0 ^ 64u; ko2 = kv0 ^ 128u; ko3 = kv0 ^ 192u;
    }
    if (aw >= ntask) return;
    if (chain) lds_wait_ge(&FL[0], 1u);
    if (!early) {
      lds_wait_ge(&FL[3], (ix >= 23 && ix < 27) ? 8u : (wcu && ninth) ? 4u : 0u);
      _Pragma("nounroll") for (u32 s = 0; s < (SPIN_CAP << 3); ++s) { if (FL[2] >= 4u || FL[1] != 0u) break; __builtin_amdgcn_s_sleep(1); }
      MEMFENCE();
    }
    const int hh = n >> 2, e2 = n & 3;
    u32 qseen = (!early && ix >= 16) ? 1u : 0u;
    const u32 key0 = (u32)(xc * 16384 + toff * 32 + aw * 64);
    const bool halft = 2 * aw + 1 == nhalf;
#define THROTTLE() do { if (!qseen) { const v2u fl_ = *(volatile LAS v2u*)FL; qseen = __builtin_amdgcn_readfirstlane(fl_.y); \
      if (!qseen) { if (__builtin_amdgcn_readfirstlane(fl_.x)) asm volatile("s_waitcnt vmcnt(%0)" :: "i"(THR_W - 4) : "memory"); else asm volatile("s_waitcnt vmcnt(%0)" :: "i"(THR_Q - 4) : "memory"); } } SCHED(); } while (0)
    const u64 kb = (u64)p.K + (u64)key0 * 256;
    if (!head) KGROUP(kb, 0);
    THROTTLE(); KGROUP(kb + 4096, 4096);
    THROTTLE();
    if (!halft) { KGROUP(kb + 8192, 8192); THROTTLE(); KGROUP(kb + 12288, 12288); THROTTLE(); }
    v4u vv[16];
    {
      const rsrc_t rv = mkr(p.V, 8 * 16384 * 256);
      const int vo = (halft && g >= 2) ? 0x40000000 : g * 4096 + n * 16;
#pragma unroll
      for (int i = 0; i < 16; ++i) { vv[i] = ld4<NT>(rv, vo + i * 256, key0 * 256); if ((i & 3) == 3 && i < 15) THROTTLE(); }
    }
    SCHED();
    lds_inc(&FL[2], lane);
    lds_wait_ge(&FL[1], 1u);
    asm volatile("s_waitcnt vmcnt(16)" ::: "memory");
    SCHED();
    float s2[4], pe[4];
    {
      const LAS unsigned char* kbase = L + L_K + aw * 16384 + n * 256;
      const LAS unsigned char* qa = L + L_Q + (n & 3) * 320 + g * 16;
      f32x4 sa[4];
#pragma unroll
      for (int t = 0; t < 4; ++t) sa[t] = f32x4{0.f, 0.f, 0.f, 0.f};
#pragma unroll
      for (int s = 0; s < 4; ++s) {
        const v4u a = *(const LAS v4u*)(qa + 64 * s);
#pragma unroll
        for (int t = 0; t < 4; ++t) sa[t] = mfma32(a, *(const LAS v4u*)(kbase + t * 4096 + (((4 * s + g) ^ n) << 4)), sa[t]);
      }
      const f32x4 lo = (g & 1) ? sa[1] : sa[0], hi = (g & 1) ? sa[3] : sa[2], sacc = (g & 2) ? hi : lo;
      constexpr float SC = 0.08838834764831845f;
      const f32x2 c = up2(pk2(up2(pk(sacc[0], sacc[1])) * SC)), d = up2(pk2(up2(pk(sacc[2], sacc[3])) * SC));
      const bool dead = halft && g >= 2;
      s2[0] = dead ? -__builtin_inff() : c.x; s2[1] = dead ? -__builtin_inff() : c.y; s2[2] = dead ? -__builtin_inff() : d.x; s2[3] = dead ? -__builtin_inff() : d.y;
    }
    const float mrow = row_max(tm16(tm32(s2[0], s2[2]), tm32(s2[1], s2[3])));
#pragma unroll
    for (int h2 = 0; h2 < 4; ++h2) pe[h2] = __builtin_amdgcn_exp2f((s2[h2] - rdlf(mrow, 16 * h2)) * LOG2E);
    const u32 w0 = pk(pe[0], pe[1]), w1 = pk(pe[2], pe[3]);
    const float lrow = row_sum(tr16(tr32(pe[0], pe[2]), tr32(pe[1], pe[3])));
    if (n == 0) *(LAS f32x2*)(L + L_ML + g * 128 + aw * 8) = f32x2{mrow, lrow};
    MEMFENCE();
    lds_inc(&FL[4], lane);
    const u32 two = (u32)(2 * hh) | ((u32)(2 * hh + 1) << 8);
    const u32 selv = (e2 & 1) ? ((two << 16) | 0x0C0Cu) : (0x0C0C0000u | two);
    const u32 sel0 = (e2 >> 1) == 0 ? selv : 0x0C0C0C0Cu, sel1 = (e2 >> 1) == 1 ? selv : 0x0C0C0C0Cu;
    v2u xb[16];
#define PV_PREP(i) { const u32 b0_ = dppu<BC + (i)>(w0), b1_ = dppu<BC + (i)>(w1); xb[i] = v2u{__builtin_amdgcn_perm(b1_, b0_, sel0), __builtin_amdgcn_perm(b1_, b0_, sel1)}; }
    PV_PREP(0) PV_PREP(1) PV_PREP(2) PV_PREP(3) PV_PREP(4) PV_PREP(5) PV_PREP(6) PV_PREP(7)
    PV_PREP(8) PV_PREP(9) PV_PREP(10) PV_PREP(11) PV_PREP(12) PV_PREP(13) PV_PREP(14) PV_PREP(15)
#undef PV_PREP
    u32 obw = (u32)(uintptr_t)L + L_K + aw * 16384 + (hh * 128 + e2 * 16 + 4 * g) * 4;
    PIN1(obw);
    SCHED();
    f32x4 o1 = {0.f, 0.f, 0.f, 0.f}, o2 = {0.f, 0.f, 0.f, 0.f};
#pragma unroll
    for (int i = 0; i < 16; ++i) {
      o1 = __builtin_amdgcn_mfma_f32_16x16x16bf16_1k(__builtin_bit_cast(s16x4, v2u{vv[i].x, vv[i].y}), __builtin_bit_cast(s16x4, xb[i]), o1, 0, 0, 0);
      o2 = __builtin_amdgcn_mfma_f32_16x16x16bf16_1k(__builtin_bit_cast(s16x4, v2u{vv[i].z, vv[i].w}), __builtin_bit_cast(s16x4, xb[i]), o2, 0, 0, 0);
    }
    *(LAS f32x4*)(uintptr_t)obw = o1; *(LAS f32x4*)(uintptr_t)(obw + 256) = o2;
    if (aw < 2 || (aw > 3 && aw < 8) || (ninth && !gcu)) { BARRIER(); return; }
    if (!ninth) {
    const int tau = (aw - 2) * 64 + lane, h = tau >> 5;
    float f[9];
    lds_wait_ge(&FL[5], 1u);
    const int par = __builtin_amdgcn_readfirstlane(FL[5]) >> 1;
    lds_wait_ge(&FL[4], (u32)ntask);
    {
      float m[10], l[10];
#pragma unroll
      for (int w = 0; w < 5; ++w) { const f32x4 t = *(const LAS f32x4*)(L + L_ML + h * 128 + w * 16); m[2 * w] = t.x; l[2 * w] = t.y; m[2 * w + 1] = t.z; l[2 * w + 1] = t.w; }
#pragma unroll
      for (int w = 4; w < 9; ++w) { m[w] = w < ntask ? m[w] : -__builtin_inff(); l[w] = w < ntask ? l[w] : 0.f; }
      const float M = mx(mx(mx(mx(m[0], m[1]), mx(m[2], m[3])), mx(mx(m[4], m[5]), mx(m[6], m[7]))), m[8]);
      float Ls = 0.f;
#pragma unroll
      for (int w = 0; w < 9; ++w) { f[w] = __builtin_amdgcn_exp2f((m[w] - M) * LOG2E); Ls = fmaf(l[w], f[w], Ls); PIN1(f[w]); }
      if ((lane & 31) == 0) stc2(ws, ml_at(xc, par) + (ix * 2 + aw - 2) * 128 + (lane >> 5) * 8, v2u{asu(M), asu(Ls)});
    }
    int t1o = t1_at(xc, par, ((h * 4 + ((tau >> 3) & 3)) * 32 + ix) * 128 + (tau & 7) * 16);
    u32 pa[9];
#pragma unroll
    for (int w = 0; w < 9; ++w) { pa[w] = (u32)(uintptr_t)(L + L_K) + tau * 16 + (w < ntask ? w : 0) * 16384; PIN1(pa[w]); }
    PIN1(t1o);
    __builtin_amdgcn_s_setprio(3);
    BARRIER();
    f32x4 part[9];
#pragma unroll
    for (int w = 0; w < 9; ++w) part[w] = *(const LAS f32x4*)(uintptr_t)pa[w];
    const f32x4 acc = (((part[0] * f[0] + part[1] * f[1]) + (part[2] * f[2] + part[3] * f[3])) + ((part[4] * f[4] + part[5] * f[5]) + (part[6] * f[6] + part[7] * f[7]))) + part[8] * f[8];
    stc(ws, t1o, __builtin_bit_cast(v4u, acc));
    lds_inc(&FL[8], lane);
    return;
    }
    __builtin_amdgcn_s_setprio(3);
  }

  LAS unsigned char* tb = L + L_K + wave * 16384 + 8192;
  if (ix < 16) {
    if (wave < 4) return;
    const int x = wave & 3, hl = ix >> 2, hg = xc * 4 + hl, qd = ix & 3;
    const rsrc_t ro = mkr(p.wo, 4096 * 512 * 2);
    const int col = xc * 64 + rb * 8;
    lds_wait_ge(&FL[2], (u32)ntask);
    v4u w[8];
    {
      const int rho = lane >> 3, vo = (64 * (rho >> 2) + 8 * (rho & 3)) * 1024 + (lane & 7) * 16;
#pragma unroll
      for (int i = 0; i < 8; ++i) w[i] = ld4<NT>(ro, vo, (hg * 128 + 32 * ((i >> 1) & 1) + 2 * qd + (i & 1)) * 1024 + x * 256 + (i >> 2) * 128);
    }
    BARRIER();
    lds_wait_ge(&FL[8], 2u);
    const int par = __builtin_amdgcn_readfirstlane(FL[5]) >> 1;
    v4u f[8];
    tile_frags(tb, lane, w, f); tile_frags(tb, lane, w + 4, f + 4);
    const v2u ma = poll2(ws, ml_at(xc, par) + ((lane >> 1) * 2 + (hl >> 1)) * 128 + (hl & 1) * 8);
    int ob = t1_at(xc, par, ((hl * 4 + qd) * 32 + (lane >> 1)) * 128 + x * 32 + (lane & 1) * 16);
    int t2o = T2_OFF + row_at(xc * 16 + ix, par, x * 512 + lane * 4), t2p = T2_OFF + row_at(xc * 16 + ix, par, x * 512 + 256 + lane * 4);
    float M = asf(ma.x);
    M = mx(M, dpp<XOR2>(M)); M = mx(M, dpp<ROR4>(M)); M = mx(M, dpp<ROR8>(M)); M = tm16(M, M); M = tm32(M, M);
    float fa = __builtin_amdgcn_exp2f((asf(ma.x) - M) * LOG2E);
    float Ls = fa * asf(ma.y);
    Ls = tr32(Ls, Ls); Ls = tr16(Ls, Ls); Ls += dpp<ROR8>(Ls); Ls += dpp<ROR4>(Ls); Ls += dpp<XOR2>(Ls);
    float inv = __builtin_amdgcn_rcpf(Ls);
    u32 oa = (u32)(uintptr_t)(L + L_X) + x * 16 + (lane & 1) * 8 + (lane >> 5) * 4;
    u32 oa2 = (u32)(uintptr_t)(L + L_X) + g * 16;
    fa *= inv;
    PIN1(fa); PIN1(ob); PIN1(t2o); PIN1(t2p); PIN1(oa); PIN1(oa2);
    SCHED();
    const v4u a = poll4(ws, ob);
    float u0 = tr32(fa * asf(a.x), fa * asf(a.z)), u1 = tr32(fa * asf(a.y), fa * asf(a.w));
    u0 = tr16(u0, u0); u1 = tr16(u1, u1);
    u0 += dpp<ROR8>(u0); u1 += dpp<ROR8>(u1);
    u0 += dpp<ROR4>(u0); u1 += dpp<ROR4>(u1);
    u0 += dpp<XOR2>(u0); u1 += dpp<XOR2>(u1);
    if (!(lane & 30)) *(LAS u32*)(uintptr_t)oa = pk(u0, u1);
    BARRIER();
    const v4u av = *(const LAS v4u*)(uintptr_t)oa2;
    const float y0 = gemv<1>(&av, f, g), y1 = gemv<1>(&av, f + 4, g);
    stc_row(ws, t2o, y0, lane); stc_row(ws, t2p, y1, lane);
    if (!chain || wave != 4) return;
    f32x2 xin = up2(*(const GAS u32*)((const GAS u16*)p.x + col + 2 * g));
    u32 gaw, gbw;
    poll1x2<3072>(ws, MR_OFF + (col + 2 * g) * 2, gaw, gbw);
    f32x2 ga = up2(gaw), gb = up2(gbw);
    int tio = T2_OFF + row_at(lane, par, col * 4), tio2 = tio + 131072, yo = lane < 56 ? Y_OFF + row_at(lane, par, col * 4) : 0x40000000;
    PIN1(ga); PIN1(gb); PIN1(xin); PIN1(tio); PIN1(tio2); PIN1(yo);
    v4u pa, pb, pc, pd;
    poll4x4(ws, tio, tio2, pa, pb, pc, pd);
    pa = __builtin_bit_cast(v4u, __builtin_bit_cast(f32x4, pa) + __builtin_bit_cast(f32x4, pc));
    pb = __builtin_bit_cast(v4u, __builtin_bit_cast(f32x4, pb) + __builtin_bit_cast(f32x4, pd));
    const u32 x1 = nosent(pk2(xin + up2(pk2(ga * up2(pk2(sum8x64(pa, pb)))))));
    const v4u o = {rdl(x1, 0), rdl(x1, 16), rdl(x1, 32), rdl(x1, 48)};
    if (lane < 2) stc(ws, X1_OFF + lane * 8192 + col * 16, o);
    if (bid == 0 && lane == 0) stc1(ws, EPOCH_OFF, tag);
    f32x2 x1f = up2(x1);
    PIN1(x1f);
    poll4x2<16>(ws, yo, pa, pb);
    const u32 d = pk2(x1f + up2(pk2(gb * up2(pk2(sum8x64(pa, pb))))));
    if (!n) *(GAS u32*)((GAS u16*)p.out + col + 2 * g) = d;
  } else if (gcu) {
    const int gi = xc * 7 + (ix - 16);
    const rsrc_t rd = mkr(p.wd, 1792 * 512 * 2);
    BARRIER();
    lds_wait_ge(&FL[8], 2u);
    const int par = __builtin_amdgcn_readfirstlane(FL[5]) >> 1;
    v4u w[12];
    {
      const int rho = lane >> 3;
      const GAS unsigned char* src = (const GAS unsigned char*)((lane & 4) ? p.wu : p.wg) + (size_t)(wave * 64 + 8 * (rho >> 2) + (rho & 3)) * 3584 + gi * 64 + (lane & 3) * 16;
#pragma unroll
      for (int i = 0; i < 8; ++i) w[i] = *(const GAS v4u*)(src + (32 * (i >> 2) + 16 * (i & 1) + 4 * ((i >> 1) & 1)) * 3584);
    }
    tile_load<NT>(rd, tile_voff(lane, 1024), gi * 32 * 1024 + wave * 128, 1024, w + 8);
    v4u f[12];
    tile_frags(tb, lane, w, f); tile_frags(tb, lane, w + 4, f + 4); tile_frags(tb, lane, w + 8, f + 8);
    int yo = Y_OFF + row_at(gi, par, wave * 256 + lane * 4);
    u32 aa = (u32)(uintptr_t)(L + L_X) + wave * 128 + g * 16, ra = (u32)(uintptr_t)(RED + lane * 8 + wave), sa = (u32)(uintptr_t)(RED + ((n >> 3) * 32 + g * 8 + (n & 7)) * 8);
    u32 ha = (u32)(uintptr_t)(L + L_X) + lane * 16 + wave * 4;
    PIN1(yo); PIN1(aa); PIN1(ra); PIN1(sa); PIN1(ha);
    SCHED();
    if (wave < 4) {
#define SEL4(v) ((wave & 2) ? ((wave & 1) ? (v).w : (v).z) : ((wave & 1) ? (v).y : (v).x))
      v4u sbw, cbw;
      poll4x2<1024>(ws, MR_OFF + 1024 + lane * 16, sbw, cbw);
      f32x2 sb = up2(SEL4(sbw)), c1 = one_plus(SEL4(cbw));
      int xb1 = X1_OFF + par_of(par, xc) + lane * 128;
      PIN1(sb); PIN1(c1); PIN1(xb1);
      const v4u x1v = poll4(ws, xb1);
      float mb, inv;
      ln_stats(x1v, mb, inv);
      *(LAS u32*)(uintptr_t)ha = modulate(ln_pair(SEL4(x1v), mb, inv), c1, sb);
    }
    BARRIER();
    const v4u av[2] = {*(const LAS v4u*)(uintptr_t)aa, *(const LAS v4u*)(uintptr_t)(aa + 64)};
    *(LAS float*)(uintptr_t)ra = gemv<2>(av, f, g);
    BARRIER();
    const float s = rbf(sum8(sa));
    const float gt = s, up = dpp<ROR8>(s);
    const float u2 = (-2.f * 0.7978845608028654f * LOG2E) * (gt + 0.044715f * gt * gt * gt);
    const float gv = rbf(gt * __builtin_amdgcn_rcpf(1.f + __builtin_amdgcn_exp2f(u2))) * up;
    const u32 d = pk(gv, dpp<SHL1>(gv));
    const v4u ag = {dppu<BC + 0>(d), dppu<BC + 2>(d), dppu<BC + 4>(d), dppu<BC + 6>(d)};
    stc_row(ws, yo, gemv<1>(&ag, f + 8, g), lane);
  }
}
}

extern "C" void kernel_launch(void* const* d_in, const int* in_sizes, int n_in, void* d_out, int out_size, void* d_ws, size_t ws_size, hipStream_t stream) {
  Params a;
  Ptrs& p = a.p;
  p.x = (const u16*)d_in[0]; p.K = (const u16*)d_in[1]; p.V = (const u16*)d_in[2]; p.wq = (const u16*)d_in[3]; p.wo = (const u16*)d_in[4];
  p.wg = (const u16*)d_in[6]; p.wu = (const u16*)d_in[7]; p.wd = (const u16*)d_in[8]; p.c = (const u16*)d_in[9]; p.wmod = (const u16*)d_in[10];
  p.out = (u16*)d_out; p.ws = d_ws;
  for (int i = 0; i < 32; ++i) a.tab[i] = (u32)toff_of(i) | ((u32)nhalf_of(i) << 16);
  hipLaunchKernelGGL(fused_kernel, dim3(NWG), dim3(NTHR), 0, stream, a);
}
```
